```python
import torch

def _softmax(x, axis=-1):
    return torch.softmax(x, dim=axis)
def _gelu(x):
    return torch.nn.functional.gelu(x, approximate="tanh")
def _silu(x):
    return torch.nn.functional.silu(x)
def _sigmoid(x):
    return torch.sigmoid(x)
def _layernorm(x, axis=-1, eps=1e-5):
    m = x.mean(dim=axis, keepdim=True)
    v = x.var(dim=axis, keepdim=True, unbiased=False)
    return (x - m) / torch.sqrt(v + eps)
def _scatter_add(tgt, idx, src, axis=0):
    return tgt.clone().index_add_(axis, idx, src)
def _segment_sum(src, seg, n):
    out = torch.zeros((n,) + src.shape[1:], dtype=src.dtype)
    return out.index_add_(0, seg, src)
def _pad(x, pw):
    flat = tuple(v for pair in reversed(pw) for v in pair)
    return torch.nn.functional.pad(x, flat)

kernel_name = 'sequential_preLN__sliding_block__geglu_ffn__prompt'

def setup_inputs():

    _w = (lambda a: (lambda t: t.to(torch.bfloat16) if t.is_floating_point() else t)(torch.as_tensor(a)))
    def _A_setup():
        B, S, D = 1, 2048, 512
        return {
            "x": _w((lambda *s: torch.randn(*s, dtype=torch.bfloat16) * 0.05)(B, S, D)),
            "wqkv": _w((lambda *s: torch.randn(*s, dtype=torch.bfloat16) * 0.05)(D, 3 * D)),
            "wo": _w((lambda *s: torch.randn(*s, dtype=torch.bfloat16) * 0.05)(D, D)),
        }
    _A_in = _A_setup()

    def _B_setup():
        B, S, D, F = 1, 2048, 512, 1376
        return {
            "x": _w((lambda *s: torch.randn(*s, dtype=torch.bfloat16) * 0.05)(B, S, D)),
            "w_gate": _w((lambda *s: torch.randn(*s, dtype=torch.bfloat16) * 0.05)(D, F)),
            "w_up": _w((lambda *s: torch.randn(*s, dtype=torch.bfloat16) * 0.05)(D, F)),
            "w_down": _w((lambda *s: torch.randn(*s, dtype=torch.bfloat16) * 0.05)(F, D)),
        }
    _B_in = _B_setup()

    return {
        "A_x": _A_in["x"],
        "A_wqkv": _A_in["wqkv"],
        "A_wo": _A_in["wo"],
        "B_x": _B_in["x"],
        "B_w_gate": _B_in["w_gate"],
        "B_w_up": _B_in["w_up"],
        "B_w_down": _B_in["w_down"],
    }

def reference(A_x, A_wqkv, A_wo, B_x, B_w_gate, B_w_up, B_w_down):
    def _A_ref(x, wqkv, wo):
        B, S, D = x.shape
        H = 32; Dh = D // H; W = 256
        qkv = (lambda x, *d: x.permute(*d))((x @ wqkv).reshape(B, S, 3, H, Dh), 2, 0, 3, 1, 4)
        q, k, v = qkv[0], qkv[1], qkv[2]
        scores = torch.einsum("bhqd,bhkd->bhqk", q, k) * (Dh ** -0.5)
        idx = torch.arange(S)
        mask = (idx[:, None] - idx[None, :] >= 0) & (idx[:, None] - idx[None, :] < W)
        scores = torch.where(mask[None, None], scores, -1e9)
        p = _softmax(scores, axis=-1)
        o = torch.einsum("bhqk,bhkd->bhqd", p, v)
        return (lambda x, *d: x.permute(*d))(o, 0, 2, 1, 3).reshape(B, S, D) @ wo

    def _B_ref(x, w_gate, w_up, w_down):
        return (_gelu(x @ w_gate) * (x @ w_up)) @ w_down

    x = A_x
    h = x + _A_ref(_layernorm(x, axis=-1), A_wqkv, A_wo)
    h2 = h + _B_ref(_layernorm(h, axis=-1), B_w_gate, B_w_up, B_w_down)
    return h2
```

```cpp
#include <hip/hip_runtime.h>
#include <hip/hip_bf16.h>
typedef __hip_bfloat16 bf16;
constexpr int S=2048, D=512, H=32, DH=16, W=256, F=1376;
__device__ inline float b2f(bf16 v){return __bfloat162float(v);}
__global__ void ln_k(const bf16* x, bf16* y) {
    int row = blockIdx.x; int l = threadIdx.x;
    float v[8]; float s=0;
    for (int i=0;i<8;i++){ v[i]=b2f(x[row*D+l*8+i]); s+=v[i]; }
    for (int m=32;m>=1;m>>=1) s+=__shfl_xor(s,m);
    float mean=s/D; float q=0;
    for (int i=0;i<8;i++){ q+=(v[i]-mean)*(v[i]-mean);}
    for (int m=32;m>=1;m>>=1) q+=__shfl_xor(q,m);
    float r=rsqrtf(q/D+1e-5f);
    for (int i=0;i<8;i++) y[row*D+l*8+i]=__float2bfloat16((v[i]-mean)*r);
}
template<int MODE> __global__ void gemm_k(const bf16* A, const bf16* B, const bf16* B2, const bf16* R, bf16* C, int N, int K) {
    __shared__ float As[16][17], Bs[16][17], B2s[16][17];
    int tx=threadIdx.x&15, ty=threadIdx.x>>4;
    int row=blockIdx.y*16+ty, col=blockIdx.x*16+tx;
    float acc=0, acc2=0;
    for (int k0=0;k0<K;k0+=16){
        As[ty][tx]=b2f(A[row*K+k0+tx]);
        Bs[ty][tx]=b2f(B[(k0+ty)*N+col]);
        if (MODE==2) B2s[ty][tx]=b2f(B2[(k0+ty)*N+col]);
        __syncthreads();
        for (int k=0;k<16;k++){ acc+=As[ty][k]*Bs[k][tx]; if (MODE==2) acc2+=As[ty][k]*B2s[k][tx]; }
        __syncthreads();
    }
    if (MODE==1) acc += b2f(R[row*N+col]);
    if (MODE==2) { float g=acc; float t=tanhf(0.7978845608f*(g+0.044715f*g*g*g)); acc=0.5f*g*(1+t)*acc2; }
    C[row*N+col]=__float2bfloat16(acc);
}
__global__ void attn_k(const bf16* qkv, bf16* o) {
    int t = blockIdx.x*blockDim.x+threadIdx.x; int h=t%H, q=t/H;
    float qv[16]; for(int i=0;i<16;i++) qv[i]=b2f(qkv[q*3*D+h*16+i])*0.25f;
    float m=-1e30f, l=0, acc[16]; for(int i=0;i<16;i++) acc[i]=0;
    int k0 = q-W+1; if(k0<0)k0=0;
    for(int k=k0;k<=q;k++){
        float s=0; for(int i=0;i<16;i++) s+=qv[i]*b2f(qkv[k*3*D+D+h*16+i]);
        float mn=fmaxf(m,s); float a=__expf(m-mn), p=__expf(s-mn);
        l=l*a+p; for(int i=0;i<16;i++) acc[i]=acc[i]*a+p*b2f(qkv[k*3*D+2*D+h*16+i]);
        m=mn;
    }
    for(int i=0;i<16;i++) o[q*D+h*16+i]=__float2bfloat16(acc[i]/l);
}
extern "C" void kernel_launch(void* const* d_in, const int* in_sizes, int n_in,
                              void* d_out, int out_size, void* d_ws, size_t ws_size,
                              hipStream_t stream) {
    const bf16* x=(const bf16*)d_in[0]; const bf16* wqkv=(const bf16*)d_in[1]; const bf16* wo=(const bf16*)d_in[2];
    const bf16* wg=(const bf16*)d_in[4]; const bf16* wu=(const bf16*)d_in[5]; const bf16* wd=(const bf16*)d_in[6];
    bf16* ws=(bf16*)d_ws; bf16* xn=ws; bf16* qkv=xn+S*D; bf16* o=qkv+S*3*D; bf16* h=o+S*D; bf16* hn=h+S*D; bf16* g=hn+S*D;
    ln_k<<<S,64,0,stream>>>(x,xn);
    gemm_k<0><<<dim3(3*D/16,S/16),256,0,stream>>>(xn,wqkv,nullptr,nullptr,qkv,3*D,D);
    attn_k<<<S*H/256,256,0,stream>>>(qkv,o);
    gemm_k<1><<<dim3(D/16,S/16),256,0,stream>>>(o,wo,nullptr,x,h,D,D);
    ln_k<<<S,64,0,stream>>>(h,hn);
    gemm_k<2><<<dim3(F/16,S/16),256,0,stream>>>(hn,wg,wu,nullptr,g,F,D);
    gemm_k<1><<<dim3(D/16,S/16),256,0,stream>>>(g,wd,nullptr,h,(bf16*)d_out,D,F);
}
```

```cpp
#include <hip/hip_runtime.h>
#include <hip/hip_bf16.h>
#include <stdint.h>
#pragma clang diagnostic ignored "-Wint-to-pointer-cast"

typedef __bf16 bf8 __attribute__((ext_vector_type(8)));
typedef __bf16 bf2 __attribute__((ext_vector_type(2)));
typedef short s4 __attribute__((ext_vector_type(4)));
typedef short s8 __attribute__((ext_vector_type(8)));
typedef float f2 __attribute__((ext_vector_type(2)));
typedef float f4 __attribute__((ext_vector_type(4)));
typedef float f16 __attribute__((ext_vector_type(16)));
typedef unsigned u2 __attribute__((ext_vector_type(2)));
typedef unsigned u4 __attribute__((ext_vector_type(4)));
typedef unsigned long long u64;
#define LAS __attribute__((address_space(3)))
#define DEV __device__ __forceinline__

constexpr unsigned WS_F1 = 32768, WS_F2 = 65536, WS_F3 = 98304;
constexpr unsigned WS_KV = 1u << 20;
constexpr unsigned WS_O = 5u << 20;
constexpr unsigned WS_H = 7u << 20;
constexpr unsigned WS_ST = 9u << 20;
constexpr unsigned WS_G = 10u << 20;
constexpr unsigned WS_DIAG = 32u << 20;
constexpr unsigned LDS_BYTES = 163840;
constexpr unsigned L_HOLE = 73728;
constexpr unsigned L_OC = L_HOLE;
constexpr unsigned L_A = 98304;
constexpr unsigned L_CSP = 155648, L_CF = L_CSP + 3072;
constexpr int SPIN_CAP = 1 << 14;
constexpr unsigned OOB = 0x40000000u;

#define MFMA16(a, b, c) __builtin_amdgcn_mfma_f32_16x16x32_bf16(__builtin_bit_cast(bf8, a), __builtin_bit_cast(bf8, b), c, 0, 0, 0)
#define MFMA32(a, b, c) __builtin_amdgcn_mfma_f32_32x32x16_bf16(__builtin_bit_cast(bf8, a), __builtin_bit_cast(bf8, b), c, 0, 0, 0)
#define SBAR() __builtin_amdgcn_sched_barrier(0)
#define BAR_VM(N) asm volatile("s_waitcnt vmcnt(%0) lgkmcnt(0)\n\ts_barrier" ::"i"(N) : "memory")
#define BAR_L() asm volatile("s_waitcnt lgkmcnt(0)\n\ts_barrier" ::: "memory")

DEV u4 mk_rsrc(const void* p, unsigned bytes) { const u64 a = (u64)p; return (u4){(unsigned)a, (unsigned)(a >> 32) & 0xffffu, bytes, 0x00020000u}; }
DEV void dma_buf(unsigned lds, unsigned voff, u4 rs, unsigned soff) {
    asm volatile("s_mov_b32 m0, %0\n\ts_nop 0\n\tbuffer_load_dwordx4 %1, %2, %3 offen lds" ::"s"(lds), "v"(voff), "s"(rs), "s"(soff) : "memory"); }
template <unsigned LI, unsigned SI> DEV void dma_bufi(unsigned lds, unsigned voff, u4 rs, unsigned soff) {
    unsigned t; asm volatile("s_add_u32 m0, %1, %5\n\ts_add_u32 %0, %4, %6\n\tbuffer_load_dwordx4 %2, %3, %0 offen lds" : "=&s"(t) : "s"(lds), "v"(voff), "s"(rs), "s"(soff), "i"(LI), "i"(SI) : "memory", "scc"); }
template <unsigned LI> DEV void dma_glbi(unsigned lds, const char* p) {
    asm volatile("s_add_u32 m0, %0, %2\n\ts_nop 0\n\tglobal_load_lds_dwordx4 %1, off" ::"s"(lds), "v"(p), "i"(LI) : "memory", "scc"); }
DEV void dma_glb(unsigned lds, const char* p) {
    asm volatile("s_mov_b32 m0, %0\n\ts_nop 0\n\tglobal_load_lds_dwordx4 %1, off" ::"s"(lds), "v"(p) : "memory"); }
DEV void ld16(u4& d, unsigned voff, u4 rs, unsigned soff) {
    asm volatile("buffer_load_dwordx4 %0, %1, %2, %3 offen sc1" : "=v"(d) : "v"(voff), "s"(rs), "s"(soff) : "memory"); }
DEV void ld16_plain(u4& d, unsigned voff, u4 rs, unsigned soff) {
    asm volatile("buffer_load_dwordx4 %0, %1, %2, %3 offen" : "=v"(d) : "v"(voff), "s"(rs), "s"(soff) : "memory"); }
template <int OFF> DEV void ld16_imm(u4& d, unsigned voff, u4 rs) {
    asm volatile("buffer_load_dwordx4 %0, %1, %2, 0 offen offset:%3" : "=v"(d) : "v"(voff), "s"(rs), "i"(OFF) : "memory"); }
DEV void ld8(u2& d, unsigned voff, u4 rs, unsigned soff) {
    asm volatile("buffer_load_dwordx2 %0, %1, %2, %3 offen sc1" : "=v"(d) : "v"(voff), "s"(rs), "s"(soff) : "memory"); }
DEV void ld16_v(u4& d0, u4& d1, unsigned voff, u4 rs, unsigned soff) {
    asm volatile("s_mov_b64 exec, %5\n\tbuffer_load_dwordx4 %0, %2, %3, %4 offen offset:1024 sc1\n\tbuffer_load_dwordx4 %1, %2, %3, %4 offen offset:1536 sc1\n\ts_mov_b64 exec, -1"
                 : "+v"(d0), "+v"(d1) : "v"(voff), "s"(rs), "s"(soff), "s"(0x0000FFFF0000FFFFull) : "memory"); }
DEV void st16(u4 d, unsigned voff, u4 rs, unsigned soff) {
    asm volatile("buffer_store_dwordx4 %0, %1, %2, %3 offen sc1\n\ts_nop 1" ::"v"(d), "v"(voff), "s"(rs), "s"(soff) : "memory"); }
DEV void st16_plain(u4 d, unsigned voff, u4 rs, unsigned soff) {
    asm volatile("buffer_store_dwordx4 %0, %1, %2, %3 offen\n\ts_nop 1" ::"v"(d), "v"(voff), "s"(rs), "s"(soff) : "memory"); }
DEV void st8(u2 d, unsigned voff, u4 rs, unsigned soff) {
    asm volatile("buffer_store_dwordx2 %0, %1, %2, %3 offen sc1\n\ts_nop 1" ::"v"(d), "v"(voff), "s"(rs), "s"(soff) : "memory"); }
#define WAITV1(N, a) asm volatile("s_waitcnt vmcnt(%1)" : "+v"(a) : "i"(N) : "memory")
#define WAITV1D(ND, NS, a) asm volatile("s_waitcnt vmcnt(%1)\n\ts_cmp_lt_u32 %3, 4\n\ts_cbranch_scc1 .Lw%=\n\ts_waitcnt vmcnt(%2)\n.Lw%=:" : "+v"(a) : "i"(ND), "i"(NS), "s"(w) : "memory", "scc")
#define WAITV3(N, a, b, c) asm volatile("s_waitcnt vmcnt(%3)" : "+v"(a), "+v"(b), "+v"(c) : "i"(N) : "memory")
#define WAITV4(N, a, b, c, d) asm volatile("s_waitcnt vmcnt(%4)" : "+v"(a), "+v"(b), "+v"(c), "+v"(d) : "i"(N) : "memory")

DEV u4 lds16(unsigned a) { return *(LAS const u4*)a; }
DEV f4 lds16f(unsigned a) { return *(LAS const f4*)a; }
DEV float lds4f(unsigned a) { return *(LAS const float*)a; }
DEV void sts16(unsigned a, u4 v) { *(LAS u4*)a = v; }
DEV void sts16f(unsigned a, f4 v) { *(LAS f4*)a = v; }
DEV void sts8f(unsigned a, f2 v) { *(LAS f2*)a = v; }
DEV void sts4f(unsigned a, float v) { *(LAS float*)a = v; }
DEV u4 ldtr(unsigned a0, unsigned a1) {
    const s4 lo = __builtin_amdgcn_ds_read_tr16_b64_v4i16((LAS s4*)a0), hi = __builtin_amdgcn_ds_read_tr16_b64_v4i16((LAS s4*)a1);
    return __builtin_bit_cast(u4, __builtin_shufflevector(lo, hi, 0, 1, 2, 3, 4, 5, 6, 7)); }
DEV unsigned pk2(f2 a) { return __builtin_bit_cast(unsigned, __builtin_convertvector(a, bf2)); }
DEV f2 lo2(f4 a) { return (f2){a[0], a[1]}; }
DEV f2 hi2(f4 a) { return (f2){a[2], a[3]}; }
DEV unsigned pk(float a, float b) { return __builtin_bit_cast(unsigned, __builtin_convertvector((f2){a, b}, bf2)); }
DEV float blo(unsigned v) { return __builtin_bit_cast(float, v << 16); }
DEV float bhi(unsigned v) { return __builtin_bit_cast(float, v & 0xffff0000u); }
DEV float fu(unsigned v) { return __builtin_bit_cast(float, v); }
DEV unsigned uf(float v) { return __builtin_bit_cast(unsigned, v); }
DEV float add_x32(float v) { auto r = __builtin_amdgcn_permlane32_swap(uf(v), uf(v), false, false); const unsigned a = r[0], b = r[1]; return fu(a) + fu(b); }
DEV float add_x16(float v) { auto r = __builtin_amdgcn_permlane16_swap(uf(v), uf(v), false, false); const unsigned a = r[0], b = r[1]; return fu(a) + fu(b); }
DEV float max_x32(float v) { auto r = __builtin_amdgcn_permlane32_swap(uf(v), uf(v), false, false); const unsigned a = r[0], b = r[1]; return __builtin_fmaxf(fu(a), fu(b)); }
template <int C> DEV float add_dpp(float v) { return v + fu(__builtin_amdgcn_update_dpp(0u, uf(v), C, 0xf, 0xf, true)); }
DEV float add_x8(float v) { return v + fu(__builtin_amdgcn_update_dpp(0u, uf(v), 0x128, 0xf, 0xf, true)); }
constexpr u64 diag_mask(int r) { u64 m = 0; for (int l = 0; l < 64; ++l) { const int key = 16 * ((r >> 2) & 1) + 8 * (l >> 5) + 4 * (r >> 3) + (r & 3); if (key <= (l & 31)) m |= 1ull << l; } return m; }
DEV float sel(u64 m, float t, float f) { return __builtin_amdgcn_inverse_ballot_w64(m) ? t : f; }

template <unsigned LDS> DEV void touch(unsigned voff, u4 rs, unsigned soff) {
    asm volatile("s_mov_b32 m0, %3\n\ts_nop 0\n\tbuffer_load_dword %0, %1, %2 offen lds" ::"v"(voff), "s"(rs), "s"(soff), "i"(LDS) : "memory"); }
typedef short sh2 __attribute__((ext_vector_type(2)));
constexpr int kgo(int i) { return i < 8 ? 4 * i : (i - 8) / 3 * 4 + (i - 8) % 3 + 1; }
DEV bool any_lane(bool c) { return __builtin_amdgcn_ballot_w64(c) != 0; }
#define UNLIKELY(x) __builtin_expect(!!(x), 0)
#define NRETRY()
#define FRESH16(d, voff, rs, soff) do { if (UNLIKELY(any_lane((d)[3] == ~0u))) { \
        _Pragma("unroll 1") for (int s_ = 0; s_ < SPIN_CAP; ++s_) { NRETRY(); u4 t_; asm volatile("buffer_load_dwordx4 %0, %1, %2, %3 offen sc1\n\ts_waitcnt vmcnt(0)" : "=&v"(t_) : "v"(voff), "s"(rs), "s"(soff) : "memory"); \
            d = t_; if (!any_lane(t_[3] == ~0u)) break; __builtin_amdgcn_s_sleep(2); } } } while (0)
#define POLL(voff) do { \
        _Pragma("unroll 1") for (int s_ = 0; s_ < 4 * SPIN_CAP; ++s_) { u2 t_; asm volatile("buffer_load_dwordx2 %0, %1, %2, 0 offen sc1\n\ts_waitcnt vmcnt(0)" : "=&v"(t_) : "v"(voff), "s"(rsF) : "memory"); \
            if (!any_lane(t_[0] != tag[0] || t_[1] != tag[1])) break; NRETRY(); __builtin_amdgcn_s_sleep(1); } } while (0)
#define FLAG(off) st8(tag, l == 0 ? (off) : OOB, rsF, 0)
#define STAMP(i)

__global__ __launch_bounds__(512, 2) void fused_block(const char* __restrict__ x, const char* __restrict__ wqkv, const char* __restrict__ wo, const char* __restrict__ wg,
                                                      const char* __restrict__ wu, const char* __restrict__ wd, char* __restrict__ out, char* __restrict__ ws) {
#if defined(__HIP_DEVICE_COMPILE__)
    const unsigned l = threadIdx.x & 63, w = __builtin_amdgcn_readfirstlane(threadIdx.x >> 6);
    const unsigned b = blockIdx.x, xc = b & 7, rk = b >> 3;
    const unsigned T64 = 8 * (xc >> 1) + (rk >> 2), hq = 4 * (xc & 1) + (rk & 3);
    STAMP(0);
    const unsigned u8 = l >> 3, l7 = l & 7;
    u4 xr[8];
    const u4 rsX = mk_rsrc(x + T64 * 65536u, 65536u);
    const unsigned vx = (8 * w + u8) * 1024 + l7 * 16;
    ld16_imm<0>(xr[0], vx, rsX); ld16_imm<128>(xr[1], vx, rsX); ld16_imm<256>(xr[2], vx, rsX); ld16_imm<384>(xr[3], vx, rsX);
#define XB 6
    if (XB > 4) { ld16_imm<512>(xr[4], vx, rsX); ld16_imm<640>(xr[5], vx, rsX); }
    if (XB > 6) { ld16_imm<768>(xr[6], vx, rsX); ld16_imm<896>(xr[7], vx, rsX); }
    const u4 rsW = mk_rsrc(wqkv + hq * 128u, 512u * 3072u - hq * 128u);
    SBAR();
    const unsigned w3 = w & 3, wk2 = w3 * 2048;
    unsigned vw0 = (16 * w3 + u8) * 3072 + 32 * ((l7 >> 1) ^ ((l >> 4) & 1)) + 16 * (l7 & 1), vw1 = (vw0 + 24576) ^ 64;
#define SLOT(i) ((((i) + 1) & 3) * 24576u)
#define SP(i, p) dma_bufi<SLOT(i) + ((p) >> 1) * 8192 + ((p) & 1) * 1024, (i) * 196608u + ((p) >> 1) * 1024u>(wk2, ((p) & 1) ? vw1 : vw0, rsW, 0)
    const unsigned U64 = 4 * xc + (rk >> 3), fb = rk & 7;
    const unsigned g = 2 * (xc >> 1) + (rk >> 4), r = 16 * (xc & 1) + (rk & 15);
    const unsigned j = w & 1, hh = w >> 1;
    const unsigned t16 = l & 15, g4 = l >> 4, h = l >> 5, t32 = l & 31, q4 = t16 >> 2, pp = l & 3, l16 = l * 16, l128 = l * 128;
    const u4 rsF = mk_rsrc(ws, 16u << 20);
    const u4 ones = {0x3f803f80u, 0x3f803f80u, 0x3f803f80u, 0x3f803f80u};
    const unsigned kvb = WS_KV + (4 * hq + hh) * 131072u;
    const unsigned skv = kvb + (2 * T64 + j) * 2048;
    const unsigned sOw = WS_O + (2 * T64 + j) * 32768u + (4 * hq + hh) * 1024;
    u2 tag; u4 xres;
    unsigned vres = (32 * j + 8 * hh + l7) * 1024 + fb * 128 + u8 * 16;
    unsigned xw0 = L_A + (8 * w + u8) * 1024 + 16 * (l7 ^ (8 * (w & 1) + u8)), xw1 = xw0 ^ 128;
    unsigned xa[4];
#pragma unroll
    for (int q = 0; q < 4; ++q) { xa[q] = L_A + (32 * j + t16) * 1024 + 16 * ((4 * q + g4) ^ t16); asm volatile("" : "+v"(xa[q])); }
    unsigned wa = (8 * g4 + q4) * 128 + 32 * (hh ^ ((q4 >> 1) | ((g4 & 1) << 1))) + 8 * pp;
    f4 aq[2] = {}, ak[2] = {}, av[2] = {};
    unsigned vk = (h * 32 + (16 * ((t32 >> 3) & 1) + 8 * ((t32 >> 2) & 1) + 4 * (t32 >> 4) + (t32 & 3))) * 16, vv_ = (h * 16 + t16) * 16;
    unsigned vst = 1024 + ((g4 & 1) * 32 + (g4 >> 1) * 16 + t16) * 16;
    unsigned vrow = u8 * 1024 + 16 * (l7 ^ (((l >> 4) & 1) << 2));
    const char* gp[3];
#pragma unroll
    for (int i = 0; i < 3; ++i) { const unsigned z = 64 * i + l, k = (z * 43691u) >> 19, c = z - 12 * k, m = c >= 6, cc = c - 6 * m; unsigned un = 3 * r + (cc >> 1); un = un > 85 ? 85 : un;
        gp[i] = (m ? wu : wg) + ((16 * w + k) * 2752 + un * 32 + (cc & 1) * 16); asm volatile("" : "+v"(gp[i])); }
    const unsigned wk3 = w * 3072;
#define GU(R, c) dma_glbi<(R) * 24576 + (c) * 1024>(wk3, gp[c] + (R) * 128 * 2752)
#define GUD(R, q) dma_glbi<(R) * 24576 + ((q) / 3) * 12288 + ((q) % 3) * 1024>(wk3, gp[(q) % 3] + ((R) * 128 + ((q) / 3) * 64) * 2752)
    asm volatile("" : "+v"(vw0), "+v"(vw1), "+v"(xw0), "+v"(xw1), "+v"(wa), "+v"(vres), "+v"(vk), "+v"(vv_), "+v"(vrow), "+v"(vst));
    STAMP(39);
    if (XB <= 4) { ld16_imm<512>(xr[4], vx, rsX); ld16_imm<640>(xr[5], vx, rsX); }
    if (XB <= 6) { ld16_imm<768>(xr[6], vx, rsX); ld16_imm<896>(xr[7], vx, rsX); }
#define NO ((void)0)
#define SSTAMP(i)
    {
        f2 xf[32], s1 = {0.f, 0.f}, s2 = {0.f, 0.f};
#define LN_SUM(i, N, D0, D1) do { WAITV1D(N, 7 - (i), xr[i]); _Pragma("unroll") for (int e = 0; e < 4; ++e) { const unsigned d_ = xr[i][e]; f2 v_ = {blo(d_), bhi(d_)}; asm volatile("" : "+v"(v_)); xf[4 * (i) + e] = v_; \
        s1 += v_; s2 += v_ * v_; } asm volatile("" : "+v"(s1), "+v"(s2)); if (w < 4) { D0; D1; } SBAR(); } while (0)
        LN_SUM(0, 7, NO, NO); LN_SUM(1, 6, NO, NO); LN_SUM(2, 5, NO, NO); LN_SUM(3, 4, NO, NO);
        LN_SUM(4, 3, SP(0, 0), SP(0, 1)); LN_SUM(5, 4, SP(0, 2), SP(0, 3)); LN_SUM(6, 5, SP(0, 4), NO); LN_SUM(7, 5, SP(0, 5), NO);
        SSTAMP(34);
        if (w < 4) { SP(1, 0); SP(1, 1); SP(1, 2); SP(1, 3); SP(1, 4); SP(1, 5); }
        else {
            const unsigned c = h ? 7 : w - 1, tv = t32 < 24 && (!h || w == 4) ? (c * 64 + (T64 & 7) * 8 + l7) * 3072 + (t32 >> 3) * 1024 : OOB;
            touch<SLOT(3)>(tv, rsW, 0); }
        const float t1 = add_dpp<0x141>(add_dpp<0x4e>(add_dpp<0xb1>(s1[0] + s1[1]))), t2 = add_dpp<0x141>(add_dpp<0x4e>(add_dpp<0xb1>(s2[0] + s2[1])));
        const float mu = t1 * (1.f / 512), rs = __builtin_amdgcn_rsqf(__builtin_fmaxf(t2 * (1.f / 512) - mu * mu, 0.f) + 1e-5f), mr = -mu * rs;
#pragma unroll
        for (int i = 0; i < 8; ++i) sts16(((i & 1) ? xw1 : xw0) + (i >> 1) * 256, (u4){pk2(xf[4 * i] * rs + mr), pk2(xf[4 * i + 1] * rs + mr), pk2(xf[4 * i + 2] * rs + mr), pk2(xf[4 * i + 3] * rs + mr)});
    }
#define P1_STEP(n, D0, D1, D2) do { constexpr int c_ = (n) & 1, x_ = c_ ^ 1; constexpr bool m_ = (n) < 15; \
        const unsigned xb = xa[((n) + 1) & 3] + (((n) + 1) >> 2) * 256, wb = wa + SLOT(((n) + 1) >> 1) + x_ * 4096; \
        if (m_) X0[x_] = lds16(xb); SBAR(); \
        aq[0] = MFMA16(Wq[c_], X0[c_], aq[0]); if (m_) Wq[x_] = ldtr(wb, wb + 512); SBAR(); \
        aq[1] = MFMA16(Wq[c_], X1[c_], aq[1]); if (m_) X1[x_] = lds16(xb + 16384); D0; SBAR(); \
        ak[0] = MFMA16(Wk[c_], X0[c_], ak[0]); if (m_) Wk[x_] = ldtr(wb + 8192, wb + 8704); SBAR(); \
        ak[1] = MFMA16(Wk[c_], X1[c_], ak[1]); D1; SBAR(); \
        av[0] = MFMA16(X0[c_], Wv[c_], av[0]); if (m_) Wv[x_] = ldtr(wb + 16384, wb + 16896); SBAR(); \
        av[1] = MFMA16(X1[c_], Wv[c_], av[1]); D2; SBAR(); } while (0)
#define P1_FIRST() do { const unsigned wb = wa + SLOT(0); X0[0] = lds16(xa[0]); Wq[0] = ldtr(wb, wb + 512); X1[0] = lds16(xa[0] + 16384); Wk[0] = ldtr(wb + 8192, wb + 8704); Wv[0] = ldtr(wb + 16384, wb + 16896); } while (0)
#define XRES() ld16_plain(xres, vres, mk_rsrc(x + U64 * 65536u, 65536u), 0)
    if (w < 4) {
        u4 X0[2], X1[2], Wq[2], Wk[2], Wv[2];
        SP(2, 0); SP(2, 1); SP(2, 2); SP(2, 3); SP(2, 4); SP(2, 5);
        BAR_VM(12); STAMP(16);  P1_FIRST(); P1_STEP(0, (SP(3, 0), SP(3, 1)), (SP(3, 2), SP(3, 3)), (SP(3, 4), SP(3, 5)));
        BAR_VM(12); SSTAMP(17); P1_STEP(1, SP(4, 0), SP(4, 1), SP(4, 2)); P1_STEP(2, SP(4, 3), SP(4, 4), SP(4, 5));
        BAR_VM(12); SSTAMP(18); P1_STEP(3, SP(5, 0), SP(5, 1), SP(5, 2)); P1_STEP(4, SP(5, 3), SP(5, 4), SP(5, 5));
        BAR_VM(12); SSTAMP(19); P1_STEP(5, SP(6, 0), SP(6, 1), SP(6, 2)); P1_STEP(6, SP(6, 3), SP(6, 4), SP(6, 5));
        BAR_VM(12); SSTAMP(20); P1_STEP(7, SP(7, 0), SP(7, 1), SP(7, 2)); P1_STEP(8, SP(7, 3), SP(7, 4), SP(7, 5));
        BAR_VM(12); SSTAMP(21); P1_STEP(9, XRES(), GUD(1, 0), NO); P1_STEP(10, GUD(1, 1), GUD(1, 2), NO);
        BAR_VM(10); SSTAMP(22); P1_STEP(11, GUD(1, 3), GUD(1, 4), NO); P1_STEP(12, GUD(1, 5), NO, NO);
        BAR_VM(7);  SSTAMP(23); P1_STEP(13, NO, NO, NO); P1_STEP(14, NO, NO, NO); P1_STEP(15, NO, NO, NO);
        WAITV1(6, xres); tag = (u2){0u, 0u};
    } else {
        u4 X0[2], X1[2], Wq[2], Wk[2], Wv[2];
        const u4 bad = {~0u, ~0u, ~0u, ~0u};
#define MK(v, s_) st16(bad, v, rsF, s_)
        BAR_VM(0); STAMP(16);  P1_FIRST(); P1_STEP(0, ld8(tag, 0u * l, rsF, 0), MK(l16, skv), NO);
        BAR_L(); SSTAMP(17); P1_STEP(1, NO, MK(l16 + 1024, skv), NO); P1_STEP(2, NO, MK(l16, skv - 262144), NO);
        BAR_L(); SSTAMP(18); P1_STEP(3, NO, MK(l16 + 1024, skv - 262144), NO); P1_STEP(4, NO, MK(l16, sOw), NO);
        BAR_L(); SSTAMP(19); P1_STEP(5, NO, MK(l16, sOw - 2048), NO); P1_STEP(6, XRES(), NO, NO);
        unsigned tch = 0;
        const char* tw = wo + (fb * 128 + ((rk >> 3) * 128 + (w & 1) * 64 + l) * 1024);
        const char* tg = ((rk >> 4) ? wu : wg) + ((w3 * 32 + (l >> 1)) * 2752 + (r > 28 ? 28 : r) * 96 + (l & 1) * (r < 28 ? 92 : 60));
#define TCH(p) NO
        BAR_L(); SSTAMP(20); P1_STEP(7, if (w < 6) TCH(tw), NO, NO); P1_STEP(8, TCH(tg + 256 * 2752), NO, NO);
        BAR_L(); SSTAMP(21); P1_STEP(9, NO, NO, NO); P1_STEP(10, TCH(tg), NO, NO);
        asm volatile("s_waitcnt vmcnt(0)" : "+v"(tag), "+v"(xres) :: "memory");
        BAR_L(); SSTAMP(22);
        { const unsigned lo = tag.x + 1u; tag.y += (lo == 0u); tag.x = lo; }
                             P1_STEP(11, FLAG(w == 4 ? WS_F1 + (T64 * 8 + hq) * 128 : OOB), NO, NO); P1_STEP(12, NO, NO, NO);
        BAR_L(); SSTAMP(23); P1_STEP(13, NO, NO, NO); P1_STEP(14, NO, NO, NO); P1_STEP(15, NO, NO, NO);
        WAITV1(1, tch);
    }
    STAMP(1);
    u4 Qf;
    {
        { auto a = __builtin_amdgcn_permlane16_swap(pk2(lo2(ak[0])), pk2(lo2(ak[1])), false, false); auto c = __builtin_amdgcn_permlane16_swap(pk2(hi2(ak[0])), pk2(hi2(ak[1])), false, false);
          st16((u4){a[0], c[0], a[1], c[1]}, l16, rsF, skv); }
        st16((u4){pk2(lo2(av[0])), pk2(hi2(av[0])), pk2(lo2(av[1])), pk2(hi2(av[1]))}, vst, rsF, skv);
        STAMP(32);
        SBAR();
        constexpr float QS = 0.25f * 1.4426950408889634f;
        { auto a = __builtin_amdgcn_permlane16_swap(pk2(lo2(aq[0]) * QS), pk2(lo2(aq[1]) * QS), false, false); auto c = __builtin_amdgcn_permlane16_swap(pk2(hi2(aq[0]) * QS), pk2(hi2(aq[1]) * QS), false, false);
          Qf = (u4){a[0], c[0], a[1], c[1]}; }
        sts16(L_OC + (w ^ 1) * 2304 + l16, Qf);
    }
    const int Tb = 2 * (int)T64 - 8;
    u4 K[5], V0[5], V1[5];
    const int blk[5] = {Tb + 8 + (int)j, Tb + (int)j, Tb + 2 + 3 * (int)j, Tb + 3 + 3 * (int)j, Tb + 4 + 3 * (int)j};
#pragma unroll
    for (int i = 0; i < 5; ++i) { const unsigned o = blk[i] < 0 ? 0u : 0x3f803f80u; V0[i] = (u4){o, o, o, o}; V1[i] = V0[i]; asm volatile("" : "+v"(V0[i]), "+v"(V1[i])); }
    unsigned pm[8];
    { const short uq = (short)(t32 - 8 * h);
#pragma unroll
      for (int i = 0; i < 8; ++i) { const short c = (short)(16 * ((i >> 1) & 1) + 4 * (i >> 2) + 2 * (i & 1)); pm[i] = __builtin_bit_cast(unsigned, ((sh2){uq, uq} - (sh2){c, (short)(c + 1)}) >> (sh2){15, 15}); asm volatile("" : "+v"(pm[i])); } }
    unsigned so[5];
#pragma unroll
    for (int i = 0; i < 5; ++i) so[i] = blk[i] < 0 ? OOB : kvb + (unsigned)blk[i] * 2048;
    if (w == 7) {
        const int Tp = (int)T64 - 1 - (int)(l & 3); const unsigned vc = WS_F1 + (l < 4 ? (Tp < 0 ? T64 : (unsigned)Tp) * 8 + hq : U64 * 8 + l7) * 128; POLL(vc); }
    STAMP(35);
    BAR_VM(0);
    STAMP(2);
#define KVLD(i) do { ld16(K[i], vk, rsF, so[i]); ld16_v(V0[i], V1[i], vv_, rsF, so[i]); } while (0)
#define KVOK(N, i) do { WAITV3(N, K[i], V0[i], V1[i]); \
        if (UNLIKELY(any_lane(max(max(K[i][3], V0[i][3]), V1[i][3]) == ~0u))) { \
            _Pragma("unroll 1") for (int s_ = 0; s_ < SPIN_CAP; ++s_) { NRETRY(); KVLD(i); WAITV3(0, K[i], V0[i], V1[i]); if (!any_lane(max(max(K[i][3], V0[i][3]), V1[i][3]) == ~0u)) break; __builtin_amdgcn_s_sleep(2); } } } while (0)
    KVLD(2); KVLD(3);
    const u4 Qm = Qf, Qo = lds16(L_OC + w * 2304 + l16);
    f16 Om = {}, Oo = {};
    const f16 z16 = {};
    auto pv_tile = [&](const f16& S, const u4& va, const u4& vb, f16& O) {
        float p[16];
#pragma unroll
        for (int e = 0; e < 16; ++e) p[e] = __builtin_amdgcn_exp2f(S[e]);
        const u4 P0 = {pk(p[0], p[1]), pk(p[2], p[3]), pk(p[4], p[5]), pk(p[6], p[7])}, P1 = {pk(p[8], p[9]), pk(p[10], p[11]), pk(p[12], p[13]), pk(p[14], p[15])};
        O = MFMA32(va, P0, O); O = MFMA32(vb, P1, O);
    };
    const u4 rsWo = mk_rsrc(wo + fb * 128u, 512u * 1024u - fb * 128u);
    const unsigned wol = L_A + w * 8192, wos = w * 65536;
    const unsigned vh = (u8 >> 1) * 1024 + ((u8 & 1) * 32 + 8 * hh + l7) * 16, vs = l < 8 ? (8 * hh + l) * 8 : OOB;
    const unsigned sHw = WS_H + (2 * U64 + j) * 32768u + fb * 4096, sSw = WS_ST + (2 * U64 + j) * 2048u + fb * 256;
#define WO_(i) dma_bufi<(i) * 1024, (i) * 8192>(wol, vrow, rsWo, wos)
#define PIN16(a) asm volatile("" : "+v"(a))
#define MEM(...) do { SBAR(); __VA_ARGS__; SBAR(); } while (0)
#define ATT2(i, M0, M1, M2) do { f16 Sa = MFMA32(K[i], Qm, z16), Sb = MFMA32(K[i], Qo, z16); MEM(M0); pv_tile(Sa, V0[i], V1[i], Om); PIN16(Om); MEM(M1); pv_tile(Sb, V0[i], V1[i], Oo); PIN16(Oo); MEM(M2); } while (0)
    KVOK(3, 2);
    STAMP(24);
    ATT2(2, KVLD(4), KVLD(0), KVLD(1));
    KVOK(9, 3);
    SSTAMP(42);
    ATT2(3, (WO_(0), WO_(1)), (WO_(2), WO_(3), WO_(4)), (WO_(5), WO_(6), WO_(7)));
    KVOK(14, 4);
    SSTAMP(43);
    { const u4 bad = {~0u, ~0u, ~0u, ~0u};
      ATT2(4, (st16(bad, vh, rsF, sHw), st8((u2){~0u, ~0u}, vs, rsF, sSw)), (GU(2, 0), GU(2, 1)), GU(2, 2)); }
    KVOK(16, 0);
    KVOK(13, 1);
    SSTAMP(44);
    if (j == 0) { const f16 Sb = MFMA32(K[0], Qo, z16); pv_tile(Sb, V0[0], V1[0], Oo); }
    else        { const f16 Sb = MFMA32(K[1], Qo, z16); pv_tile(Sb, V0[1], V1[1], Oo); }
    const unsigned oc = L_OC + w * 2304;
    {
        const f16 Sx = MFMA32(K[0], Qm, z16), Sy = MFMA32(K[1], Qm, z16);
        sts16f(oc + l16, (f4){Oo[0], Oo[1], Oo[2], Oo[3]}); sts16f(oc + 1024 + l16, (f4){Oo[4], Oo[5], Oo[6], Oo[7]}); sts4f(oc + 2048 + l * 4, Oo[8]);
        float p[16];
#pragma unroll
        for (int e = 0; e < 16; ++e) p[e] = __builtin_amdgcn_exp2f(sel(diag_mask(e), Sx[e], Sy[e]));
        unsigned P[8];
#pragma unroll
        for (int e = 0; e < 8; ++e) P[e] = pk(p[2 * e], p[2 * e + 1]);
        const u4 Y0 = {P[0] & pm[0], P[1] & pm[1], P[2] & pm[2], P[3] & pm[3]}, Y1 = {P[4] & pm[4], P[5] & pm[5], P[6] & pm[6], P[7] & pm[7]};
        const u4 X0_ = {P[0] ^ Y0[0], P[1] ^ Y0[1], P[2] ^ Y0[2], P[3] ^ Y0[3]}, X1_ = {P[4] ^ Y1[0], P[5] ^ Y1[1], P[6] ^ Y1[2], P[7] ^ Y1[3]};
        Om = MFMA32(V0[0], X0_, Om); Om = MFMA32(V1[0], X1_, Om); Om = MFMA32(V0[1], Y0, Om); Om = MFMA32(V1[1], Y1, Om);
    }
    SBAR();
    STAMP(25);
    BAR_VM(3);
    SSTAMP(45);
    const unsigned op = L_OC + (w ^ 1) * 2304;
    f4 oa = lds16f(op + l16) + (f4){Om[0], Om[1], Om[2], Om[3]}, ob = lds16f(op + 1024 + l16) + (f4){Om[4], Om[5], Om[6], Om[7]};
    float den = Om[8] + lds4f(op + 2048 + l * 4);
    if (UNLIKELY(any_lane(!(den >= 0x1p-60f && den <= 0x1p60f)))) {
        f16 O = {}; float m = -__builtin_inff();
#pragma unroll 1
        for (int kb = 8; kb >= 0; --kb) {
            const int T = 2 * (int)T64 + (int)j - 8 + kb;
            if (T < 0) break;
            u4 Ks, Va = ones, Vb = ones;
            ld16(Ks, vk, rsF, kvb + (unsigned)T * 2048); ld16_v(Va, Vb, vv_, rsF, kvb + (unsigned)T * 2048);
            WAITV3(0, Ks, Va, Vb);
            f16 S = MFMA32(Ks, Qm, z16);
            const u64 all = (kb != 0 && kb != 8) ? ~0ull : 0ull, inv = kb == 0 ? ~0ull : 0ull;
            float mx = m;
#pragma unroll
            for (int e = 0; e < 16; ++e) { S[e] = sel((diag_mask(e) ^ inv) | all, S[e], -__builtin_inff()); mx = __builtin_fmaxf(mx, S[e]); }
            mx = max_x32(mx);
            const float al = __builtin_amdgcn_exp2f(m - mx); m = mx;
#pragma unroll
            for (int e = 0; e < 16; ++e) { O[e] *= al; S[e] -= mx; }
            pv_tile(S, Va, Vb, O);
        }
        oa = (f4){O[0], O[1], O[2], O[3]}; ob = (f4){O[4], O[5], O[6], O[7]}; den = O[8];
    }
    const float rd = __builtin_amdgcn_rcpf(den);
    st16((u4){pk2(lo2(oa) * rd), pk2(hi2(oa) * rd), pk2(lo2(ob) * rd), pk2(hi2(ob) * rd)}, l16, rsF, sOw);
    if (w == 4) FLAG(WS_F2 + (U64 * 8 + fb) * 128);
    STAMP(26);
    const unsigned ta0 = (4 * h + q4) * 128 + 64 * (q4 >> 1) + 32 * (g4 & 1) + 8 * pp, ta1 = ta0 ^ 64;
    f16 acc0 = {}, acc1 = {};
    {
        const unsigned sO = WS_O + (2 * U64 + j) * 32768u + hh * 8192;
        const unsigned oa0 = L_A + hh * 16384 + ta0, oa1 = L_A + hh * 16384 + ta1;
        u4 A0[8], A1[8], B[8];
#pragma unroll
        for (int i = 0; i < 8; ++i) { A0[i] = ldtr(oa0 + i * 2048, oa0 + i * 2048 + 1024); A1[i] = ldtr(oa1 + i * 2048, oa1 + i * 2048 + 1024); }
        STAMP(37);
        if (w == 4) BAR_VM(1); else BAR_VM(0);
        STAMP(5);
#pragma unroll
        for (int i = 0; i < 8; ++i) ld16(B[i], l16, rsF, sO + i * 1024);
#define P3_PAIR(i, NW, M0, M1) do { asm volatile("s_waitcnt vmcnt(%2)" : "+v"(B[i]), "+v"(B[(i) + 1]) : "i"(NW) : "memory"); \
            if (UNLIKELY(any_lane(max(B[i][3], B[(i) + 1][3]) == ~0u))) { FRESH16(B[i], l16, rsF, sO + (i) * 1024); FRESH16(B[(i) + 1], l16, rsF, sO + ((i) + 1) * 1024); } if ((i) == 0) STAMP(4); \
            acc0 = MFMA32(A0[i], B[i], acc0); acc1 = MFMA32(A1[i], B[i], acc1); M0; SBAR(); acc0 = MFMA32(A0[(i) + 1], B[(i) + 1], acc0); acc1 = MFMA32(A1[(i) + 1], B[(i) + 1], acc1); M1; SBAR(); } while (0)
        P3_PAIR(0, 6, NO, NO); P3_PAIR(2, 4, GU(0, 0), GU(0, 1)); P3_PAIR(4, 4, GU(0, 2), GU(3, 0)); P3_PAIR(6, 4, GU(3, 1), GU(3, 2));
    }
    u2 ck; const unsigned vc2 = WS_F2 + (g * 32 + t32) * 128;
    ld8(ck, w == 7 ? vc2 : OOB, rsF, 0);
    STAMP(27);
    const unsigned rw0 = L_A + (hh * 2 + j) * 8192 + h * 512 + t32 * 16, rw1 = rw0 ^ 128, rr = L_A + j * 8192 + u8 * 1024 + (((8 * hh + l7) ^ ((u8 & 1) << 3)) * 16);
    auto reduce = [&](f4& lo, f4& hi) {
#pragma unroll
        for (int u = 0; u < 4; ++u) {
            sts16f(((u & 1) ? rw1 : rw0) + u * 1024, (f4){acc0[4 * u], acc0[4 * u + 1], acc0[4 * u + 2], acc0[4 * u + 3]});
            sts16f(((u & 1) ? rw1 : rw0) + (4 + u) * 1024, (f4){acc1[4 * u], acc1[4 * u + 1], acc1[4 * u + 2], acc1[4 * u + 3]});
        }
        BAR_L();
        lo = (lds16f(rr) + lds16f(rr + 16384)) + (lds16f(rr + 32768) + lds16f(rr + 49152));
        hi = (lds16f(rr + 512) + lds16f(rr + 16896)) + (lds16f(rr + 33280) + lds16f(rr + 49664));
    };
    u4 Hres;
    const unsigned sG = WS_G + (8 * g + w) * 88064u + 3 * r * 1024;
#define SG(u) (3 * r + (u) < 86 ? sG + (u) * 1024 : OOB)
    const unsigned ga = (8 * h + q4) * 192 + (g4 & 1) * 96 + 8 * pp;
    f16 cs[3] = {};
    {
        f4 lo, hi; reduce(lo, hi);
        STAMP(28);
        Hres = (u4){pk2(lo2(lo) + (f2){blo(xres[0]), bhi(xres[0])}), pk2(hi2(lo) + (f2){blo(xres[1]), bhi(xres[1])}), pk2(lo2(hi) + (f2){blo(xres[2]), bhi(xres[2])}), pk2(hi2(hi) + (f2){blo(xres[3]), bhi(xres[3])})};
        st16(Hres, vh, rsF, sHw);
        f2 s1 = {0.f, 0.f}, s2 = {0.f, 0.f};
#pragma unroll
        for (int e = 0; e < 4; ++e) { const f2 a = {blo(Hres[e]), bhi(Hres[e])}; s1 += a; s2 += a * a; }
        const float t1 = add_x32(add_x16(add_x8(s1[0] + s1[1]))), t2 = add_x32(add_x16(add_x8(s2[0] + s2[1])));
        st8((u2){uf(t1), uf(t2)}, vs, rsF, sSw);
        STAMP(33);
        asm volatile("s_waitcnt vmcnt(2) lgkmcnt(0)\n\ts_barrier" : "+v"(ck) :: "memory");
        STAMP(40);
        const unsigned o = ga + w * 12288;
        u4 a[12];
#pragma unroll
        for (int i = 0; i < 12; ++i) a[i] = ldtr(o + (i / 3) * 3072 + (i % 3) * 32, o + (i / 3) * 3072 + (i % 3) * 32 + 768);
        SBAR();
#pragma unroll
        for (int i = 0; i < 12; ++i) cs[i % 3] = MFMA32(a[i], ones, cs[i % 3]);
        asm volatile("" : "+v"(cs[0]), "+v"(cs[1]), "+v"(cs[2]));
    }
    STAMP(36);
    const u4 rsWd = mk_rsrc(wd + fb * 128u, 1376u * 1024u - fb * 128u);
    if (t32 == 0) {
#pragma unroll
        for (int u = 0; u < 3; ++u)
#pragma unroll
            for (int qd = 0; qd < 4; ++qd) sts16f(L_CSP + w * 384 + u * 128 + qd * 32 + h * 16, (f4){cs[u][4 * qd], cs[u][4 * qd + 1], cs[u][4 * qd + 2], cs[u][4 * qd + 3]});
    }
    if (w == 7 && UNLIKELY(any_lane(ck[0] != tag[0] || ck[1] != tag[1]))) POLL(vc2);
    BAR_VM(0);
    STAMP(6);
    const unsigned d2l = hh * 6144 + j * 3072, d2s = (352 * hh + 112 + 24 * j) * 1024;
#define WD2(R, i) dma_bufi<(R) * 24576 + (i) * 1024, (R) * 49152 + (i) * 8192>(d2l, vrow, rsWd, d2s)
    {
        const unsigned sH = WS_H + (8 * g + w) * 32768u, sS = WS_ST + (8 * g + w) * 2048u;
#define PD4 6
        constexpr int PD = PD4;
        u4 Hb[32], A0[2], A1[2];
        u2 st[4];
        f16 ag[3] = {};
        const unsigned cf = L_CF + w * 384, vstat = h * 1024 + t32 * 8;
#pragma unroll
        for (int i = 0; i < PD; ++i) ld16(Hb[kgo(i)], l16, rsF, sH + kgo(i) * 1024);
        if (l < 48) { f2 s = {0.f, 0.f};
#pragma unroll
            for (int i = 0; i < 8; ++i) s += *(LAS const f2*)(L_CSP + i * 384 + l * 8);
            sts8f(cf + l * 8, s); }
        A0[0] = ldtr(ga, ga + 768); A1[0] = ldtr(ga + 32, ga + 800);
        unsigned mx = 0;
#define P4_STEP(i) do { constexpr int c = (i) & 1, n = c ^ 1, k = kgo(i); const unsigned o = ga + ((i) < 31 ? kgo((i) + 1) * 3072 : 64); \
            if ((i) + PD < 32) ld16(Hb[kgo((i) + PD < 32 ? (i) + PD : 0)], l16, rsF, sH + kgo((i) + PD) * 1024); \
            else if ((i) + PD < 36) ld8(st[((i) + PD - 32) & 3], vstat, rsF, sS + ((i) + PD - 32) * 256); \
            WAITV1((i) + PD < 36 ? PD : 35 - (i), Hb[k]); \
            if ((i) < 8) FRESH16(Hb[k], l16, rsF, sH + k * 1024); else { mx = max(mx, Hb[k][3]); asm volatile("" : "+v"(mx)); } if ((i) == 0) STAMP(7); SBAR(); \
            ag[0] = MFMA32(A0[c], Hb[k], ag[0]); A0[n] = ldtr(o, o + 768); SBAR(); \
            ag[1] = MFMA32(A1[c], Hb[k], ag[1]); if ((i) < 31) A1[n] = ldtr(o + 32, o + 800); SBAR(); } while (0)
#define P4_4(i) P4_STEP(i); P4_STEP((i) + 1); P4_STEP((i) + 2); P4_STEP((i) + 3)
        P4_4(0); P4_4(4); P4_4(8); P4_4(12); P4_4(16); P4_4(20); P4_4(24); P4_4(28);
        WAITV4(0, st[0], st[1], st[2], st[3]);
        if (UNLIKELY(any_lane(max(max(mx, max(st[0][1], st[1][1])), max(st[2][1], st[3][1])) == ~0u))) {
#pragma unroll
            for (int i = 0; i < 32; ++i) FRESH16(Hb[i], l16, rsF, sH + i * 1024);
#pragma unroll 1
            for (int s_ = 0; s_ < SPIN_CAP; ++s_) { NRETRY();
#pragma unroll
                for (int i = 0; i < 4; ++i) ld8(st[i], vstat, rsF, sS + i * 256);
                WAITV4(0, st[0], st[1], st[2], st[3]);
                if (!any_lane(max(max(st[0][1], st[1][1]), max(st[2][1], st[3][1])) == ~0u)) break;
            }
            ag[0] = z16; ag[1] = z16;
#pragma unroll
            for (int i = 0; i < 32; ++i) { const unsigned o = ga + kgo(i) * 3072; ag[0] = MFMA32(ldtr(o, o + 768), Hb[kgo(i)], ag[0]); ag[1] = MFMA32(ldtr(o + 32, o + 800), Hb[kgo(i)], ag[1]); }
        }
        STAMP(8);
        const float t1 = add_x32(fu(st[0].x) + fu(st[1].x) + fu(st[2].x) + fu(st[3].x)), t2 = add_x32(fu(st[0].y) + fu(st[1].y) + fu(st[2].y) + fu(st[3].y));
        float nm = t1 * (-1.f / 512), ve = __builtin_fmaxf(t2 * (1.f / 512) - nm * nm, 0.f) + 1e-5f;
        const float rs = __builtin_amdgcn_rsqf(ve);
        float ka = rs * -2.3022081986f, kb = rs * rs * rs * -0.1029432397f;
        asm volatile("" : "+v"(nm), "+v"(ve), "+v"(ka), "+v"(kb));
        const unsigned d1l = L_A + hh * 14336 + 7 * j * 1024, d1s = (352 * hh + 56 * j) * 1024;
#define WD1(i) dma_bufi<(i) * 1024, (i) * 8192>(d1l, vrow, rsWd, d1s)
        f4 cg, cu; f2 gt, gu, zz; u4 Gy;
#define GE_C(u, qd) do { cg = lds16f(cf + (u) * 128 + (qd) * 32 + h * 16); cu = lds16f(cf + (u) * 128 + (2 + (qd)) * 32 + h * 16); } while (0)
#define GE_0(u, pr) do { gt = (f2){ag[u][2 * (pr)], ag[u][2 * (pr) + 1]} + (((pr) & 1) ? hi2(cg) : lo2(cg)) * nm; gu = (f2){ag[u][8 + 2 * (pr)], ag[u][9 + 2 * (pr)]} + (((pr) & 1) ? hi2(cu) : lo2(cu)) * nm; \
            zz = gt * gt * kb + ka; asm volatile("" : "+v"(gt), "+v"(gu), "+v"(zz)); } while (0)
#define GE_1() do { zz = gt * zz; gu = gt * gu; zz = (f2){__builtin_amdgcn_exp2f(zz[0]), __builtin_amdgcn_exp2f(zz[1])}; asm volatile("" : "+v"(gu), "+v"(zz)); } while (0)
#define GE_2() do { zz = zz * ve + ve; zz = (f2){__builtin_amdgcn_rcpf(zz[0]), __builtin_amdgcn_rcpf(zz[1])}; asm volatile("" : "+v"(zz)); } while (0)
#define GE_3(pr) do { unsigned y_ = pk2(gu * zz); asm volatile("" : "+v"(y_)); Gy[pr] = y_; } while (0)
        GE_C(0, 0);
#define P4B_STEP(i) do { constexpr int c = (i) & 1, n = c ^ 1, p = (i) >> 2, u = p >> 2, pr = p & 3; const unsigned o = ga + ((i) + 1) * 3072 + 64; \
            if ((i) == 16) BAR_L(); SBAR(); \
            ag[2] = MFMA32(A0[c], Hb[i], ag[2]); if ((i) < 31) A0[n] = ldtr(o, o + 768); SBAR(); \
            if (((i) & 3) == 0) GE_0(u, pr); if (((i) & 3) == 1) { GE_1(); if ((pr & 1) && p < 7) GE_C((p + 1) >> 2, (((p + 1) >> 1) & 1)); } if (((i) & 3) == 2) GE_2(); \
            if (((i) & 3) == 3) { GE_3(pr); if (pr == 3) st16(Gy, l16, rsF, SG(u)); } \
            if ((i) < 14 && !((i) & 1)) WD1(((i) >> 1) & 7); \
            if ((i) >= 17 && (i) < 29 && ((i) & 1)) WD2((((i) + 7) >> 1) / 3 & 1, (((i) + 7) >> 1) % 3); SBAR(); } while (0)
#define P4B_4(i) P4B_STEP(i); P4B_STEP((i) + 1); P4B_STEP((i) + 2); P4B_STEP((i) + 3)
        P4B_4(0); P4B_4(4); P4B_4(8); P4B_4(12); P4B_4(16); P4B_4(20); P4B_4(24); P4B_4(28);
        STAMP(41);
#pragma unroll
        for (int pr = 0; pr < 4; ++pr) {
            if (!(pr & 1)) GE_C(2, pr >> 1);
            const f2 a = (f2){ag[2][2 * pr], ag[2][2 * pr + 1]} + ((pr & 1) ? hi2(cg) : lo2(cg)) * nm, c = (f2){ag[2][8 + 2 * pr], ag[2][9 + 2 * pr]} + ((pr & 1) ? hi2(cu) : lo2(cu)) * nm;
            const f2 z = a * (a * a * kb + ka), d = (f2){__builtin_amdgcn_exp2f(z[0]), __builtin_amdgcn_exp2f(z[1])} * ve + ve;
            Gy[pr] = pk2(a * c * (f2){__builtin_amdgcn_rcpf(d[0]), __builtin_amdgcn_rcpf(d[1])});
        }
        st16(Gy, l16, rsF, SG(2));
        STAMP(30);
    }
    BAR_VM(9);
    STAMP(9);
    {
        u4 D0[7], D1[7];
#pragma unroll
        for (int i = 0; i < 7; ++i) { const unsigned o = L_A + hh * 14336 + i * 2048; D0[i] = ldtr(o + ta0, o + ta0 + 1024); D1[i] = ldtr(o + ta1, o + ta1 + 1024); }
        WD2(2, 0); WD2(2, 1); WD2(2, 2); WD2(3, 0); WD2(3, 1); WD2(3, 2);
        const unsigned d3l = L_A + hh * 6144 + 3 * j * 1024, d3s = (352 * hh + 304 + 24 * j) * 1024;
#define WD3(i) dma_bufi<(i) * 1024, (i) * 8192>(d3l, vrow, rsWd, d3s)
        BAR_VM(6);
        STAMP(29);
        if (w == 4) FLAG(WS_F3 + (g * 32 + r) * 128);
#define PSLEEP 6
        if (w == 7) { const unsigned vc = WS_F3 + (xc * 32 + t32) * 128; __builtin_amdgcn_s_sleep(PSLEEP); POLL(vc); }
        if (w == 6 && b == 0) {
#pragma unroll 1
            for (unsigned i = 0; i < 4; ++i) { const unsigned vc = WS_F1 + (64 * i + l) * 128; POLL(vc); }
            FLAG(0u);
        }
        BAR_L();
        STAMP(46);
        constexpr int PD = 6;
        u4 Gb[22], A0[2], A1[2];
        const unsigned gs = WS_G + (2 * U64 + j) * 88064u + 22 * hh * 1024, gz = hh == 3 ? OOB : gs, o0 = hh * 6144 + ta0, o1 = hh * 6144 + ta1, p0 = L_A + hh * 6144 + ta0, p1 = L_A + hh * 6144 + ta1;
#pragma unroll
        for (int i = 0; i < PD; ++i) ld16(Gb[i], l16, rsF, gs + i * 1024);
        WD3(0); WD3(1); WD3(2);
        acc0 = z16; acc1 = z16;
#define P5_STEP(i) do { constexpr int c = (i) & 1, n = c ^ 1; \
            if ((i) + PD < 22) ld16(Gb[(i) + PD < 22 ? (i) + PD : 0], l16, rsF, ((i) + PD >= 20 ? gz : gs) + ((i) + PD) * 1024); \
            WAITV1(((i) + PD < 22 ? PD : 21 - (i)) + ((i) < PD ? 3 : 0), Gb[i]); if ((i) == 0) STAMP(10); \
            if ((i) == 6) BAR_L(); SBAR(); \
            constexpr int m_ = (i) >= 6 ? (i) - 6 : 0; constexpr unsigned q_ = (m_ / 3) * 24576 + (m_ % 3) * 2048; \
            const unsigned a0_ = (i) < 18 ? o0 + q_ : p0 + ((i) - 18) * 2048, a1_ = (i) < 18 ? o1 + q_ : p1 + ((i) - 18) * 2048; \
            acc0 = MFMA32((i) < 7 ? D0[(i) < 7 ? (i) : 0] : A0[c], Gb[i], acc0); if ((i) >= 6 && (i) < 21) A0[n] = ldtr(a0_, a0_ + 1024); SBAR(); \
            acc1 = MFMA32((i) < 7 ? D1[(i) < 7 ? (i) : 0] : A1[c], Gb[i], acc1); if ((i) >= 6 && (i) < 21) A1[n] = ldtr(a1_, a1_ + 1024); SBAR(); } while (0)
        P5_STEP(0); P5_STEP(1); P5_STEP(2); P5_STEP(3); P5_STEP(4); P5_STEP(5); P5_STEP(6); P5_STEP(7); P5_STEP(8); P5_STEP(9); P5_STEP(10);
        P5_STEP(11); P5_STEP(12); P5_STEP(13); P5_STEP(14); P5_STEP(15); P5_STEP(16); P5_STEP(17); P5_STEP(18); P5_STEP(19); P5_STEP(20); P5_STEP(21);
        }
    STAMP(11);
    BAR_L();
    {
        f4 lo, hi; reduce(lo, hi);
        const u4 Ores = {pk2(lo2(lo) + (f2){blo(Hres[0]), bhi(Hres[0])}), pk2(hi2(lo) + (f2){blo(Hres[1]), bhi(Hres[1])}), pk2(lo2(hi) + (f2){blo(Hres[2]), bhi(Hres[2])}), pk2(hi2(hi) + (f2){blo(Hres[3]), bhi(Hres[3])})};
        st16_plain(Ores, vres, mk_rsrc(out + U64 * 65536u, 65536u), 0);
    }
    STAMP(12);
#endif
}

extern "C" void kernel_launch(void* const* d_in, const int* in_sizes, int n_in, void* d_out, int out_size, void* d_ws, size_t ws_size, hipStream_t stream) {
    (void)in_sizes; (void)n_in; (void)out_size; (void)ws_size;
    (void)hipFuncSetAttribute((const void*)fused_block, hipFuncAttributeMaxDynamicSharedMemorySize, LDS_BYTES);
    fused_block<<<256, 512, LDS_BYTES, stream>>>((const char*)d_in[0], (const char*)d_in[1], (const char*)d_in[2], (const char*)d_in[4], (const char*)d_in[5], (const char*)d_in[6],
                                                 (char*)d_out, (char*)d_ws);
}
#define LASTSTAMP 12
#define STAMP_NAMES "start", "P1 loop end", "KV drained", 0, "O first data", "O drained", "h drained", "h first data", "P4 pass1 end", "P4 end bar", "G first data", "P5 loop end", "end", 0, 0, 0, \
  "st0", "st1", "st2", "st3", "st4", "st5", "st6", "st7", "KV first data", "att done", "O stored", "P3 loop end", "P3 reduced", "G drained", "G stored", 0, "KV stored", "h stored", \
  "LN sums", "pre KV bar", "colsum done", "A read", 0, "setup done", "GU bar", "P4 pass2 end", "att blk3", "att blk4", "att blk0", "comb bar", "G flags"
```

```cpp
#include <hip/hip_runtime.h>
#include <hip/hip_bf16.h>
#include <stdint.h>
#pragma clang diagnostic ignored "-Wint-to-pointer-cast"

typedef __bf16 bf8 __attribute__((ext_vector_type(8)));
typedef __bf16 bf2 __attribute__((ext_vector_type(2)));
typedef short s4 __attribute__((ext_vector_type(4)));
typedef short s8 __attribute__((ext_vector_type(8)));
typedef float f2 __attribute__((ext_vector_type(2)));
typedef float f4 __attribute__((ext_vector_type(4)));
typedef float f16 __attribute__((ext_vector_type(16)));
typedef unsigned u2 __attribute__((ext_vector_type(2)));
typedef unsigned u4 __attribute__((ext_vector_type(4)));
typedef unsigned long long u64;
#define LAS __attribute__((address_space(3)))
#define DEV __device__ __forceinline__

constexpr unsigned WS_F1 = 32768, WS_F2 = 65536, WS_F3 = 98304;
constexpr unsigned WS_KV = 1u << 20;
constexpr unsigned WS_O = 5u << 20;
constexpr unsigned WS_H = 7u << 20;
constexpr unsigned WS_ST = 9u << 20;
constexpr unsigned WS_G = 10u << 20;
constexpr unsigned WS_DIAG = 32u << 20;
constexpr unsigned LDS_BYTES = 163840;
constexpr unsigned L_HOLE = 73728;
constexpr unsigned L_OC = L_HOLE;
constexpr unsigned L_A = 98304;
constexpr unsigned L_CSP = 155648, L_CF = L_CSP + 3072;
constexpr int SPIN_CAP = 1 << 14;
constexpr unsigned OOB = 0x40000000u;

#define MFMA16(a, b, c) __builtin_amdgcn_mfma_f32_16x16x32_bf16(__builtin_bit_cast(bf8, a), __builtin_bit_cast(bf8, b), c, 0, 0, 0)
#define MFMA32(a, b, c) __builtin_amdgcn_mfma_f32_32x32x16_bf16(__builtin_bit_cast(bf8, a), __builtin_bit_cast(bf8, b), c, 0, 0, 0)
#define SBAR() __builtin_amdgcn_sched_barrier(0)
#define BAR_VM(N) asm volatile("s_waitcnt vmcnt(%0) lgkmcnt(0)\n\ts_barrier" ::"i"(N) : "memory")
#define BAR_L() asm volatile("s_waitcnt lgkmcnt(0)\n\ts_barrier" ::: "memory")

DEV u4 mk_rsrc(const void* p, unsigned bytes) { const u64 a = (u64)p; return (u4){(unsigned)a, (unsigned)(a >> 32) & 0xffffu, bytes, 0x00020000u}; }
DEV void dma_buf(unsigned lds, unsigned voff, u4 rs, unsigned soff) {
    asm volatile("s_mov_b32 m0, %0\n\ts_nop 0\n\tbuffer_load_dwordx4 %1, %2, %3 offen lds" ::"s"(lds), "v"(voff), "s"(rs), "s"(soff) : "memory"); }
template <unsigned LI, unsigned SI> DEV void dma_bufi(unsigned lds, unsigned voff, u4 rs, unsigned soff) {
    unsigned t; asm volatile("s_add_u32 m0, %1, %5\n\ts_add_u32 %0, %4, %6\n\tbuffer_load_dwordx4 %2, %3, %0 offen lds" : "=&s"(t) : "s"(lds), "v"(voff), "s"(rs), "s"(soff), "i"(LI), "i"(SI) : "memory", "scc"); }
template <unsigned LI> DEV void dma_glbi(unsigned lds, const char* p) {
    asm volatile("s_add_u32 m0, %0, %2\n\ts_nop 0\n\tglobal_load_lds_dwordx4 %1, off" ::"s"(lds), "v"(p), "i"(LI) : "memory", "scc"); }
DEV void dma_glb(unsigned lds, const char* p) {
    asm volatile("s_mov_b32 m0, %0\n\ts_nop 0\n\tglobal_load_lds_dwordx4 %1, off" ::"s"(lds), "v"(p) : "memory"); }
DEV void ld16(u4& d, unsigned voff, u4 rs, unsigned soff) {
    asm volatile("buffer_load_dwordx4 %0, %1, %2, %3 offen sc1" : "=v"(d) : "v"(voff), "s"(rs), "s"(soff) : "memory"); }
DEV void ld16_plain(u4& d, unsigned voff, u4 rs, unsigned soff) {
    asm volatile("buffer_load_dwordx4 %0, %1, %2, %3 offen" : "=v"(d) : "v"(voff), "s"(rs), "s"(soff) : "memory"); }
template <int OFF> DEV void ld16_imm(u4& d, unsigned voff, u4 rs) {
    asm volatile("buffer_load_dwordx4 %0, %1, %2, 0 offen offset:%3" : "=v"(d) : "v"(voff), "s"(rs), "i"(OFF) : "memory"); }
DEV void ld8(u2& d, unsigned voff, u4 rs, unsigned soff) {
    asm volatile("buffer_load_dwordx2 %0, %1, %2, %3 offen sc1" : "=v"(d) : "v"(voff), "s"(rs), "s"(soff) : "memory"); }
DEV void ld16_v(u4& d0, u4& d1, unsigned voff, u4 rs, unsigned soff) {
    asm volatile("s_mov_b64 exec, %5\n\tbuffer_load_dwordx4 %0, %2, %3, %4 offen offset:1024 sc1\n\tbuffer_load_dwordx4 %1, %2, %3, %4 offen offset:1536 sc1\n\ts_mov_b64 exec, -1"
                 : "+v"(d0), "+v"(d1) : "v"(voff), "s"(rs), "s"(soff), "s"(0x0000FFFF0000FFFFull) : "memory"); }
DEV void st16(u4 d, unsigned voff, u4 rs, unsigned soff) {
    asm volatile("buffer_store_dwordx4 %0, %1, %2, %3 offen sc1\n\ts_nop 1" ::"v"(d), "v"(voff), "s"(rs), "s"(soff) : "memory"); }
DEV void st16_plain(u4 d, unsigned voff, u4 rs, unsigned soff) {
    asm volatile("buffer_store_dwordx4 %0, %1, %2, %3 offen sc0 sc1\n\ts_nop 1" ::"v"(d), "v"(voff), "s"(rs), "s"(soff) : "memory"); }
DEV void st8(u2 d, unsigned voff, u4 rs, unsigned soff) {
    asm volatile("buffer_store_dwordx2 %0, %1, %2, %3 offen sc1\n\ts_nop 1" ::"v"(d), "v"(voff), "s"(rs), "s"(soff) : "memory"); }
#define WAITV1(N, a) asm volatile("s_waitcnt vmcnt(%1)" : "+v"(a) : "i"(N) : "memory")
#define WAITV1D(ND, NS, a) asm volatile("s_waitcnt vmcnt(%1)\n\ts_cmp_lt_u32 %3, 4\n\ts_cbranch_scc1 .Lw%=\n\ts_waitcnt vmcnt(%2)\n.Lw%=:" : "+v"(a) : "i"(ND), "i"(NS), "s"(w) : "memory", "scc")
#define WAITV3(N, a, b, c) asm volatile("s_waitcnt vmcnt(%3)" : "+v"(a), "+v"(b), "+v"(c) : "i"(N) : "memory")
#define WAITV4(N, a, b, c, d) asm volatile("s_waitcnt vmcnt(%4)" : "+v"(a), "+v"(b), "+v"(c), "+v"(d) : "i"(N) : "memory")

DEV u4 lds16(unsigned a) { return *(LAS const u4*)a; }
DEV f4 lds16f(unsigned a) { return *(LAS const f4*)a; }
DEV float lds4f(unsigned a) { return *(LAS const float*)a; }
DEV void sts16(unsigned a, u4 v) { *(LAS u4*)a = v; }
DEV void sts16f(unsigned a, f4 v) { *(LAS f4*)a = v; }
DEV void sts8f(unsigned a, f2 v) { *(LAS f2*)a = v; }
DEV void sts4f(unsigned a, float v) { *(LAS float*)a = v; }
DEV u4 ldtr(unsigned a0, unsigned a1) {
    const s4 lo = __builtin_amdgcn_ds_read_tr16_b64_v4i16((LAS s4*)a0), hi = __builtin_amdgcn_ds_read_tr16_b64_v4i16((LAS s4*)a1);
    return __builtin_bit_cast(u4, __builtin_shufflevector(lo, hi, 0, 1, 2, 3, 4, 5, 6, 7)); }
DEV unsigned pk2(f2 a) { return __builtin_bit_cast(unsigned, __builtin_convertvector(a, bf2)); }
DEV f2 lo2(f4 a) { return (f2){a[0], a[1]}; }
DEV f2 hi2(f4 a) { return (f2){a[2], a[3]}; }
DEV unsigned pk(float a, float b) { return __builtin_bit_cast(unsigned, __builtin_convertvector((f2){a, b}, bf2)); }
DEV float blo(unsigned v) { return __builtin_bit_cast(float, v << 16); }
DEV float bhi(unsigned v) { return __builtin_bit_cast(float, v & 0xffff0000u); }
DEV float fu(unsigned v) { return __builtin_bit_cast(float, v); }
DEV unsigned uf(float v) { return __builtin_bit_cast(unsigned, v); }
DEV float add_x32(float v) { auto r = __builtin_amdgcn_permlane32_swap(uf(v), uf(v), false, false); const unsigned a = r[0], b = r[1]; return fu(a) + fu(b); }
DEV float add_x16(float v) { auto r = __builtin_amdgcn_permlane16_swap(uf(v), uf(v), false, false); const unsigned a = r[0], b = r[1]; return fu(a) + fu(b); }
DEV float max_x32(float v) { auto r = __builtin_amdgcn_permlane32_swap(uf(v), uf(v), false, false); const unsigned a = r[0], b = r[1]; return __builtin_fmaxf(fu(a), fu(b)); }
template <int C> DEV float add_dpp(float v) { return v + fu(__builtin_amdgcn_update_dpp(0u, uf(v), C, 0xf, 0xf, true)); }
DEV float add_x8(float v) { return v + fu(__builtin_amdgcn_update_dpp(0u, uf(v), 0x128, 0xf, 0xf, true)); }
constexpr u64 diag_mask(int r) { u64 m = 0; for (int l = 0; l < 64; ++l) { const int key = 16 * ((r >> 2) & 1) + 8 * (l >> 5) + 4 * (r >> 3) + (r & 3); if (key <= (l & 31)) m |= 1ull << l; } return m; }
DEV float sel(u64 m, float t, float f) { return __builtin_amdgcn_inverse_ballot_w64(m) ? t : f; }

template <unsigned LDS> DEV void touch(unsigned voff, u4 rs, unsigned soff) {
    asm volatile("s_mov_b32 m0, %3\n\ts_nop 0\n\tbuffer_load_dword %0, %1, %2 offen lds" ::"v"(voff), "s"(rs), "s"(soff), "i"(LDS) : "memory"); }
typedef short sh2 __attribute__((ext_vector_type(2)));
constexpr int kgo(int i) { return i < 8 ? 4 * i : (i - 8) / 3 * 4 + (i - 8) % 3 + 1; }
DEV bool any_lane(bool c) { return __builtin_amdgcn_ballot_w64(c) != 0; }
#define UNLIKELY(x) __builtin_expect(!!(x), 0)
#define NRETRY()
#define FRESH16(d, voff, rs, soff) do { if (UNLIKELY(any_lane((d)[3] == ~0u))) { \
        _Pragma("unroll 1") for (int s_ = 0; s_ < SPIN_CAP; ++s_) { NRETRY(); u4 t_; asm volatile("buffer_load_dwordx4 %0, %1, %2, %3 offen sc1\n\ts_waitcnt vmcnt(0)" : "=&v"(t_) : "v"(voff), "s"(rs), "s"(soff) : "memory"); \
            d = t_; if (!any_lane(t_[3] == ~0u)) break; __builtin_amdgcn_s_sleep(2); } } } while (0)
#define POLL(voff) do { \
        _Pragma("unroll 1") for (int s_ = 0; s_ < 4 * SPIN_CAP; ++s_) { u2 t_; asm volatile("buffer_load_dwordx2 %0, %1, %2, 0 offen sc1\n\ts_waitcnt vmcnt(0)" : "=&v"(t_) : "v"(voff), "s"(rsF) : "memory"); \
            if (!any_lane(t_[0] != tag[0] || t_[1] != tag[1])) break; NRETRY(); __builtin_amdgcn_s_sleep(1); } } while (0)
#define FLAG(off) st8(tag, l == 0 ? (off) : OOB, rsF, 0)
#define STAMP(i)

__global__ __launch_bounds__(512, 2) void fused_block(const char* __restrict__ x, const char* __restrict__ wqkv, const char* __restrict__ wo, const char* __restrict__ wg,
                                                      const char* __restrict__ wu, const char* __restrict__ wd, char* __restrict__ out, char* __restrict__ ws) {
#if defined(__HIP_DEVICE_COMPILE__)
    const unsigned l = threadIdx.x & 63, w = __builtin_amdgcn_readfirstlane(threadIdx.x >> 6);
    const unsigned b = blockIdx.x, xc = b & 7, rk = b >> 3;
    const unsigned T64 = 8 * (xc >> 1) + (rk >> 2), hq = 4 * (xc & 1) + (rk & 3);
    STAMP(0);
    const unsigned u8 = l >> 3, l7 = l & 7;
    u4 xr[8];
    const u4 rsX = mk_rsrc(x + T64 * 65536u, 65536u);
    const unsigned vx = (8 * w + u8) * 1024 + l7 * 16;
    {
        const char* pc; unsigned nb; asm volatile("s_getpc_b64 %0\n.Lfb_code0:\n\ts_mov_b32 %1, .Lfb_code1-.Lfb_code0" : "=s"(pc), "=s"(nb));
        const char* p = l < 6 ? wqkv + ((b * 8 + w) * 6 + l) * 128 : pc + (((rk * 8 + w) * 2 + (l & 1)) * 128 < nb ? ((rk * 8 + w) * 2 + (l & 1)) * 128 : 0);
        asm volatile("s_mov_b32 m0, %1\n\ts_nop 0\n\tglobal_load_lds_dword %0, off" ::"v"(p), "s"(L_A + w * 8192) : "memory"); }
    ld16_imm<0>(xr[0], vx, rsX); ld16_imm<128>(xr[1], vx, rsX); ld16_imm<256>(xr[2], vx, rsX); ld16_imm<384>(xr[3], vx, rsX);
#define XB 6
    if (XB > 4) { ld16_imm<512>(xr[4], vx, rsX); ld16_imm<640>(xr[5], vx, rsX); }
    if (XB > 6) { ld16_imm<768>(xr[6], vx, rsX); ld16_imm<896>(xr[7], vx, rsX); }
    const u4 rsW = mk_rsrc(wqkv + hq * 128u, 512u * 3072u - hq * 128u);
    SBAR();
    const unsigned w3 = w & 3, wk2 = w3 * 2048;
    unsigned vw0 = (16 * w3 + u8) * 3072 + 32 * ((l7 >> 1) ^ ((l >> 4) & 1)) + 16 * (l7 & 1), vw1 = (vw0 + 24576) ^ 64;
#define SLOT(i) ((((i) + 1) & 3) * 24576u)
#define SP(i, p) dma_bufi<SLOT(i) + ((p) >> 1) * 8192 + ((p) & 1) * 1024, (i) * 196608u + ((p) >> 1) * 1024u>(wk2, ((p) & 1) ? vw1 : vw0, rsW, 0)
    const unsigned U64 = 4 * xc + (rk >> 3), fb = rk & 7;
    const unsigned g = 2 * (xc >> 1) + (rk >> 4), r = 16 * (xc & 1) + (rk & 15);
    const unsigned j = w & 1, hh = w >> 1;
    const unsigned t16 = l & 15, g4 = l >> 4, h = l >> 5, t32 = l & 31, q4 = t16 >> 2, pp = l & 3, l16 = l * 16, l128 = l * 128;
    const u4 rsF = mk_rsrc(ws, 16u << 20);
    const u4 ones = {0x3f803f80u, 0x3f803f80u, 0x3f803f80u, 0x3f803f80u};
    const unsigned kvb = WS_KV + (4 * hq + hh) * 131072u;
    const unsigned skv = kvb + (2 * T64 + j) * 2048;
    const unsigned sOw = WS_O + (2 * T64 + j) * 32768u + (4 * hq + hh) * 1024;
    u2 tag; u4 xres;
    unsigned vres = (32 * j + 8 * hh + l7) * 1024 + fb * 128 + u8 * 16;
    unsigned xw0 = L_A + (8 * w + u8) * 1024 + 16 * (l7 ^ (8 * (w & 1) + u8)), xw1 = xw0 ^ 128;
    unsigned xa[4];
#pragma unroll
    for (int q = 0; q < 4; ++q) { xa[q] = L_A + (32 * j + t16) * 1024 + 16 * ((4 * q + g4) ^ t16); asm volatile("" : "+v"(xa[q])); }
    unsigned wa = (8 * g4 + q4) * 128 + 32 * (hh ^ ((q4 >> 1) | ((g4 & 1) << 1))) + 8 * pp;
    f4 aq[2] = {}, ak[2] = {}, av[2] = {};
    unsigned vk = (h * 32 + (16 * ((t32 >> 3) & 1) + 8 * ((t32 >> 2) & 1) + 4 * (t32 >> 4) + (t32 & 3))) * 16, vv_ = (h * 16 + t16) * 16;
    unsigned vst = 1024 + ((g4 & 1) * 32 + (g4 >> 1) * 16 + t16) * 16;
    unsigned vrow = u8 * 1024 + 16 * (l7 ^ (((l >> 4) & 1) << 2));
    const char* gp[3];
#pragma unroll
    for (int i = 0; i < 3; ++i) { const unsigned z = 64 * i + l, k = (z * 43691u) >> 19, c = z - 12 * k, m = c >= 6, cc = c - 6 * m; unsigned un = 3 * r + (cc >> 1); un = un > 85 ? 85 : un;
        gp[i] = (m ? wu : wg) + ((16 * w + k) * 2752 + un * 32 + (cc & 1) * 16); asm volatile("" : "+v"(gp[i])); }
    const unsigned wk3 = w * 3072;
#define GU(R, c) dma_glbi<(R) * 24576 + (c) * 1024>(wk3, gp[c] + (R) * 128 * 2752)
#define GUD(R, q) dma_glbi<(R) * 24576 + ((q) / 3) * 12288 + ((q) % 3) * 1024>(wk3, gp[(q) % 3] + ((R) * 128 + ((q) / 3) * 64) * 2752)
    asm volatile("" : "+v"(vw0), "+v"(vw1), "+v"(xw0), "+v"(xw1), "+v"(wa), "+v"(vres), "+v"(vk), "+v"(vv_), "+v"(vrow), "+v"(vst));
    STAMP(39);
    if (XB <= 4) { ld16_imm<512>(xr[4], vx, rsX); ld16_imm<640>(xr[5], vx, rsX); }
    if (XB <= 6) { ld16_imm<768>(xr[6], vx, rsX); ld16_imm<896>(xr[7], vx, rsX); }
#define NO ((void)0)
#define TBPOS 0
#define TOUCH_B() do { const char* tp = (w == 4 ? wo : w == 5 ? wg : w == 6 ? wu : wd) + (w == 4 ? b * 16 + (l & 15) : b * 43 + (l < 43 ? l : 0)) * 128; \
        asm volatile("global_load_dword v255, %0, off" ::"v"(tp) : "memory", "v255"); } while (0)
#if !defined(NOTB) && TBPOS == 0
    if (w >= 4) TOUCH_B();
#define XW 1
#else
#define XW 0
#endif
#define SSTAMP(i)
    {
        f2 xf[32], s1 = {0.f, 0.f}, s2 = {0.f, 0.f};
#define LN_SUM(i, N, D0, D1) do { if (XW && (N) < 8 - (i)) WAITV1(N, xr[i]); else WAITV1D(N, 7 - (i) + XW, xr[i]); _Pragma("unroll") for (int e = 0; e < 4; ++e) { const unsigned d_ = xr[i][e]; f2 v_ = {blo(d_), bhi(d_)}; asm volatile("" : "+v"(v_)); xf[4 * (i) + e] = v_; \
        s1 += v_; s2 += v_ * v_; } asm volatile("" : "+v"(s1), "+v"(s2)); if (w < 4) { D0; D1; } SBAR(); } while (0)
        LN_SUM(0, 7, NO, NO); LN_SUM(1, 6, NO, NO); LN_SUM(2, 5, NO, NO); LN_SUM(3, 4, NO, NO);
        LN_SUM(4, 3, SP(0, 0), SP(0, 1)); LN_SUM(5, 4, SP(0, 2), SP(0, 3)); LN_SUM(6, 5, SP(0, 4), NO); LN_SUM(7, 5, SP(0, 5), NO);
        SSTAMP(34);
        if (w < 4) { SP(1, 0); SP(1, 1); SP(1, 2); SP(1, 3); SP(1, 4); SP(1, 5); }
#if !defined(NOTB) && TBPOS == 1
        else TOUCH_B();
#endif
        const float t1 = add_dpp<0x141>(add_dpp<0x4e>(add_dpp<0xb1>(s1[0] + s1[1]))), t2 = add_dpp<0x141>(add_dpp<0x4e>(add_dpp<0xb1>(s2[0] + s2[1])));
        const float mu = t1 * (1.f / 512), rs = __builtin_amdgcn_rsqf(__builtin_fmaxf(t2 * (1.f / 512) - mu * mu, 0.f) + 1e-5f), mr = -mu * rs;
#pragma unroll
        for (int i = 0; i < 8; ++i) sts16(((i & 1) ? xw1 : xw0) + (i >> 1) * 256, (u4){pk2(xf[4 * i] * rs + mr), pk2(xf[4 * i + 1] * rs + mr), pk2(xf[4 * i + 2] * rs + mr), pk2(xf[4 * i + 3] * rs + mr)});
    }
#define P1_STEP(n, D0, D1, D2) do { constexpr int c_ = (n) & 1, x_ = c_ ^ 1; constexpr bool m_ = (n) < 15; \
        const unsigned xb = xa[((n) + 1) & 3] + (((n) + 1) >> 2) * 256, wb = wa + SLOT(((n) + 1) >> 1) + x_ * 4096; \
        if (m_) X0[x_] = lds16(xb); SBAR(); \
        aq[0] = MFMA16(Wq[c_], X0[c_], aq[0]); if (m_) Wq[x_] = ldtr(wb, wb + 512); SBAR(); \
        aq[1] = MFMA16(Wq[c_], X1[c_], aq[1]); if (m_) X1[x_] = lds16(xb + 16384); D0; SBAR(); \
        ak[0] = MFMA16(Wk[c_], X0[c_], ak[0]); if (m_) Wk[x_] = ldtr(wb + 8192, wb + 8704); SBAR(); \
        ak[1] = MFMA16(Wk[c_], X1[c_], ak[1]); D1; SBAR(); \
        av[0] = MFMA16(X0[c_], Wv[c_], av[0]); if (m_) Wv[x_] = ldtr(wb + 16384, wb + 16896); SBAR(); \
        av[1] = MFMA16(X1[c_], Wv[c_], av[1]); D2; SBAR(); } while (0)
#define P1_FIRST() do { const unsigned wb = wa + SLOT(0); X0[0] = lds16(xa[0]); Wq[0] = ldtr(wb, wb + 512); X1[0] = lds16(xa[0] + 16384); Wk[0] = ldtr(wb + 8192, wb + 8704); Wv[0] = ldtr(wb + 16384, wb + 16896); } while (0)
#define XRES() ld16_plain(xres, vres, mk_rsrc(x + U64 * 65536u, 65536u), 0)
    if (w < 4) {
        u4 X0[2], X1[2], Wq[2], Wk[2], Wv[2];
        SP(2, 0); SP(2, 1); SP(2, 2); SP(2, 3); SP(2, 4); SP(2, 5);
        BAR_VM(12); STAMP(16);  P1_FIRST(); P1_STEP(0, (SP(3, 0), SP(3, 1)), (SP(3, 2), SP(3, 3)), (SP(3, 4), SP(3, 5)));
        BAR_VM(12); SSTAMP(17); P1_STEP(1, SP(4, 0), SP(4, 1), SP(4, 2)); P1_STEP(2, SP(4, 3), SP(4, 4), SP(4, 5));
        BAR_VM(12); SSTAMP(18); P1_STEP(3, SP(5, 0), SP(5, 1), SP(5, 2)); P1_STEP(4, SP(5, 3), SP(5, 4), SP(5, 5));
        BAR_VM(12); SSTAMP(19); P1_STEP(5, SP(6, 0), SP(6, 1), SP(6, 2)); P1_STEP(6, SP(6, 3), SP(6, 4), SP(6, 5));
        BAR_VM(12); SSTAMP(20); P1_STEP(7, SP(7, 0), SP(7, 1), SP(7, 2)); P1_STEP(8, SP(7, 3), SP(7, 4), SP(7, 5));
        BAR_VM(12); SSTAMP(21); P1_STEP(9, XRES(), GUD(1, 0), NO); P1_STEP(10, GUD(1, 1), GUD(1, 2), NO);
        BAR_VM(10); SSTAMP(22); P1_STEP(11, GUD(1, 3), GUD(1, 4), NO); P1_STEP(12, GUD(1, 5), NO, NO);
        BAR_VM(7);  SSTAMP(23); P1_STEP(13, NO, NO, NO); P1_STEP(14, NO, NO, NO); P1_STEP(15, NO, NO, NO);
        WAITV1(6, xres); tag = (u2){0u, 0u};
    } else {
        u4 X0[2], X1[2], Wq[2], Wk[2], Wv[2];
        const u4 bad = {~0u, ~0u, ~0u, ~0u};
#define MK(v, s_) st16(bad, v, rsF, s_)
#if !defined(NOTB) && TBPOS == 2
#define TCH(c) if (w == (c)) TOUCH_B()
#else
#define TCH(c) NO
#endif
        BAR_L(); STAMP(16);  P1_FIRST(); P1_STEP(0, ld8(tag, 0u * l, rsF, 0), MK(l16, skv), TCH(5));
        BAR_L(); SSTAMP(17); P1_STEP(1, NO, MK(l16 + 1024, skv), TCH(6)); P1_STEP(2, NO, MK(l16, skv - 262144), NO);
        BAR_L(); SSTAMP(18); P1_STEP(3, NO, MK(l16 + 1024, skv - 262144), TCH(4)); P1_STEP(4, NO, MK(l16, sOw), NO);
        BAR_L(); SSTAMP(19); P1_STEP(5, NO, MK(l16, sOw - 2048), TCH(7)); P1_STEP(6, XRES(), NO, NO);
        BAR_L(); SSTAMP(20); P1_STEP(7, NO, NO, NO); P1_STEP(8, NO, NO, NO);
        BAR_L(); SSTAMP(21); P1_STEP(9, NO, NO, NO); P1_STEP(10, NO, NO, NO);
        asm volatile("s_waitcnt vmcnt(0)" : "+v"(tag), "+v"(xres) :: "memory");
        BAR_L(); SSTAMP(22);
        { const unsigned lo = tag.x + 1u; tag.y += (lo == 0u); tag.x = lo; }
                             P1_STEP(11, FLAG(w == 4 ? WS_F1 + (T64 * 8 + hq) * 128 : OOB), NO, NO); P1_STEP(12, NO, NO, NO);
        BAR_L(); SSTAMP(23); P1_STEP(13, NO, NO, NO); P1_STEP(14, NO, NO, NO); P1_STEP(15, NO, NO, NO);
    }
    STAMP(1);
    u4 Qf;
    {
        { auto a = __builtin_amdgcn_permlane16_swap(pk2(lo2(ak[0])), pk2(lo2(ak[1])), false, false); auto c = __builtin_amdgcn_permlane16_swap(pk2(hi2(ak[0])), pk2(hi2(ak[1])), false, false);
          st16((u4){a[0], c[0], a[1], c[1]}, l16, rsF, skv); }
        st16((u4){pk2(lo2(av[0])), pk2(hi2(av[0])), pk2(lo2(av[1])), pk2(hi2(av[1]))}, vst, rsF, skv);
        STAMP(32);
        SBAR();
        constexpr float QS = 0.25f * 1.4426950408889634f;
        { auto a = __builtin_amdgcn_permlane16_swap(pk2(lo2(aq[0]) * QS), pk2(lo2(aq[1]) * QS), false, false); auto c = __builtin_amdgcn_permlane16_swap(pk2(hi2(aq[0]) * QS), pk2(hi2(aq[1]) * QS), false, false);
          Qf = (u4){a[0], c[0], a[1], c[1]}; }
        sts16(L_OC + (w ^ 1) * 2304 + l16, Qf);
    }
    const int Tb = 2 * (int)T64 - 8;
    u4 K[5], V0[5], V1[5];
    const int blk[5] = {Tb + 8 + (int)j, Tb + (int)j, Tb + 2 + 3 * (int)j, Tb + 3 + 3 * (int)j, Tb + 4 + 3 * (int)j};
#pragma unroll
    for (int i = 0; i < 5; ++i) { const unsigned o = blk[i] < 0 ? 0u : 0x3f803f80u; V0[i] = (u4){o, o, o, o}; V1[i] = V0[i]; asm volatile("" : "+v"(V0[i]), "+v"(V1[i])); }
    unsigned pm[8];
    { const short uq = (short)(t32 - 8 * h);
#pragma unroll
      for (int i = 0; i < 8; ++i) { const short c = (short)(16 * ((i >> 1) & 1) + 4 * (i >> 2) + 2 * (i & 1)); pm[i] = __builtin_bit_cast(unsigned, ((sh2){uq, uq} - (sh2){c, (short)(c + 1)}) >> (sh2){15, 15}); asm volatile("" : "+v"(pm[i])); } }
    unsigned so[5];
#pragma unroll
    for (int i = 0; i < 5; ++i) so[i] = blk[i] < 0 ? OOB : kvb + (unsigned)blk[i] * 2048;
    if (w == 7) {
        const int Tp = (int)T64 - 1 - (int)(l & 3); const unsigned vc = WS_F1 + (l < 4 ? (Tp < 0 ? T64 : (unsigned)Tp) * 8 + hq : U64 * 8 + l7) * 128; POLL(vc); }
    STAMP(35);
    BAR_VM(0);
    STAMP(2);
#define KVLD(i) do { ld16(K[i], vk, rsF, so[i]); ld16_v(V0[i], V1[i], vv_, rsF, so[i]); } while (0)
#define KVOK(N, i) do { WAITV3(N, K[i], V0[i], V1[i]); \
        if (UNLIKELY(any_lane(max(max(K[i][3], V0[i][3]), V1[i][3]) == ~0u))) { \
            _Pragma("unroll 1") for (int s_ = 0; s_ < SPIN_CAP; ++s_) { NRETRY(); KVLD(i); WAITV3(0, K[i], V0[i], V1[i]); if (!any_lane(max(max(K[i][3], V0[i][3]), V1[i][3]) == ~0u)) break; __builtin_amdgcn_s_sleep(2); } } } while (0)
    KVLD(2); KVLD(3);
    const u4 Qm = Qf, Qo = lds16(L_OC + w * 2304 + l16);
    f16 Om = {}, Oo = {};
    const f16 z16 = {};
    auto pv_tile = [&](const f16& S, const u4& va, const u4& vb, f16& O) {
        float p[16];
#pragma unroll
        for (int e = 0; e < 16; ++e) p[e] = __builtin_amdgcn_exp2f(S[e]);
        const u4 P0 = {pk(p[0], p[1]), pk(p[2], p[3]), pk(p[4], p[5]), pk(p[6], p[7])}, P1 = {pk(p[8], p[9]), pk(p[10], p[11]), pk(p[12], p[13]), pk(p[14], p[15])};
        O = MFMA32(va, P0, O); O = MFMA32(vb, P1, O);
    };
    const u4 rsWo = mk_rsrc(wo + fb * 128u, 512u * 1024u - fb * 128u);
    const unsigned wol = L_A + w * 8192, wos = w * 65536;
    const unsigned vh = (u8 >> 1) * 1024 + ((u8 & 1) * 32 + 8 * hh + l7) * 16, vs = l < 8 ? (8 * hh + l) * 8 : OOB;
    const unsigned sHw = WS_H + (2 * U64 + j) * 32768u + fb * 4096, sSw = WS_ST + (2 * U64 + j) * 2048u + fb * 256;
#define WO_(i) dma_bufi<(i) * 1024, (i) * 8192>(wol, vrow, rsWo, wos)
#define PIN16(a) asm volatile("" : "+v"(a))
#define MEM(...) do { SBAR(); __VA_ARGS__; SBAR(); } while (0)
#define ATT2(i, M0, M1, M2) do { f16 Sa = MFMA32(K[i], Qm, z16), Sb = MFMA32(K[i], Qo, z16); MEM(M0); pv_tile(Sa, V0[i], V1[i], Om); PIN16(Om); MEM(M1); pv_tile(Sb, V0[i], V1[i], Oo); PIN16(Oo); MEM(M2); } while (0)
    KVOK(3, 2);
    STAMP(24);
    ATT2(2, KVLD(4), KVLD(0), KVLD(1));
    KVOK(9, 3);
    SSTAMP(42);
    ATT2(3, (WO_(0), WO_(1)), (WO_(2), WO_(3), WO_(4)), (WO_(5), WO_(6), WO_(7)));
    KVOK(14, 4);
    SSTAMP(43);
    { const u4 bad = {~0u, ~0u, ~0u, ~0u};
      ATT2(4, (st16(bad, vh, rsF, sHw), st8((u2){~0u, ~0u}, vs, rsF, sSw)), (GU(2, 0), GU(2, 1)), GU(2, 2)); }
    KVOK(16, 0);
    KVOK(13, 1);
    SSTAMP(44);
    if (j == 0) { const f16 Sb = MFMA32(K[0], Qo, z16); pv_tile(Sb, V0[0], V1[0], Oo); }
    else        { const f16 Sb = MFMA32(K[1], Qo, z16); pv_tile(Sb, V0[1], V1[1], Oo); }
    const unsigned oc = L_OC + w * 2304;
    {
        const f16 Sx = MFMA32(K[0], Qm, z16), Sy = MFMA32(K[1], Qm, z16);
        sts16f(oc + l16, (f4){Oo[0], Oo[1], Oo[2], Oo[3]}); sts16f(oc + 1024 + l16, (f4){Oo[4], Oo[5], Oo[6], Oo[7]}); sts4f(oc + 2048 + l * 4, Oo[8]);
        float p[16];
#pragma unroll
        for (int e = 0; e < 16; ++e) p[e] = __builtin_amdgcn_exp2f(sel(diag_mask(e), Sx[e], Sy[e]));
        unsigned P[8];
#pragma unroll
        for (int e = 0; e < 8; ++e) P[e] = pk(p[2 * e], p[2 * e + 1]);
        const u4 Y0 = {P[0] & pm[0], P[1] & pm[1], P[2] & pm[2], P[3] & pm[3]}, Y1 = {P[4] & pm[4], P[5] & pm[5], P[6] & pm[6], P[7] & pm[7]};
        const u4 X0_ = {P[0] ^ Y0[0], P[1] ^ Y0[1], P[2] ^ Y0[2], P[3] ^ Y0[3]}, X1_ = {P[4] ^ Y1[0], P[5] ^ Y1[1], P[6] ^ Y1[2], P[7] ^ Y1[3]};
        Om = MFMA32(V0[0], X0_, Om); Om = MFMA32(V1[0], X1_, Om); Om = MFMA32(V0[1], Y0, Om); Om = MFMA32(V1[1], Y1, Om);
    }
    SBAR();
    STAMP(25);
    BAR_VM(3);
    SSTAMP(45);
    const unsigned op = L_OC + (w ^ 1) * 2304;
    f4 oa = lds16f(op + l16) + (f4){Om[0], Om[1], Om[2], Om[3]}, ob = lds16f(op + 1024 + l16) + (f4){Om[4], Om[5], Om[6], Om[7]};
    float den = Om[8] + lds4f(op + 2048 + l * 4);
    if (UNLIKELY(any_lane(!(den >= 0x1p-60f && den <= 0x1p60f)))) {
        f16 O = {}; float m = -__builtin_inff();
#pragma unroll 1
        for (int kb = 8; kb >= 0; --kb) {
            const int T = 2 * (int)T64 + (int)j - 8 + kb;
            if (T < 0) break;
            u4 Ks, Va = ones, Vb = ones;
            ld16(Ks, vk, rsF, kvb + (unsigned)T * 2048); ld16_v(Va, Vb, vv_, rsF, kvb + (unsigned)T * 2048);
            WAITV3(0, Ks, Va, Vb);
            f16 S = MFMA32(Ks, Qm, z16);
            const u64 all = (kb != 0 && kb != 8) ? ~0ull : 0ull, inv = kb == 0 ? ~0ull : 0ull;
            float mx = m;
#pragma unroll
            for (int e = 0; e < 16; ++e) { S[e] = sel((diag_mask(e) ^ inv) | all, S[e], -__builtin_inff()); mx = __builtin_fmaxf(mx, S[e]); }
            mx = max_x32(mx);
            const float al = __builtin_amdgcn_exp2f(m - mx); m = mx;
#pragma unroll
            for (int e = 0; e < 16; ++e) { O[e] *= al; S[e] -= mx; }
            pv_tile(S, Va, Vb, O);
        }
        oa = (f4){O[0], O[1], O[2], O[3]}; ob = (f4){O[4], O[5], O[6], O[7]}; den = O[8];
    }
    const float rd = __builtin_amdgcn_rcpf(den);
    st16((u4){pk2(lo2(oa) * rd), pk2(hi2(oa) * rd), pk2(lo2(ob) * rd), pk2(hi2(ob) * rd)}, l16, rsF, sOw);
    if (w == 4) FLAG(WS_F2 + (U64 * 8 + fb) * 128);
    STAMP(26);
    const unsigned ta0 = (4 * h + q4) * 128 + 64 * (q4 >> 1) + 32 * (g4 & 1) + 8 * pp, ta1 = ta0 ^ 64;
    f16 acc0 = {}, acc1 = {};
    {
        const unsigned sO = WS_O + (2 * U64 + j) * 32768u + hh * 8192;
        const unsigned oa0 = L_A + hh * 16384 + ta0, oa1 = L_A + hh * 16384 + ta1;
        u4 A0[8], A1[8], B[8];
#pragma unroll
        for (int i = 0; i < 8; ++i) { A0[i] = ldtr(oa0 + i * 2048, oa0 + i * 2048 + 1024); A1[i] = ldtr(oa1 + i * 2048, oa1 + i * 2048 + 1024); }
        STAMP(37);
        if (w == 4) BAR_VM(1); else BAR_VM(0);
        STAMP(5);
#pragma unroll
        for (int i = 0; i < 8; ++i) ld16(B[i], l16, rsF, sO + i * 1024);
#define P3_PAIR(i, NW, M0, M1) do { asm volatile("s_waitcnt vmcnt(%2)" : "+v"(B[i]), "+v"(B[(i) + 1]) : "i"(NW) : "memory"); \
            if (UNLIKELY(any_lane(max(B[i][3], B[(i) + 1][3]) == ~0u))) { FRESH16(B[i], l16, rsF, sO + (i) * 1024); FRESH16(B[(i) + 1], l16, rsF, sO + ((i) + 1) * 1024); } if ((i) == 0) STAMP(4); \
            acc0 = MFMA32(A0[i], B[i], acc0); acc1 = MFMA32(A1[i], B[i], acc1); M0; SBAR(); acc0 = MFMA32(A0[(i) + 1], B[(i) + 1], acc0); acc1 = MFMA32(A1[(i) + 1], B[(i) + 1], acc1); M1; SBAR(); } while (0)
        P3_PAIR(0, 6, NO, NO); P3_PAIR(2, 4, GU(0, 0), GU(0, 1)); P3_PAIR(4, 4, GU(0, 2), GU(3, 0)); P3_PAIR(6, 4, GU(3, 1), GU(3, 2));
    }
    u2 ck; const unsigned vc2 = WS_F2 + (g * 32 + t32) * 128;
    ld8(ck, w == 7 ? vc2 : OOB, rsF, 0);
    STAMP(27);
    const unsigned rw0 = L_A + (hh * 2 + j) * 8192 + h * 512 + t32 * 16, rw1 = rw0 ^ 128, rr = L_A + j * 8192 + u8 * 1024 + (((8 * hh + l7) ^ ((u8 & 1) << 3)) * 16);
    auto reduce = [&](f4& lo, f4& hi) {
#pragma unroll
        for (int u = 0; u < 4; ++u) {
            sts16f(((u & 1) ? rw1 : rw0) + u * 1024, (f4){acc0[4 * u], acc0[4 * u + 1], acc0[4 * u + 2], acc0[4 * u + 3]});
            sts16f(((u & 1) ? rw1 : rw0) + (4 + u) * 1024, (f4){acc1[4 * u], acc1[4 * u + 1], acc1[4 * u + 2], acc1[4 * u + 3]});
        }
        BAR_L();
        lo = (lds16f(rr) + lds16f(rr + 16384)) + (lds16f(rr + 32768) + lds16f(rr + 49152));
        hi = (lds16f(rr + 512) + lds16f(rr + 16896)) + (lds16f(rr + 33280) + lds16f(rr + 49664));
    };
    u4 Hres;
    const unsigned sG = WS_G + (8 * g + w) * 88064u + 3 * r * 1024;
#define SG(u) (3 * r + (u) < 86 ? sG + (u) * 1024 : OOB)
    const unsigned ga = (8 * h + q4) * 192 + (g4 & 1) * 96 + 8 * pp;
    f16 cs[3] = {};
    {
        f4 lo, hi; reduce(lo, hi);
        STAMP(28);
        Hres = (u4){pk2(lo2(lo) + (f2){blo(xres[0]), bhi(xres[0])}), pk2(hi2(lo) + (f2){blo(xres[1]), bhi(xres[1])}), pk2(lo2(hi) + (f2){blo(xres[2]), bhi(xres[2])}), pk2(hi2(hi) + (f2){blo(xres[3]), bhi(xres[3])})};
        st16(Hres, vh, rsF, sHw);
        f2 s1 = {0.f, 0.f}, s2 = {0.f, 0.f};
#pragma unroll
        for (int e = 0; e < 4; ++e) { const f2 a = {blo(Hres[e]), bhi(Hres[e])}; s1 += a; s2 += a * a; }
        const float t1 = add_x32(add_x16(add_x8(s1[0] + s1[1]))), t2 = add_x32(add_x16(add_x8(s2[0] + s2[1])));
        st8((u2){uf(t1), uf(t2)}, vs, rsF, sSw);
        STAMP(33);
        asm volatile("s_waitcnt vmcnt(2) lgkmcnt(0)\n\ts_barrier" : "+v"(ck) :: "memory");
        STAMP(40);
        const unsigned o = ga + w * 12288;
        u4 a[12];
#pragma unroll
        for (int i = 0; i < 12; ++i) a[i] = ldtr(o + (i / 3) * 3072 + (i % 3) * 32, o + (i / 3) * 3072 + (i % 3) * 32 + 768);
        SBAR();
#pragma unroll
        for (int i = 0; i < 12; ++i) cs[i % 3] = MFMA32(a[i], ones, cs[i % 3]);
        asm volatile("" : "+v"(cs[0]), "+v"(cs[1]), "+v"(cs[2]));
    }
    STAMP(36);
    const u4 rsWd = mk_rsrc(wd + fb * 128u, 1376u * 1024u - fb * 128u);
    if (t32 == 0) {
#pragma unroll
        for (int u = 0; u < 3; ++u)
#pragma unroll
            for (int qd = 0; qd < 4; ++qd) sts16f(L_CSP + w * 384 + u * 128 + qd * 32 + h * 16, (f4){cs[u][4 * qd], cs[u][4 * qd + 1], cs[u][4 * qd + 2], cs[u][4 * qd + 3]});
    }
    if (w == 7 && UNLIKELY(any_lane(ck[0] != tag[0] || ck[1] != tag[1]))) POLL(vc2);
    BAR_VM(0);
    STAMP(6);
    const unsigned d2l = hh * 6144 + j * 3072, d2s = (352 * hh + 112 + 24 * j) * 1024;
#define WD2(R, i) dma_bufi<(R) * 24576 + (i) * 1024, (R) * 49152 + (i) * 8192>(d2l, vrow, rsWd, d2s)
    {
        const unsigned sH = WS_H + (8 * g + w) * 32768u, sS = WS_ST + (8 * g + w) * 2048u;
#define PD4 6
        constexpr int PD = PD4;
        u4 Hb[32], A0[2], A1[2];
        u2 st[4];
        f16 ag[3] = {};
        const unsigned cf = L_CF + w * 384, vstat = h * 1024 + t32 * 8;
#pragma unroll
        for (int i = 0; i < PD; ++i) ld16(Hb[kgo(i)], l16, rsF, sH + kgo(i) * 1024);
        if (l < 48) { f2 s = {0.f, 0.f};
#pragma unroll
            for (int i = 0; i < 8; ++i) s += *(LAS const f2*)(L_CSP + i * 384 + l * 8);
            sts8f(cf + l * 8, s); }
        A0[0] = ldtr(ga, ga + 768); A1[0] = ldtr(ga + 32, ga + 800);
        unsigned mx = 0;
#define P4_STEP(i) do { constexpr int c = (i) & 1, n = c ^ 1, k = kgo(i); const unsigned o = ga + ((i) < 31 ? kgo((i) + 1) * 3072 : 64); \
            if ((i) + PD < 32) ld16(Hb[kgo((i) + PD < 32 ? (i) + PD : 0)], l16, rsF, sH + kgo((i) + PD) * 1024); \
            else if ((i) + PD < 36) ld8(st[((i) + PD - 32) & 3], vstat, rsF, sS + ((i) + PD - 32) * 256); \
            WAITV1((i) + PD < 36 ? PD : 35 - (i), Hb[k]); \
            if ((i) < 8) FRESH16(Hb[k], l16, rsF, sH + k * 1024); else { mx = max(mx, Hb[k][3]); asm volatile("" : "+v"(mx)); } if ((i) == 0) STAMP(7); SBAR(); \
            ag[0] = MFMA32(A0[c], Hb[k], ag[0]); A0[n] = ldtr(o, o + 768); SBAR(); \
            ag[1] = MFMA32(A1[c], Hb[k], ag[1]); if ((i) < 31) A1[n] = ldtr(o + 32, o + 800); SBAR(); } while (0)
#define P4_4(i) P4_STEP(i); P4_STEP((i) + 1); P4_STEP((i) + 2); P4_STEP((i) + 3)
        P4_4(0); P4_4(4); P4_4(8); P4_4(12); P4_4(16); P4_4(20); P4_4(24); P4_4(28);
        WAITV4(0, st[0], st[1], st[2], st[3]);
        if (UNLIKELY(any_lane(max(max(mx, max(st[0][1], st[1][1])), max(st[2][1], st[3][1])) == ~0u))) {
#pragma unroll
            for (int i = 0; i < 32; ++i) FRESH16(Hb[i], l16, rsF, sH + i * 1024);
#pragma unroll 1
            for (int s_ = 0; s_ < SPIN_CAP; ++s_) { NRETRY();
#pragma unroll
                for (int i = 0; i < 4; ++i) ld8(st[i], vstat, rsF, sS + i * 256);
                WAITV4(0, st[0], st[1], st[2], st[3]);
                if (!any_lane(max(max(st[0][1], st[1][1]), max(st[2][1], st[3][1])) == ~0u)) break;
            }
            ag[0] = z16; ag[1] = z16;
#pragma unroll
            for (int i = 0; i < 32; ++i) { const unsigned o = ga + kgo(i) * 3072; ag[0] = MFMA32(ldtr(o, o + 768), Hb[kgo(i)], ag[0]); ag[1] = MFMA32(ldtr(o + 32, o + 800), Hb[kgo(i)], ag[1]); }
        }
        STAMP(8);
        const float t1 = add_x32(fu(st[0].x) + fu(st[1].x) + fu(st[2].x) + fu(st[3].x)), t2 = add_x32(fu(st[0].y) + fu(st[1].y) + fu(st[2].y) + fu(st[3].y));
        float nm = t1 * (-1.f / 512), ve = __builtin_fmaxf(t2 * (1.f / 512) - nm * nm, 0.f) + 1e-5f;
        const float rs = __builtin_amdgcn_rsqf(ve);
        float ka = rs * -2.3022081986f, kb = rs * rs * rs * -0.1029432397f;
        asm volatile("" : "+v"(nm), "+v"(ve), "+v"(ka), "+v"(kb));
        const unsigned d1l = L_A + hh * 14336 + 7 * j * 1024, d1s = (352 * hh + 56 * j) * 1024;
#define WD1(i) dma_bufi<(i) * 1024, (i) * 8192>(d1l, vrow, rsWd, d1s)
        f4 cg, cu; f2 gt, gu, zz; u4 Gy;
#define GE_C(u, qd) do { cg = lds16f(cf + (u) * 128 + (qd) * 32 + h * 16); cu = lds16f(cf + (u) * 128 + (2 + (qd)) * 32 + h * 16); } while (0)
#define GE_0(u, pr) do { gt = (f2){ag[u][2 * (pr)], ag[u][2 * (pr) + 1]} + (((pr) & 1) ? hi2(cg) : lo2(cg)) * nm; gu = (f2){ag[u][8 + 2 * (pr)], ag[u][9 + 2 * (pr)]} + (((pr) & 1) ? hi2(cu) : lo2(cu)) * nm; \
            zz = gt * gt * kb + ka; asm volatile("" : "+v"(gt), "+v"(gu), "+v"(zz)); } while (0)
#define GE_1() do { zz = gt * zz; gu = gt * gu; zz = (f2){__builtin_amdgcn_exp2f(zz[0]), __builtin_amdgcn_exp2f(zz[1])}; asm volatile("" : "+v"(gu), "+v"(zz)); } while (0)
#define GE_2() do { zz = zz * ve + ve; zz = (f2){__builtin_amdgcn_rcpf(zz[0]), __builtin_amdgcn_rcpf(zz[1])}; asm volatile("" : "+v"(zz)); } while (0)
#define GE_3(pr) do { unsigned y_ = pk2(gu * zz); asm volatile("" : "+v"(y_)); Gy[pr] = y_; } while (0)
        GE_C(0, 0);
#define P4B_STEP(i) do { constexpr int c = (i) & 1, n = c ^ 1, p = (i) >> 2, u = p >> 2, pr = p & 3; const unsigned o = ga + ((i) + 1) * 3072 + 64; \
            if ((i) == 16) BAR_L(); SBAR(); \
            ag[2] = MFMA32(A0[c], Hb[i], ag[2]); if ((i) < 31) A0[n] = ldtr(o, o + 768); SBAR(); \
            if (((i) & 3) == 0) GE_0(u, pr); if (((i) & 3) == 1) { GE_1(); if ((pr & 1) && p < 7) GE_C((p + 1) >> 2, (((p + 1) >> 1) & 1)); } if (((i) & 3) == 2) GE_2(); \
            if (((i) & 3) == 3) { GE_3(pr); if (pr == 3) st16(Gy, l16, rsF, SG(u)); } \
            if ((i) < 14 && !((i) & 1)) WD1(((i) >> 1) & 7); \
            if ((i) >= 17 && (i) < 29 && ((i) & 1)) WD2((((i) + 7) >> 1) / 3 & 1, (((i) + 7) >> 1) % 3); SBAR(); } while (0)
#define P4B_4(i) P4B_STEP(i); P4B_STEP((i) + 1); P4B_STEP((i) + 2); P4B_STEP((i) + 3)
        P4B_4(0); P4B_4(4); P4B_4(8); P4B_4(12); P4B_4(16); P4B_4(20); P4B_4(24); P4B_4(28);
        STAMP(41);
#pragma unroll
        for (int pr = 0; pr < 4; ++pr) {
            if (!(pr & 1)) GE_C(2, pr >> 1);
            const f2 a = (f2){ag[2][2 * pr], ag[2][2 * pr + 1]} + ((pr & 1) ? hi2(cg) : lo2(cg)) * nm, c = (f2){ag[2][8 + 2 * pr], ag[2][9 + 2 * pr]} + ((pr & 1) ? hi2(cu) : lo2(cu)) * nm;
            const f2 z = a * (a * a * kb + ka), d = (f2){__builtin_amdgcn_exp2f(z[0]), __builtin_amdgcn_exp2f(z[1])} * ve + ve;
            Gy[pr] = pk2(a * c * (f2){__builtin_amdgcn_rcpf(d[0]), __builtin_amdgcn_rcpf(d[1])});
        }
        st16(Gy, l16, rsF, SG(2));
        STAMP(30);
    }
    BAR_VM(9);
    STAMP(9);
    {
        u4 D0[7], D1[7];
#pragma unroll
        for (int i = 0; i < 7; ++i) { const unsigned o = L_A + hh * 14336 + i * 2048; D0[i] = ldtr(o + ta0, o + ta0 + 1024); D1[i] = ldtr(o + ta1, o + ta1 + 1024); }
        WD2(2, 0); WD2(2, 1); WD2(2, 2); WD2(3, 0); WD2(3, 1); WD2(3, 2);
        const unsigned d3l = L_A + hh * 6144 + 3 * j * 1024, d3s = (352 * hh + 304 + 24 * j) * 1024;
#define WD3(i) dma_bufi<(i) * 1024, (i) * 8192>(d3l, vrow, rsWd, d3s)
        BAR_VM(6);
        STAMP(29);
        if (w == 4) FLAG(WS_F3 + (g * 32 + r) * 128);
#define PSLEEP 6
        if (w == 7) { const unsigned vc = WS_F3 + (xc * 32 + t32) * 128; __builtin_amdgcn_s_sleep(PSLEEP); POLL(vc); }
        if (w == 6 && b == 0) {
#pragma unroll 1
            for (unsigned i = 0; i < 4; ++i) { const unsigned vc = WS_F1 + (64 * i + l) * 128; POLL(vc); }
            FLAG(0u);
        }
        BAR_L();
        STAMP(46);
        constexpr int PD = 6;
        u4 Gb[22], A0[2], A1[2];
        const unsigned gs = WS_G + (2 * U64 + j) * 88064u + 22 * hh * 1024, gz = hh == 3 ? OOB : gs, o0 = hh * 6144 + ta0, o1 = hh * 6144 + ta1, p0 = L_A + hh * 6144 + ta0, p1 = L_A + hh * 6144 + ta1;
#pragma unroll
        for (int i = 0; i < PD; ++i) ld16(Gb[i], l16, rsF, gs + i * 1024);
        WD3(0); WD3(1); WD3(2);
        acc0 = z16; acc1 = z16;
#define P5_STEP(i) do { constexpr int c = (i) & 1, n = c ^ 1; \
            if ((i) + PD < 22) ld16(Gb[(i) + PD < 22 ? (i) + PD : 0], l16, rsF, ((i) + PD >= 20 ? gz : gs) + ((i) + PD) * 1024); \
            WAITV1(((i) + PD < 22 ? PD : 21 - (i)) + ((i) < PD ? 3 : 0), Gb[i]); if ((i) == 0) STAMP(10); \
            if ((i) == 6) BAR_L(); SBAR(); \
            constexpr int m_ = (i) >= 6 ? (i) - 6 : 0; constexpr unsigned q_ = (m_ / 3) * 24576 + (m_ % 3) * 2048; \
            const unsigned a0_ = (i) < 18 ? o0 + q_ : p0 + ((i) - 18) * 2048, a1_ = (i) < 18 ? o1 + q_ : p1 + ((i) - 18) * 2048; \
            acc0 = MFMA32((i) < 7 ? D0[(i) < 7 ? (i) : 0] : A0[c], Gb[i], acc0); if ((i) >= 6 && (i) < 21) A0[n] = ldtr(a0_, a0_ + 1024); SBAR(); \
            acc1 = MFMA32((i) < 7 ? D1[(i) < 7 ? (i) : 0] : A1[c], Gb[i], acc1); if ((i) >= 6 && (i) < 21) A1[n] = ldtr(a1_, a1_ + 1024); SBAR(); } while (0)
        P5_STEP(0); P5_STEP(1); P5_STEP(2); P5_STEP(3); P5_STEP(4); P5_STEP(5); P5_STEP(6); P5_STEP(7); P5_STEP(8); P5_STEP(9); P5_STEP(10);
        P5_STEP(11); P5_STEP(12); P5_STEP(13); P5_STEP(14); P5_STEP(15); P5_STEP(16); P5_STEP(17); P5_STEP(18); P5_STEP(19); P5_STEP(20); P5_STEP(21);
        }
    STAMP(11);
    BAR_L();
    {
        f4 lo, hi; reduce(lo, hi);
        const u4 Ores = {pk2(lo2(lo) + (f2){blo(Hres[0]), bhi(Hres[0])}), pk2(hi2(lo) + (f2){blo(Hres[1]), bhi(Hres[1])}), pk2(lo2(hi) + (f2){blo(Hres[2]), bhi(Hres[2])}), pk2(hi2(hi) + (f2){blo(Hres[3]), bhi(Hres[3])})};
        st16_plain(Ores, vres, mk_rsrc(out + U64 * 65536u, 65536u), 0);
    }
    asm volatile(".Lfb_code1:");
    STAMP(12);
#endif
}

#define WSOFF 0
extern "C" void kernel_launch(void* const* d_in, const int* in_sizes, int n_in, void* d_out, int out_size, void* d_ws, size_t ws_size, hipStream_t stream) {
    (void)in_sizes; (void)n_in; (void)out_size; (void)ws_size;
    (void)hipFuncSetAttribute((const void*)fused_block, hipFuncAttributeMaxDynamicSharedMemorySize, LDS_BYTES);
    fused_block<<<256, 512, LDS_BYTES, stream>>>((const char*)d_in[0], (const char*)d_in[1], (const char*)d_in[2], (const char*)d_in[4], (const char*)d_in[5], (const char*)d_in[6],
                                                 (char*)d_out, (char*)d_ws + WSOFF);
}
#define LASTSTAMP 12
#define STAMP_NAMES "start", "P1 loop end", "KV drained", 0, "O first data", "O drained", "h drained", "h first data", "P4 pass1 end", "P4 end bar", "G first data", "P5 loop end", "end", 0, 0, 0, \
  "st0", "st1", "st2", "st3", "st4", "st5", "st6", "st7", "KV first data", "att done", "O stored", "P3 loop end", "P3 reduced", "G drained", "G stored", 0, "KV stored", "h stored", \
  "LN sums", "pre KV bar", "colsum done", "A read", 0, "setup done", "GU bar", "P4 pass2 end", "att blk3", "att blk4", "att blk0", "comb bar", "G flags"
```

```cpp
#include <hip/hip_runtime.h>
#include <hip/hip_bf16.h>
#include <stdint.h>
#pragma clang diagnostic ignored "-Wint-to-pointer-cast"

typedef __bf16 bf8 __attribute__((ext_vector_type(8)));
typedef __bf16 bf2 __attribute__((ext_vector_type(2)));
typedef short s4 __attribute__((ext_vector_type(4)));
typedef short s8 __attribute__((ext_vector_type(8)));
typedef float f2 __attribute__((ext_vector_type(2)));
typedef float f4 __attribute__((ext_vector_type(4)));
typedef float f16 __attribute__((ext_vector_type(16)));
typedef unsigned u2 __attribute__((ext_vector_type(2)));
typedef unsigned u4 __attribute__((ext_vector_type(4)));
typedef unsigned long long u64;
#define LAS __attribute__((address_space(3)))
#define DEV __device__ __forceinline__

constexpr unsigned WS_F1 = 32768, WS_F2 = 65536, WS_F3 = 98304;
constexpr unsigned WS_KV = 1u << 20;
constexpr unsigned WS_O = 5u << 20;
constexpr unsigned WS_H = 7u << 20;
constexpr unsigned WS_ST = 9u << 20;
constexpr unsigned WS_G = 10u << 20;
constexpr unsigned WS_DIAG = 32u << 20;
constexpr unsigned LDS_BYTES = 163840;
constexpr unsigned L_HOLE = 73728;
constexpr unsigned L_OC = L_HOLE;
constexpr unsigned L_A = 98304;
constexpr unsigned L_CSP = 155648, L_CF = L_CSP + 3072;
constexpr int SPIN_CAP = 1 << 14;
constexpr unsigned OOB = 0x40000000u;

#define MFMA16(a, b, c) __builtin_amdgcn_mfma_f32_16x16x32_bf16(__builtin_bit_cast(bf8, a), __builtin_bit_cast(bf8, b), c, 0, 0, 0)
#define MFMA32(a, b, c) __builtin_amdgcn_mfma_f32_32x32x16_bf16(__builtin_bit_cast(bf8, a), __builtin_bit_cast(bf8, b), c, 0, 0, 0)
#define SBAR() __builtin_amdgcn_sched_barrier(0)
#define BAR_VM(N) asm volatile("s_waitcnt vmcnt(%0) lgkmcnt(0)\n\ts_barrier" ::"i"(N) : "memory")
#define BAR_L() asm volatile("s_waitcnt lgkmcnt(0)\n\ts_barrier" ::: "memory")

DEV u4 mk_rsrc(const void* p, unsigned bytes) { const u64 a = (u64)p; return (u4){(unsigned)a, (unsigned)(a >> 32) & 0xffffu, bytes, 0x00020000u}; }
DEV void dma_buf(unsigned lds, unsigned voff, u4 rs, unsigned soff) {
    asm volatile("s_mov_b32 m0, %0\n\ts_nop 0\n\tbuffer_load_dwordx4 %1, %2, %3 offen lds" ::"s"(lds), "v"(voff), "s"(rs), "s"(soff) : "memory"); }
template <unsigned LI, unsigned SI> DEV void dma_bufi(unsigned lds, unsigned voff, u4 rs, unsigned soff) {
    unsigned t; asm volatile("s_add_u32 m0, %1, %5\n\ts_add_u32 %0, %4, %6\n\tbuffer_load_dwordx4 %2, %3, %0 offen lds" : "=&s"(t) : "s"(lds), "v"(voff), "s"(rs), "s"(soff), "i"(LI), "i"(SI) : "memory", "scc"); }
template <unsigned LI> DEV void dma_glbi(unsigned lds, const char* p) {
    asm volatile("s_add_u32 m0, %0, %2\n\ts_nop 0\n\tglobal_load_lds_dwordx4 %1, off" ::"s"(lds), "v"(p), "i"(LI) : "memory", "scc"); }
DEV void dma_glb(unsigned lds, const char* p) {
    asm volatile("s_mov_b32 m0, %0\n\ts_nop 0\n\tglobal_load_lds_dwordx4 %1, off" ::"s"(lds), "v"(p) : "memory"); }
DEV void ld16(u4& d, unsigned voff, u4 rs, unsigned soff) {
    asm volatile("buffer_load_dwordx4 %0, %1, %2, %3 offen sc1" : "=v"(d) : "v"(voff), "s"(rs), "s"(soff) : "memory"); }
DEV void ld16_plain(u4& d, unsigned voff, u4 rs, unsigned soff) {
    asm volatile("buffer_load_dwordx4 %0, %1, %2, %3 offen" : "=v"(d) : "v"(voff), "s"(rs), "s"(soff) : "memory"); }
template <int OFF> DEV void ld16_imm(u4& d, unsigned voff, u4 rs) {
    asm volatile("buffer_load_dwordx4 %0, %1, %2, 0 offen offset:%3" : "=v"(d) : "v"(voff), "s"(rs), "i"(OFF) : "memory"); }
DEV void ld8(u2& d, unsigned voff, u4 rs, unsigned soff) {
    asm volatile("buffer_load_dwordx2 %0, %1, %2, %3 offen sc1" : "=v"(d) : "v"(voff), "s"(rs), "s"(soff) : "memory"); }
DEV void ld16_v(u4& d0, u4& d1, unsigned voff, u4 rs, unsigned soff) {
    asm volatile("s_mov_b64 exec, %5\n\tbuffer_load_dwordx4 %0, %2, %3, %4 offen offset:1024 sc1\n\tbuffer_load_dwordx4 %1, %2, %3, %4 offen offset:1536 sc1\n\ts_mov_b64 exec, -1"
                 : "+v"(d0), "+v"(d1) : "v"(voff), "s"(rs), "s"(soff), "s"(0x0000FFFF0000FFFFull) : "memory"); }
DEV void st16(u4 d, unsigned voff, u4 rs, unsigned soff) {
    asm volatile("buffer_store_dwordx4 %0, %1, %2, %3 offen sc1\n\ts_nop 1" ::"v"(d), "v"(voff), "s"(rs), "s"(soff) : "memory"); }
DEV void st16_plain(u4 d, unsigned voff, u4 rs, unsigned soff) {
    asm volatile("buffer_store_dwordx4 %0, %1, %2, %3 offen\n\ts_nop 1" ::"v"(d), "v"(voff), "s"(rs), "s"(soff) : "memory"); }
DEV void st8(u2 d, unsigned voff, u4 rs, unsigned soff) {
    asm volatile("buffer_store_dwordx2 %0, %1, %2, %3 offen sc1\n\ts_nop 1" ::"v"(d), "v"(voff), "s"(rs), "s"(soff) : "memory"); }
#define WAITV1(N, a) asm volatile("s_waitcnt vmcnt(%1)" : "+v"(a) : "i"(N) : "memory")
#define WAITV1D(ND, NS, a) asm volatile("s_waitcnt vmcnt(%1)\n\ts_cmp_lt_u32 %3, 4\n\ts_cbranch_scc1 .Lw%=\n\ts_waitcnt vmcnt(%2)\n.Lw%=:" : "+v"(a) : "i"(ND), "i"(NS), "s"(w) : "memory", "scc")
#define WAITV3(N, a, b, c) asm volatile("s_waitcnt vmcnt(%3)" : "+v"(a), "+v"(b), "+v"(c) : "i"(N) : "memory")
#define WAITV4(N, a, b, c, d) asm volatile("s_waitcnt vmcnt(%4)" : "+v"(a), "+v"(b), "+v"(c), "+v"(d) : "i"(N) : "memory")

DEV u4 lds16(unsigned a) { return *(LAS const u4*)a; }
DEV f4 lds16f(unsigned a) { return *(LAS const f4*)a; }
DEV float lds4f(unsigned a) { return *(LAS const float*)a; }
DEV void sts16(unsigned a, u4 v) { *(LAS u4*)a = v; }
DEV void sts16f(unsigned a, f4 v) { *(LAS f4*)a = v; }
DEV void sts8f(unsigned a, f2 v) { *(LAS f2*)a = v; }
DEV void sts4f(unsigned a, float v) { *(LAS float*)a = v; }
DEV u4 ldtr(unsigned a0, unsigned a1) {
    const s4 lo = __builtin_amdgcn_ds_read_tr16_b64_v4i16((LAS s4*)a0), hi = __builtin_amdgcn_ds_read_tr16_b64_v4i16((LAS s4*)a1);
    return __builtin_bit_cast(u4, __builtin_shufflevector(lo, hi, 0, 1, 2, 3, 4, 5, 6, 7)); }
DEV unsigned pk2(f2 a) { return __builtin_bit_cast(unsigned, __builtin_convertvector(a, bf2)); }
DEV f2 lo2(f4 a) { return (f2){a[0], a[1]}; }
DEV f2 hi2(f4 a) { return (f2){a[2], a[3]}; }
DEV unsigned pk(float a, float b) { return __builtin_bit_cast(unsigned, __builtin_convertvector((f2){a, b}, bf2)); }
DEV float blo(unsigned v) { return __builtin_bit_cast(float, v << 16); }
DEV float bhi(unsigned v) { return __builtin_bit_cast(float, v & 0xffff0000u); }
DEV float fu(unsigned v) { return __builtin_bit_cast(float, v); }
DEV unsigned uf(float v) { return __builtin_bit_cast(unsigned, v); }
DEV float add_x32(float v) { auto r = __builtin_amdgcn_permlane32_swap(uf(v), uf(v), false, false); const unsigned a = r[0], b = r[1]; return fu(a) + fu(b); }
DEV float add_x16(float v) { auto r = __builtin_amdgcn_permlane16_swap(uf(v), uf(v), false, false); const unsigned a = r[0], b = r[1]; return fu(a) + fu(b); }
DEV float max_x32(float v) { auto r = __builtin_amdgcn_permlane32_swap(uf(v), uf(v), false, false); const unsigned a = r[0], b = r[1]; return __builtin_fmaxf(fu(a), fu(b)); }
template <int C> DEV float add_dpp(float v) { return v + fu(__builtin_amdgcn_update_dpp(0u, uf(v), C, 0xf, 0xf, true)); }
DEV float add_x8(float v) { return v + fu(__builtin_amdgcn_update_dpp(0u, uf(v), 0x128, 0xf, 0xf, true)); }
constexpr u64 diag_mask(int r) { u64 m = 0; for (int l = 0; l < 64; ++l) { const int key = 16 * ((r >> 2) & 1) + 8 * (l >> 5) + 4 * (r >> 3) + (r & 3); if (key <= (l & 31)) m |= 1ull << l; } return m; }
DEV float sel(u64 m, float t, float f) { return __builtin_amdgcn_inverse_ballot_w64(m) ? t : f; }

template <unsigned LDS> DEV void touch(unsigned voff, u4 rs, unsigned soff) {
    asm volatile("s_mov_b32 m0, %3\n\ts_nop 0\n\tbuffer_load_dword %0, %1, %2 offen lds" ::"v"(voff), "s"(rs), "s"(soff), "i"(LDS) : "memory"); }
typedef short sh2 __attribute__((ext_vector_type(2)));
constexpr int kgo(int i) { return i < 8 ? 4 * i : (i - 8) / 3 * 4 + (i - 8) % 3 + 1; }
DEV bool any_lane(bool c) { return __builtin_amdgcn_ballot_w64(c) != 0; }
#define UNLIKELY(x) __builtin_expect(!!(x), 0)
#define NRETRY()
#define FRESH16(d, voff, rs, soff) do { if (UNLIKELY(any_lane((d)[3] == ~0u))) { \
        _Pragma("unroll 1") for (int s_ = 0; s_ < SPIN_CAP; ++s_) { NRETRY(); u4 t_; asm volatile("buffer_load_dwordx4 %0, %1, %2, %3 offen sc1\n\ts_waitcnt vmcnt(0)" : "=&v"(t_) : "v"(voff), "s"(rs), "s"(soff) : "memory"); \
            d = t_; if (!any_lane(t_[3] == ~0u)) break; __builtin_amdgcn_s_sleep(2); } } } while (0)
#define POLL(voff) do { \
        _Pragma("unroll 1") for (int s_ = 0; s_ < 4 * SPIN_CAP; ++s_) { u2 t_; asm volatile("buffer_load_dwordx2 %0, %1, %2, 0 offen sc1\n\ts_waitcnt vmcnt(0)" : "=&v"(t_) : "v"(voff), "s"(rsF) : "memory"); \
            if (!any_lane(t_[0] != tag[0] || t_[1] != tag[1])) break; NRETRY(); __builtin_amdgcn_s_sleep(1); } } while (0)
#define FLAG(off) st8(tag, l == 0 ? (off) : OOB, rsF, 0)
#define STAMP(i)

__global__ __launch_bounds__(512, 2) void fused_block(const char* __restrict__ x, const char* __restrict__ wqkv, const char* __restrict__ wo, const char* __restrict__ wg,
                                                      const char* __restrict__ wu, const char* __restrict__ wd, char* __restrict__ out, char* __restrict__ ws) {
#if defined(__HIP_DEVICE_COMPILE__)
    const unsigned l = threadIdx.x & 63, w = __builtin_amdgcn_readfirstlane(threadIdx.x >> 6);
    const unsigned b = blockIdx.x, xc = b & 7, rk = b >> 3;
    const unsigned T64 = 8 * (xc >> 1) + (rk >> 2), hq = 4 * (xc & 1) + (rk & 3);
    u64 pt0; asm volatile("s_memrealtime %0\n\ts_waitcnt lgkmcnt(0)" : "=s"(pt0)::"memory");
    STAMP(0);
    const unsigned u8 = l >> 3, l7 = l & 7;
    u4 xr[8];
    const u4 rsX = mk_rsrc(x + T64 * 65536u, 65536u);
    const unsigned vx = (8 * w + u8) * 1024 + l7 * 16;
    {
        const char* pc; unsigned nb; asm volatile("s_getpc_b64 %0\n.Lfb_code0:\n\ts_mov_b32 %1, .Lfb_code1-.Lfb_code0" : "=s"(pc), "=s"(nb));
        const char* p = l < 6 ? wqkv + ((b * 8 + w) * 6 + l) * 128 : pc + (((rk * 8 + w) * 2 + (l & 1)) * 128 < nb ? ((rk * 8 + w) * 2 + (l & 1)) * 128 : 0);
        asm volatile("s_mov_b32 m0, %1\n\ts_nop 0\n\tglobal_load_lds_dword %0, off" ::"v"(p), "s"(L_A + w * 8192) : "memory"); }
    ld16_imm<0>(xr[0], vx, rsX); ld16_imm<128>(xr[1], vx, rsX); ld16_imm<256>(xr[2], vx, rsX); ld16_imm<384>(xr[3], vx, rsX);
#define XB 6
    if (XB > 4) { ld16_imm<512>(xr[4], vx, rsX); ld16_imm<640>(xr[5], vx, rsX); }
    if (XB > 6) { ld16_imm<768>(xr[6], vx, rsX); ld16_imm<896>(xr[7], vx, rsX); }
    const u4 rsW = mk_rsrc(wqkv + hq * 128u, 512u * 3072u - hq * 128u);
    SBAR();
    const unsigned w3 = w & 3, wk2 = w3 * 2048;
    unsigned vw0 = (16 * w3 + u8) * 3072 + 32 * ((l7 >> 1) ^ ((l >> 4) & 1)) + 16 * (l7 & 1), vw1 = (vw0 + 24576) ^ 64;
#define SLOT(i) ((((i) + 1) & 3) * 24576u)
#define SP(i, p) dma_bufi<SLOT(i) + ((p) >> 1) * 8192 + ((p) & 1) * 1024, (i) * 196608u + ((p) >> 1) * 1024u>(wk2, ((p) & 1) ? vw1 : vw0, rsW, 0)
    const unsigned U64 = 4 * xc + (rk >> 3), fb = rk & 7;
    const unsigned g = 2 * (xc >> 1) + (rk >> 4), r = 16 * (xc & 1) + (rk & 15);
    const unsigned j = w & 1, hh = w >> 1;
    const unsigned t16 = l & 15, g4 = l >> 4, h = l >> 5, t32 = l & 31, q4 = t16 >> 2, pp = l & 3, l16 = l * 16, l128 = l * 128;
    const u4 rsF = mk_rsrc(ws, 16u << 20);
    const u4 ones = {0x3f803f80u, 0x3f803f80u, 0x3f803f80u, 0x3f803f80u};
    const unsigned kvb = WS_KV + (4 * hq + hh) * 131072u;
    const unsigned skv = kvb + (2 * T64 + j) * 2048;
    const unsigned sOw = WS_O + (2 * T64 + j) * 32768u + (4 * hq + hh) * 1024;
    u2 tag; u4 xres;
    unsigned vres = (32 * j + 8 * hh + l7) * 1024 + fb * 128 + u8 * 16;
    unsigned xw0 = L_A + (8 * w + u8) * 1024 + 16 * (l7 ^ (8 * (w & 1) + u8)), xw1 = xw0 ^ 128;
    unsigned xa[4];
#pragma unroll
    for (int q = 0; q < 4; ++q) { xa[q] = L_A + (32 * j + t16) * 1024 + 16 * ((4 * q + g4) ^ t16); asm volatile("" : "+v"(xa[q])); }
    unsigned wa = (8 * g4 + q4) * 128 + 32 * (hh ^ ((q4 >> 1) | ((g4 & 1) << 1))) + 8 * pp;
    f4 aq[2] = {}, ak[2] = {}, av[2] = {};
    unsigned vk = (h * 32 + (16 * ((t32 >> 3) & 1) + 8 * ((t32 >> 2) & 1) + 4 * (t32 >> 4) + (t32 & 3))) * 16, vv_ = (h * 16 + t16) * 16;
    unsigned vst = 1024 + ((g4 & 1) * 32 + (g4 >> 1) * 16 + t16) * 16;
    unsigned vrow = u8 * 1024 + 16 * (l7 ^ (((l >> 4) & 1) << 2));
    const char* gp[3];
#pragma unroll
    for (int i = 0; i < 3; ++i) { const unsigned z = 64 * i + l, k = (z * 43691u) >> 19, c = z - 12 * k, m = c >= 6, cc = c - 6 * m; unsigned un = 3 * r + (cc >> 1); un = un > 85 ? 85 : un;
        gp[i] = (m ? wu : wg) + ((16 * w + k) * 2752 + un * 32 + (cc & 1) * 16); asm volatile("" : "+v"(gp[i])); }
    const unsigned wk3 = w * 3072;
#define GU(R, c) dma_glbi<(R) * 24576 + (c) * 1024>(wk3, gp[c] + (R) * 128 * 2752)
#define GUD(R, q) dma_glbi<(R) * 24576 + ((q) / 3) * 12288 + ((q) % 3) * 1024>(wk3, gp[(q) % 3] + ((R) * 128 + ((q) / 3) * 64) * 2752)
    asm volatile("" : "+v"(vw0), "+v"(vw1), "+v"(xw0), "+v"(xw1), "+v"(wa), "+v"(vres), "+v"(vk), "+v"(vv_), "+v"(vrow), "+v"(vst));
    if (threadIdx.x == 0) __hip_atomic_store((u64*)(ws + (20u << 20)) + b, pt0, __ATOMIC_RELAXED, __HIP_MEMORY_SCOPE_AGENT);
    asm volatile("s_waitcnt vmcnt(0)");
    STAMP(39);
    if (XB <= 4) { ld16_imm<512>(xr[4], vx, rsX); ld16_imm<640>(xr[5], vx, rsX); }
    if (XB <= 6) { ld16_imm<768>(xr[6], vx, rsX); ld16_imm<896>(xr[7], vx, rsX); }
#define NO ((void)0)
#define TBPOS 0
#define TOUCH_B() do { const char* tp = (w == 4 ? wo : w == 5 ? wg : w == 6 ? wu : wd) + (w == 4 ? b * 16 + (l & 15) : b * 43 + (l < 43 ? l : 0)) * 128; \
        asm volatile("global_load_dword v255, %0, off" ::"v"(tp) : "memory", "v255"); } while (0)
#if !defined(NOTB) && TBPOS == 0
    if (w >= 4) TOUCH_B();
#define XW 1
#else
#define XW 0
#endif
#define SSTAMP(i)
    {
        f2 xf[32], s1 = {0.f, 0.f}, s2 = {0.f, 0.f};
#define LN_SUM(i, N, D0, D1) do { if (XW && (N) < 8 - (i)) WAITV1(N, xr[i]); else WAITV1D(N, 7 - (i) + XW, xr[i]); _Pragma("unroll") for (int e = 0; e < 4; ++e) { const unsigned d_ = xr[i][e]; f2 v_ = {blo(d_), bhi(d_)}; asm volatile("" : "+v"(v_)); xf[4 * (i) + e] = v_; \
        s1 += v_; s2 += v_ * v_; } asm volatile("" : "+v"(s1), "+v"(s2)); if (w < 4) { D0; D1; } SBAR(); } while (0)
        LN_SUM(0, 7, NO, NO); LN_SUM(1, 6, NO, NO); LN_SUM(2, 5, NO, NO); LN_SUM(3, 4, NO, NO);
        LN_SUM(4, 3, SP(0, 0), SP(0, 1)); LN_SUM(5, 4, SP(0, 2), SP(0, 3)); LN_SUM(6, 5, SP(0, 4), NO); LN_SUM(7, 5, SP(0, 5), NO);
        SSTAMP(34);
        if (w < 4) { SP(1, 0); SP(1, 1); SP(1, 2); SP(1, 3); SP(1, 4); SP(1, 5); }
#if !defined(NOTB) && TBPOS == 1
        else TOUCH_B();
#endif
        const float t1 = add_dpp<0x141>(add_dpp<0x4e>(add_dpp<0xb1>(s1[0] + s1[1]))), t2 = add_dpp<0x141>(add_dpp<0x4e>(add_dpp<0xb1>(s2[0] + s2[1])));
        const float mu = t1 * (1.f / 512), rs = __builtin_amdgcn_rsqf(__builtin_fmaxf(t2 * (1.f / 512) - mu * mu, 0.f) + 1e-5f), mr = -mu * rs;
#pragma unroll
        for (int i = 0; i < 8; ++i) sts16(((i & 1) ? xw1 : xw0) + (i >> 1) * 256, (u4){pk2(xf[4 * i] * rs + mr), pk2(xf[4 * i + 1] * rs + mr), pk2(xf[4 * i + 2] * rs + mr), pk2(xf[4 * i + 3] * rs + mr)});
    }
#define P1_STEP(n, D0, D1, D2) do { constexpr int c_ = (n) & 1, x_ = c_ ^ 1; constexpr bool m_ = (n) < 15; \
        const unsigned xb = xa[((n) + 1) & 3] + (((n) + 1) >> 2) * 256, wb = wa + SLOT(((n) + 1) >> 1) + x_ * 4096; \
        if (m_) X0[x_] = lds16(xb); SBAR(); \
        aq[0] = MFMA16(Wq[c_], X0[c_], aq[0]); if (m_) Wq[x_] = ldtr(wb, wb + 512); SBAR(); \
        aq[1] = MFMA16(Wq[c_], X1[c_], aq[1]); if (m_) X1[x_] = lds16(xb + 16384); D0; SBAR(); \
        ak[0] = MFMA16(Wk[c_], X0[c_], ak[0]); if (m_) Wk[x_] = ldtr(wb + 8192, wb + 8704); SBAR(); \
        ak[1] = MFMA16(Wk[c_], X1[c_], ak[1]); D1; SBAR(); \
        av[0] = MFMA16(X0[c_], Wv[c_], av[0]); if (m_) Wv[x_] = ldtr(wb + 16384, wb + 16896); SBAR(); \
        av[1] = MFMA16(X1[c_], Wv[c_], av[1]); D2; SBAR(); } while (0)
#define P1_FIRST() do { const unsigned wb = wa + SLOT(0); X0[0] = lds16(xa[0]); Wq[0] = ldtr(wb, wb + 512); X1[0] = lds16(xa[0] + 16384); Wk[0] = ldtr(wb + 8192, wb + 8704); Wv[0] = ldtr(wb + 16384, wb + 16896); } while (0)
#define XRES() ld16_plain(xres, vres, mk_rsrc(x + U64 * 65536u, 65536u), 0)
    if (w < 4) {
        u4 X0[2], X1[2], Wq[2], Wk[2], Wv[2];
        SP(2, 0); SP(2, 1); SP(2, 2); SP(2, 3); SP(2, 4); SP(2, 5);
        BAR_VM(12); STAMP(16);  P1_FIRST(); P1_STEP(0, (SP(3, 0), SP(3, 1)), (SP(3, 2), SP(3, 3)), (SP(3, 4), SP(3, 5)));
        BAR_VM(12); SSTAMP(17); P1_STEP(1, SP(4, 0), SP(4, 1), SP(4, 2)); P1_STEP(2, SP(4, 3), SP(4, 4), SP(4, 5));
        BAR_VM(12); SSTAMP(18); P1_STEP(3, SP(5, 0), SP(5, 1), SP(5, 2)); P1_STEP(4, SP(5, 3), SP(5, 4), SP(5, 5));
        BAR_VM(12); SSTAMP(19); P1_STEP(5, SP(6, 0), SP(6, 1), SP(6, 2)); P1_STEP(6, SP(6, 3), SP(6, 4), SP(6, 5));
        BAR_VM(12); SSTAMP(20); P1_STEP(7, SP(7, 0), SP(7, 1), SP(7, 2)); P1_STEP(8, SP(7, 3), SP(7, 4), SP(7, 5));
        BAR_VM(12); SSTAMP(21); P1_STEP(9, XRES(), GUD(1, 0), NO); P1_STEP(10, GUD(1, 1), GUD(1, 2), NO);
        BAR_VM(10); SSTAMP(22); P1_STEP(11, GUD(1, 3), GUD(1, 4), NO); P1_STEP(12, GUD(1, 5), NO, NO);
        BAR_VM(7);  SSTAMP(23); P1_STEP(13, NO, NO, NO); P1_STEP(14, NO, NO, NO); P1_STEP(15, NO, NO, NO);
        WAITV1(6, xres); tag = (u2){0u, 0u};
    } else {
        u4 X0[2], X1[2], Wq[2], Wk[2], Wv[2];
        const u4 bad = {~0u, ~0u, ~0u, ~0u};
#define MK(v, s_) st16(bad, v, rsF, s_)
#if !defined(NOTB) && TBPOS == 2
#define TCH(c) if (w == (c)) TOUCH_B()
#else
#define TCH(c) NO
#endif
        BAR_L(); STAMP(16);  P1_FIRST(); P1_STEP(0, ld8(tag, 0u * l, rsF, 0), MK(l16, skv), TCH(5));
        BAR_L(); SSTAMP(17); P1_STEP(1, NO, MK(l16 + 1024, skv), TCH(6)); P1_STEP(2, NO, MK(l16, skv - 262144), NO);
        BAR_L(); SSTAMP(18); P1_STEP(3, NO, MK(l16 + 1024, skv - 262144), TCH(4)); P1_STEP(4, NO, MK(l16, sOw), NO);
        BAR_L(); SSTAMP(19); P1_STEP(5, NO, MK(l16, sOw - 2048), TCH(7)); P1_STEP(6, XRES(), NO, NO);
        BAR_L(); SSTAMP(20); P1_STEP(7, NO, NO, NO); P1_STEP(8, NO, NO, NO);
        BAR_L(); SSTAMP(21); P1_STEP(9, NO, NO, NO); P1_STEP(10, NO, NO, NO);
        asm volatile("s_waitcnt vmcnt(0)" : "+v"(tag), "+v"(xres) :: "memory");
        BAR_L(); SSTAMP(22);
        { const unsigned lo = tag.x + 1u; tag.y += (lo == 0u); tag.x = lo; }
                             P1_STEP(11, FLAG(w == 4 ? WS_F1 + (T64 * 8 + hq) * 128 : OOB), NO, NO); P1_STEP(12, NO, NO, NO);
        BAR_L(); SSTAMP(23); P1_STEP(13, NO, NO, NO); P1_STEP(14, NO, NO, NO); P1_STEP(15, NO, NO, NO);
    }
    STAMP(1);
    u4 Qf;
    {
        { auto a = __builtin_amdgcn_permlane16_swap(pk2(lo2(ak[0])), pk2(lo2(ak[1])), false, false); auto c = __builtin_amdgcn_permlane16_swap(pk2(hi2(ak[0])), pk2(hi2(ak[1])), false, false);
          st16((u4){a[0], c[0], a[1], c[1]}, l16, rsF, skv); }
        st16((u4){pk2(lo2(av[0])), pk2(hi2(av[0])), pk2(lo2(av[1])), pk2(hi2(av[1]))}, vst, rsF, skv);
        STAMP(32);
        SBAR();
        constexpr float QS = 0.25f * 1.4426950408889634f;
        { auto a = __builtin_amdgcn_permlane16_swap(pk2(lo2(aq[0]) * QS), pk2(lo2(aq[1]) * QS), false, false); auto c = __builtin_amdgcn_permlane16_swap(pk2(hi2(aq[0]) * QS), pk2(hi2(aq[1]) * QS), false, false);
          Qf = (u4){a[0], c[0], a[1], c[1]}; }
        sts16(L_OC + (w ^ 1) * 2304 + l16, Qf);
    }
    const int Tb = 2 * (int)T64 - 8;
    u4 K[5], V0[5], V1[5];
    const int blk[5] = {Tb + 8 + (int)j, Tb + (int)j, Tb + 2 + 3 * (int)j, Tb + 3 + 3 * (int)j, Tb + 4 + 3 * (int)j};
#pragma unroll
    for (int i = 0; i < 5; ++i) { const unsigned o = blk[i] < 0 ? 0u : 0x3f803f80u; V0[i] = (u4){o, o, o, o}; V1[i] = V0[i]; asm volatile("" : "+v"(V0[i]), "+v"(V1[i])); }
    unsigned pm[8];
    { const short uq = (short)(t32 - 8 * h);
#pragma unroll
      for (int i = 0; i < 8; ++i) { const short c = (short)(16 * ((i >> 1) & 1) + 4 * (i >> 2) + 2 * (i & 1)); pm[i] = __builtin_bit_cast(unsigned, ((sh2){uq, uq} - (sh2){c, (short)(c + 1)}) >> (sh2){15, 15}); asm volatile("" : "+v"(pm[i])); } }
    unsigned so[5];
#pragma unroll
    for (int i = 0; i < 5; ++i) so[i] = blk[i] < 0 ? OOB : kvb + (unsigned)blk[i] * 2048;
    if (w == 7) {
        const int Tp = (int)T64 - 1 - (int)(l & 3); const unsigned vc = WS_F1 + (l < 4 ? (Tp < 0 ? T64 : (unsigned)Tp) * 8 + hq : U64 * 8 + l7) * 128; POLL(vc); }
    STAMP(35);
    BAR_VM(0);
    STAMP(2);
#define KVLD(i) do { ld16(K[i], vk, rsF, so[i]); ld16_v(V0[i], V1[i], vv_, rsF, so[i]); } while (0)
#define KVOK(N, i) do { WAITV3(N, K[i], V0[i], V1[i]); \
        if (UNLIKELY(any_lane(max(max(K[i][3], V0[i][3]), V1[i][3]) == ~0u))) { \
            _Pragma("unroll 1") for (int s_ = 0; s_ < SPIN_CAP; ++s_) { NRETRY(); KVLD(i); WAITV3(0, K[i], V0[i], V1[i]); if (!any_lane(max(max(K[i][3], V0[i][3]), V1[i][3]) == ~0u)) break; __builtin_amdgcn_s_sleep(2); } } } while (0)
    KVLD(2); KVLD(3);
    const u4 Qm = Qf, Qo = lds16(L_OC + w * 2304 + l16);
    f16 Om = {}, Oo = {};
    const f16 z16 = {};
    auto pv_tile = [&](const f16& S, const u4& va, const u4& vb, f16& O) {
        float p[16];
#pragma unroll
        for (int e = 0; e < 16; ++e) p[e] = __builtin_amdgcn_exp2f(S[e]);
        const u4 P0 = {pk(p[0], p[1]), pk(p[2], p[3]), pk(p[4], p[5]), pk(p[6], p[7])}, P1 = {pk(p[8], p[9]), pk(p[10], p[11]), pk(p[12], p[13]), pk(p[14], p[15])};
        O = MFMA32(va, P0, O); O = MFMA32(vb, P1, O);
    };
    const u4 rsWo = mk_rsrc(wo + fb * 128u, 512u * 1024u - fb * 128u);
    const unsigned wol = L_A + w * 8192, wos = w * 65536;
    const unsigned vh = (u8 >> 1) * 1024 + ((u8 & 1) * 32 + 8 * hh + l7) * 16, vs = l < 8 ? (8 * hh + l) * 8 : OOB;
    const unsigned sHw = WS_H + (2 * U64 + j) * 32768u + fb * 4096, sSw = WS_ST + (2 * U64 + j) * 2048u + fb * 256;
#define WO_(i) dma_bufi<(i) * 1024, (i) * 8192>(wol, vrow, rsWo, wos)
#define PIN16(a) asm volatile("" : "+v"(a))
#define MEM(...) do { SBAR(); __VA_ARGS__; SBAR(); } while (0)
#define ATT2(i, M0, M1, M2) do { f16 Sa = MFMA32(K[i], Qm, z16), Sb = MFMA32(K[i], Qo, z16); MEM(M0); pv_tile(Sa, V0[i], V1[i], Om); PIN16(Om); MEM(M1); pv_tile(Sb, V0[i], V1[i], Oo); PIN16(Oo); MEM(M2); } while (0)
    KVOK(3, 2);
    STAMP(24);
    ATT2(2, KVLD(4), KVLD(0), KVLD(1));
    KVOK(9, 3);
    SSTAMP(42);
    ATT2(3, (WO_(0), WO_(1)), (WO_(2), WO_(3), WO_(4)), (WO_(5), WO_(6), WO_(7)));
    KVOK(14, 4);
    SSTAMP(43);
    { const u4 bad = {~0u, ~0u, ~0u, ~0u};
      ATT2(4, (st16(bad, vh, rsF, sHw), st8((u2){~0u, ~0u}, vs, rsF, sSw)), (GU(2, 0), GU(2, 1)), GU(2, 2)); }
    KVOK(16, 0);
    KVOK(13, 1);
    SSTAMP(44);
    if (j == 0) { const f16 Sb = MFMA32(K[0], Qo, z16); pv_tile(Sb, V0[0], V1[0], Oo); }
    else        { const f16 Sb = MFMA32(K[1], Qo, z16); pv_tile(Sb, V0[1], V1[1], Oo); }
    const unsigned oc = L_OC + w * 2304;
    {
        const f16 Sx = MFMA32(K[0], Qm, z16), Sy = MFMA32(K[1], Qm, z16);
        sts16f(oc + l16, (f4){Oo[0], Oo[1], Oo[2], Oo[3]}); sts16f(oc + 1024 + l16, (f4){Oo[4], Oo[5], Oo[6], Oo[7]}); sts4f(oc + 2048 + l * 4, Oo[8]);
        float p[16];
#pragma unroll
        for (int e = 0; e < 16; ++e) p[e] = __builtin_amdgcn_exp2f(sel(diag_mask(e), Sx[e], Sy[e]));
        unsigned P[8];
#pragma unroll
        for (int e = 0; e < 8; ++e) P[e] = pk(p[2 * e], p[2 * e + 1]);
        const u4 Y0 = {P[0] & pm[0], P[1] & pm[1], P[2] & pm[2], P[3] & pm[3]}, Y1 = {P[4] & pm[4], P[5] & pm[5], P[6] & pm[6], P[7] & pm[7]};
        const u4 X0_ = {P[0] ^ Y0[0], P[1] ^ Y0[1], P[2] ^ Y0[2], P[3] ^ Y0[3]}, X1_ = {P[4] ^ Y1[0], P[5] ^ Y1[1], P[6] ^ Y1[2], P[7] ^ Y1[3]};
        Om = MFMA32(V0[0], X0_, Om); Om = MFMA32(V1[0], X1_, Om); Om = MFMA32(V0[1], Y0, Om); Om = MFMA32(V1[1], Y1, Om);
    }
    SBAR();
    STAMP(25);
    BAR_VM(3);
    SSTAMP(45);
    const unsigned op = L_OC + (w ^ 1) * 2304;
    f4 oa = lds16f(op + l16) + (f4){Om[0], Om[1], Om[2], Om[3]}, ob = lds16f(op + 1024 + l16) + (f4){Om[4], Om[5], Om[6], Om[7]};
    float den = Om[8] + lds4f(op + 2048 + l * 4);
    if (UNLIKELY(any_lane(!(den >= 0x1p-60f && den <= 0x1p60f)))) {
        f16 O = {}; float m = -__builtin_inff();
#pragma unroll 1
        for (int kb = 8; kb >= 0; --kb) {
            const int T = 2 * (int)T64 + (int)j - 8 + kb;
            if (T < 0) break;
            u4 Ks, Va = ones, Vb = ones;
            ld16(Ks, vk, rsF, kvb + (unsigned)T * 2048); ld16_v(Va, Vb, vv_, rsF, kvb + (unsigned)T * 2048);
            WAITV3(0, Ks, Va, Vb);
            f16 S = MFMA32(Ks, Qm, z16);
            const u64 all = (kb != 0 && kb != 8) ? ~0ull : 0ull, inv = kb == 0 ? ~0ull : 0ull;
            float mx = m;
#pragma unroll
            for (int e = 0; e < 16; ++e) { S[e] = sel((diag_mask(e) ^ inv) | all, S[e], -__builtin_inff()); mx = __builtin_fmaxf(mx, S[e]); }
            mx = max_x32(mx);
            const float al = __builtin_amdgcn_exp2f(m - mx); m = mx;
#pragma unroll
            for (int e = 0; e < 16; ++e) { O[e] *= al; S[e] -= mx; }
            pv_tile(S, Va, Vb, O);
        }
        oa = (f4){O[0], O[1], O[2], O[3]}; ob = (f4){O[4], O[5], O[6], O[7]}; den = O[8];
    }
    const float rd = __builtin_amdgcn_rcpf(den);
    st16((u4){pk2(lo2(oa) * rd), pk2(hi2(oa) * rd), pk2(lo2(ob) * rd), pk2(hi2(ob) * rd)}, l16, rsF, sOw);
    if (w == 4) FLAG(WS_F2 + (U64 * 8 + fb) * 128);
    STAMP(26);
    const unsigned ta0 = (4 * h + q4) * 128 + 64 * (q4 >> 1) + 32 * (g4 & 1) + 8 * pp, ta1 = ta0 ^ 64;
    f16 acc0 = {}, acc1 = {};
    {
        const unsigned sO = WS_O + (2 * U64 + j) * 32768u + hh * 8192;
        const unsigned oa0 = L_A + hh * 16384 + ta0, oa1 = L_A + hh * 16384 + ta1;
        u4 A0[8], A1[8], B[8];
#pragma unroll
        for (int i = 0; i < 8; ++i) { A0[i] = ldtr(oa0 + i * 2048, oa0 + i * 2048 + 1024); A1[i] = ldtr(oa1 + i * 2048, oa1 + i * 2048 + 1024); }
        STAMP(37);
        if (w == 4) BAR_VM(1); else BAR_VM(0);
        STAMP(5);
#pragma unroll
        for (int i = 0; i < 8; ++i) ld16(B[i], l16, rsF, sO + i * 1024);
#define P3_PAIR(i, NW, M0, M1) do { asm volatile("s_waitcnt vmcnt(%2)" : "+v"(B[i]), "+v"(B[(i) + 1]) : "i"(NW) : "memory"); \
            if (UNLIKELY(any_lane(max(B[i][3], B[(i) + 1][3]) == ~0u))) { FRESH16(B[i], l16, rsF, sO + (i) * 1024); FRESH16(B[(i) + 1], l16, rsF, sO + ((i) + 1) * 1024); } if ((i) == 0) STAMP(4); \
            acc0 = MFMA32(A0[i], B[i], acc0); acc1 = MFMA32(A1[i], B[i], acc1); M0; SBAR(); acc0 = MFMA32(A0[(i) + 1], B[(i) + 1], acc0); acc1 = MFMA32(A1[(i) + 1], B[(i) + 1], acc1); M1; SBAR(); } while (0)
        P3_PAIR(0, 6, NO, NO); P3_PAIR(2, 4, GU(0, 0), GU(0, 1)); P3_PAIR(4, 4, GU(0, 2), GU(3, 0)); P3_PAIR(6, 4, GU(3, 1), GU(3, 2));
    }
    u2 ck; const unsigned vc2 = WS_F2 + (g * 32 + t32) * 128;
    ld8(ck, w == 7 ? vc2 : OOB, rsF, 0);
    STAMP(27);
    const unsigned rw0 = L_A + (hh * 2 + j) * 8192 + h * 512 + t32 * 16, rw1 = rw0 ^ 128, rr = L_A + j * 8192 + u8 * 1024 + (((8 * hh + l7) ^ ((u8 & 1) << 3)) * 16);
    auto reduce = [&](f4& lo, f4& hi) {
#pragma unroll
        for (int u = 0; u < 4; ++u) {
            sts16f(((u & 1) ? rw1 : rw0) + u * 1024, (f4){acc0[4 * u], acc0[4 * u + 1], acc0[4 * u + 2], acc0[4 * u + 3]});
            sts16f(((u & 1) ? rw1 : rw0) + (4 + u) * 1024, (f4){acc1[4 * u], acc1[4 * u + 1], acc1[4 * u + 2], acc1[4 * u + 3]});
        }
        BAR_L();
        lo = (lds16f(rr) + lds16f(rr + 16384)) + (lds16f(rr + 32768) + lds16f(rr + 49152));
        hi = (lds16f(rr + 512) + lds16f(rr + 16896)) + (lds16f(rr + 33280) + lds16f(rr + 49664));
    };
    u4 Hres;
    const unsigned sG = WS_G + (8 * g + w) * 88064u + 3 * r * 1024;
#define SG(u) (3 * r + (u) < 86 ? sG + (u) * 1024 : OOB)
    const unsigned ga = (8 * h + q4) * 192 + (g4 & 1) * 96 + 8 * pp;
    f16 cs[3] = {};
    {
        f4 lo, hi; reduce(lo, hi);
        STAMP(28);
        Hres = (u4){pk2(lo2(lo) + (f2){blo(xres[0]), bhi(xres[0])}), pk2(hi2(lo) + (f2){blo(xres[1]), bhi(xres[1])}), pk2(lo2(hi) + (f2){blo(xres[2]), bhi(xres[2])}), pk2(hi2(hi) + (f2){blo(xres[3]), bhi(xres[3])})};
        st16(Hres, vh, rsF, sHw);
        f2 s1 = {0.f, 0.f}, s2 = {0.f, 0.f};
#pragma unroll
        for (int e = 0; e < 4; ++e) { const f2 a = {blo(Hres[e]), bhi(Hres[e])}; s1 += a; s2 += a * a; }
        const float t1 = add_x32(add_x16(add_x8(s1[0] + s1[1]))), t2 = add_x32(add_x16(add_x8(s2[0] + s2[1])));
        st8((u2){uf(t1), uf(t2)}, vs, rsF, sSw);
        STAMP(33);
        asm volatile("s_waitcnt vmcnt(2) lgkmcnt(0)\n\ts_barrier" : "+v"(ck) :: "memory");
        STAMP(40);
        const unsigned o = ga + w * 12288;
        u4 a[12];
#pragma unroll
        for (int i = 0; i < 12; ++i) a[i] = ldtr(o + (i / 3) * 3072 + (i % 3) * 32, o + (i / 3) * 3072 + (i % 3) * 32 + 768);
        SBAR();
#pragma unroll
        for (int i = 0; i < 12; ++i) cs[i % 3] = MFMA32(a[i], ones, cs[i % 3]);
        asm volatile("" : "+v"(cs[0]), "+v"(cs[1]), "+v"(cs[2]));
    }
    STAMP(36);
    const u4 rsWd = mk_rsrc(wd + fb * 128u, 1376u * 1024u - fb * 128u);
    if (t32 == 0) {
#pragma unroll
        for (int u = 0; u < 3; ++u)
#pragma unroll
            for (int qd = 0; qd < 4; ++qd) sts16f(L_CSP + w * 384 + u * 128 + qd * 32 + h * 16, (f4){cs[u][4 * qd], cs[u][4 * qd + 1], cs[u][4 * qd + 2], cs[u][4 * qd + 3]});
    }
    if (w == 7 && UNLIKELY(any_lane(ck[0] != tag[0] || ck[1] != tag[1]))) POLL(vc2);
    BAR_VM(0);
    STAMP(6);
    const unsigned d2l = hh * 6144 + j * 3072, d2s = (352 * hh + 112 + 24 * j) * 1024;
#define WD2(R, i) dma_bufi<(R) * 24576 + (i) * 1024, (R) * 49152 + (i) * 8192>(d2l, vrow, rsWd, d2s)
    {
        const unsigned sH = WS_H + (8 * g + w) * 32768u, sS = WS_ST + (8 * g + w) * 2048u;
#define PD4 6
        constexpr int PD = PD4;
        u4 Hb[32], A0[2], A1[2];
        u2 st[4];
        f16 ag[3] = {};
        const unsigned cf = L_CF + w * 384, vstat = h * 1024 + t32 * 8;
#pragma unroll
        for (int i = 0; i < PD; ++i) ld16(Hb[kgo(i)], l16, rsF, sH + kgo(i) * 1024);
        if (l < 48) { f2 s = {0.f, 0.f};
#pragma unroll
            for (int i = 0; i < 8; ++i) s += *(LAS const f2*)(L_CSP + i * 384 + l * 8);
            sts8f(cf + l * 8, s); }
        A0[0] = ldtr(ga, ga + 768); A1[0] = ldtr(ga + 32, ga + 800);
        unsigned mx = 0;
#define P4_STEP(i) do { constexpr int c = (i) & 1, n = c ^ 1, k = kgo(i); const unsigned o = ga + ((i) < 31 ? kgo((i) + 1) * 3072 : 64); \
            if ((i) + PD < 32) ld16(Hb[kgo((i) + PD < 32 ? (i) + PD : 0)], l16, rsF, sH + kgo((i) + PD) * 1024); \
            else if ((i) + PD < 36) ld8(st[((i) + PD - 32) & 3], vstat, rsF, sS + ((i) + PD - 32) * 256); \
            WAITV1((i) + PD < 36 ? PD : 35 - (i), Hb[k]); \
            if ((i) < 8) FRESH16(Hb[k], l16, rsF, sH + k * 1024); else { mx = max(mx, Hb[k][3]); asm volatile("" : "+v"(mx)); } if ((i) == 0) STAMP(7); SBAR(); \
            ag[0] = MFMA32(A0[c], Hb[k], ag[0]); A0[n] = ldtr(o, o + 768); SBAR(); \
            ag[1] = MFMA32(A1[c], Hb[k], ag[1]); if ((i) < 31) A1[n] = ldtr(o + 32, o + 800); SBAR(); } while (0)
#define P4_4(i) P4_STEP(i); P4_STEP((i) + 1); P4_STEP((i) + 2); P4_STEP((i) + 3)
        P4_4(0); P4_4(4); P4_4(8); P4_4(12); P4_4(16); P4_4(20); P4_4(24); P4_4(28);
        WAITV4(0, st[0], st[1], st[2], st[3]);
        if (UNLIKELY(any_lane(max(max(mx, max(st[0][1], st[1][1])), max(st[2][1], st[3][1])) == ~0u))) {
#pragma unroll
            for (int i = 0; i < 32; ++i) FRESH16(Hb[i], l16, rsF, sH + i * 1024);
#pragma unroll 1
            for (int s_ = 0; s_ < SPIN_CAP; ++s_) { NRETRY();
#pragma unroll
                for (int i = 0; i < 4; ++i) ld8(st[i], vstat, rsF, sS + i * 256);
                WAITV4(0, st[0], st[1], st[2], st[3]);
                if (!any_lane(max(max(st[0][1], st[1][1]), max(st[2][1], st[3][1])) == ~0u)) break;
            }
            ag[0] = z16; ag[1] = z16;
#pragma unroll
            for (int i = 0; i < 32; ++i) { const unsigned o = ga + kgo(i) * 3072; ag[0] = MFMA32(ldtr(o, o + 768), Hb[kgo(i)], ag[0]); ag[1] = MFMA32(ldtr(o + 32, o + 800), Hb[kgo(i)], ag[1]); }
        }
        STAMP(8);
        const float t1 = add_x32(fu(st[0].x) + fu(st[1].x) + fu(st[2].x) + fu(st[3].x)), t2 = add_x32(fu(st[0].y) + fu(st[1].y) + fu(st[2].y) + fu(st[3].y));
        float nm = t1 * (-1.f / 512), ve = __builtin_fmaxf(t2 * (1.f / 512) - nm * nm, 0.f) + 1e-5f;
        const float rs = __builtin_amdgcn_rsqf(ve);
        float ka = rs * -2.3022081986f, kb = rs * rs * rs * -0.1029432397f;
        asm volatile("" : "+v"(nm), "+v"(ve), "+v"(ka), "+v"(kb));
        const unsigned d1l = L_A + hh * 14336 + 7 * j * 1024, d1s = (352 * hh + 56 * j) * 1024;
#define WD1(i) dma_bufi<(i) * 1024, (i) * 8192>(d1l, vrow, rsWd, d1s)
        f4 cg, cu; f2 gt, gu, zz; u4 Gy;
#define GE_C(u, qd) do { cg = lds16f(cf + (u) * 128 + (qd) * 32 + h * 16); cu = lds16f(cf + (u) * 128 + (2 + (qd)) * 32 + h * 16); } while (0)
#define GE_0(u, pr) do { gt = (f2){ag[u][2 * (pr)], ag[u][2 * (pr) + 1]} + (((pr) & 1) ? hi2(cg) : lo2(cg)) * nm; gu = (f2){ag[u][8 + 2 * (pr)], ag[u][9 + 2 * (pr)]} + (((pr) & 1) ? hi2(cu) : lo2(cu)) * nm; \
            zz = gt * gt * kb + ka; asm volatile("" : "+v"(gt), "+v"(gu), "+v"(zz)); } while (0)
#define GE_1() do { zz = gt * zz; gu = gt * gu; zz = (f2){__builtin_amdgcn_exp2f(zz[0]), __builtin_amdgcn_exp2f(zz[1])}; asm volatile("" : "+v"(gu), "+v"(zz)); } while (0)
#define GE_2() do { zz = zz * ve + ve; zz = (f2){__builtin_amdgcn_rcpf(zz[0]), __builtin_amdgcn_rcpf(zz[1])}; asm volatile("" : "+v"(zz)); } while (0)
#define GE_3(pr) do { unsigned y_ = pk2(gu * zz); asm volatile("" : "+v"(y_)); Gy[pr] = y_; } while (0)
        GE_C(0, 0);
#define P4B_STEP(i) do { constexpr int c = (i) & 1, n = c ^ 1, p = (i) >> 2, u = p >> 2, pr = p & 3; const unsigned o = ga + ((i) + 1) * 3072 + 64; \
            if ((i) == 16) BAR_L(); SBAR(); \
            ag[2] = MFMA32(A0[c], Hb[i], ag[2]); if ((i) < 31) A0[n] = ldtr(o, o + 768); SBAR(); \
            if (((i) & 3) == 0) GE_0(u, pr); if (((i) & 3) == 1) { GE_1(); if ((pr & 1) && p < 7) GE_C((p + 1) >> 2, (((p + 1) >> 1) & 1)); } if (((i) & 3) == 2) GE_2(); \
            if (((i) & 3) == 3) { GE_3(pr); if (pr == 3) st16(Gy, l16, rsF, SG(u)); } \
            if ((i) < 14 && !((i) & 1)) WD1(((i) >> 1) & 7); \
            if ((i) >= 17 && (i) < 29 && ((i) & 1)) WD2((((i) + 7) >> 1) / 3 & 1, (((i) + 7) >> 1) % 3); SBAR(); } while (0)
#define P4B_4(i) P4B_STEP(i); P4B_STEP((i) + 1); P4B_STEP((i) + 2); P4B_STEP((i) + 3)
        P4B_4(0); P4B_4(4); P4B_4(8); P4B_4(12); P4B_4(16); P4B_4(20); P4B_4(24); P4B_4(28);
        STAMP(41);
#pragma unroll
        for (int pr = 0; pr < 4; ++pr) {
            if (!(pr & 1)) GE_C(2, pr >> 1);
            const f2 a = (f2){ag[2][2 * pr], ag[2][2 * pr + 1]} + ((pr & 1) ? hi2(cg) : lo2(cg)) * nm, c = (f2){ag[2][8 + 2 * pr], ag[2][9 + 2 * pr]} + ((pr & 1) ? hi2(cu) : lo2(cu)) * nm;
            const f2 z = a * (a * a * kb + ka), d = (f2){__builtin_amdgcn_exp2f(z[0]), __builtin_amdgcn_exp2f(z[1])} * ve + ve;
            Gy[pr] = pk2(a * c * (f2){__builtin_amdgcn_rcpf(d[0]), __builtin_amdgcn_rcpf(d[1])});
        }
        st16(Gy, l16, rsF, SG(2));
        STAMP(30);
    }
    BAR_VM(9);
    STAMP(9);
    {
        u4 D0[7], D1[7];
#pragma unroll
        for (int i = 0; i < 7; ++i) { const unsigned o = L_A + hh * 14336 + i * 2048; D0[i] = ldtr(o + ta0, o + ta0 + 1024); D1[i] = ldtr(o + ta1, o + ta1 + 1024); }
        WD2(2, 0); WD2(2, 1); WD2(2, 2); WD2(3, 0); WD2(3, 1); WD2(3, 2);
        const unsigned d3l = L_A + hh * 6144 + 3 * j * 1024, d3s = (352 * hh + 304 + 24 * j) * 1024;
#define WD3(i) dma_bufi<(i) * 1024, (i) * 8192>(d3l, vrow, rsWd, d3s)
        BAR_VM(6);
        STAMP(29);
        if (w == 4) FLAG(WS_F3 + (g * 32 + r) * 128);
#define PSLEEP 6
        if (w == 7) { const unsigned vc = WS_F3 + (xc * 32 + t32) * 128; __builtin_amdgcn_s_sleep(PSLEEP); POLL(vc); }
        if (w == 6 && b == 0) {
#pragma unroll 1
            for (unsigned i = 0; i < 4; ++i) { const unsigned vc = WS_F1 + (64 * i + l) * 128; POLL(vc); }
            FLAG(0u);
        }
        BAR_L();
        STAMP(46);
        constexpr int PD = 6;
        u4 Gb[22], A0[2], A1[2];
        const unsigned gs = WS_G + (2 * U64 + j) * 88064u + 22 * hh * 1024, gz = hh == 3 ? OOB : gs, o0 = hh * 6144 + ta0, o1 = hh * 6144 + ta1, p0 = L_A + hh * 6144 + ta0, p1 = L_A + hh * 6144 + ta1;
#pragma unroll
        for (int i = 0; i < PD; ++i) ld16(Gb[i], l16, rsF, gs + i * 1024);
        WD3(0); WD3(1); WD3(2);
        acc0 = z16; acc1 = z16;
#define P5_STEP(i) do { constexpr int c = (i) & 1, n = c ^ 1; \
            if ((i) + PD < 22) ld16(Gb[(i) + PD < 22 ? (i) + PD : 0], l16, rsF, ((i) + PD >= 20 ? gz : gs) + ((i) + PD) * 1024); \
            WAITV1(((i) + PD < 22 ? PD : 21 - (i)) + ((i) < PD ? 3 : 0), Gb[i]); if ((i) == 0) STAMP(10); \
            if ((i) == 6) BAR_L(); SBAR(); \
            constexpr int m_ = (i) >= 6 ? (i) - 6 : 0; constexpr unsigned q_ = (m_ / 3) * 24576 + (m_ % 3) * 2048; \
            const unsigned a0_ = (i) < 18 ? o0 + q_ : p0 + ((i) - 18) * 2048, a1_ = (i) < 18 ? o1 + q_ : p1 + ((i) - 18) * 2048; \
            acc0 = MFMA32((i) < 7 ? D0[(i) < 7 ? (i) : 0] : A0[c], Gb[i], acc0); if ((i) >= 6 && (i) < 21) A0[n] = ldtr(a0_, a0_ + 1024); SBAR(); \
            acc1 = MFMA32((i) < 7 ? D1[(i) < 7 ? (i) : 0] : A1[c], Gb[i], acc1); if ((i) >= 6 && (i) < 21) A1[n] = ldtr(a1_, a1_ + 1024); SBAR(); } while (0)
        P5_STEP(0); P5_STEP(1); P5_STEP(2); P5_STEP(3); P5_STEP(4); P5_STEP(5); P5_STEP(6); P5_STEP(7); P5_STEP(8); P5_STEP(9); P5_STEP(10);
        P5_STEP(11); P5_STEP(12); P5_STEP(13); P5_STEP(14); P5_STEP(15); P5_STEP(16); P5_STEP(17); P5_STEP(18); P5_STEP(19); P5_STEP(20); P5_STEP(21);
        }
    STAMP(11);
    BAR_L();
    {
        f4 lo, hi; reduce(lo, hi);
        const u4 Ores = {pk2(lo2(lo) + (f2){blo(Hres[0]), bhi(Hres[0])}), pk2(hi2(lo) + (f2){blo(Hres[1]), bhi(Hres[1])}), pk2(lo2(hi) + (f2){blo(Hres[2]), bhi(Hres[2])}), pk2(hi2(hi) + (f2){blo(Hres[3]), bhi(Hres[3])})};
        st16_plain(Ores, vres, mk_rsrc(out + U64 * 65536u, 65536u), 0);
    }
    asm volatile(".Lfb_code1:");
    STAMP(12);
    if (b == 0 && w == 0) { int mx = -100000, mn = 100000; for (int i = 0; i < 4; ++i) { const int d = (int)(__hip_atomic_load((u64*)(ws + (20u << 20)) + l + 64 * i, __ATOMIC_RELAXED, __HIP_MEMORY_SCOPE_AGENT) - pt0); mx = max(mx, d); mn = min(mn, d); }
        for (int o = 32; o; o >>= 1) { mx = max(mx, __shfl_xor(mx, o)); mn = min(mn, __shfl_xor(mn, o)); }
        const u64 d = __builtin_amdgcn_readfirstlane(mx - mn); { u64 now; asm volatile("s_memrealtime %0\n\ts_waitcnt lgkmcnt(0)" : "=s"(now)::"memory"); const u64 until = now + 10 * ((d > 10000 ? 0 : d));
#pragma unroll 1
        for (int i = 0; i < 1000000 && now < until; ++i) asm volatile("s_memrealtime %0\n\ts_waitcnt lgkmcnt(0)" : "=s"(now)::"memory"); } }
#endif
}

#define WSOFF 0
extern "C" void kernel_launch(void* const* d_in, const int* in_sizes, int n_in, void* d_out, int out_size, void* d_ws, size_t ws_size, hipStream_t stream) {
    (void)in_sizes; (void)n_in; (void)out_size; (void)ws_size;
    (void)hipFuncSetAttribute((const void*)fused_block, hipFuncAttributeMaxDynamicSharedMemorySize, LDS_BYTES);
    fused_block<<<256, 512, LDS_BYTES, stream>>>((const char*)d_in[0], (const char*)d_in[1], (const char*)d_in[2], (const char*)d_in[4], (const char*)d_in[5], (const char*)d_in[6],
                                                 (char*)d_out, (char*)d_ws + WSOFF);
}
#define LASTSTAMP 12
#define STAMP_NAMES "start", "P1 loop end", "KV drained", 0, "O first data", "O drained", "h drained", "h first data", "P4 pass1 end", "P4 end bar", "G first data", "P5 loop end", "end", 0, 0, 0, \
  "st0", "st1", "st2", "st3", "st4", "st5", "st6", "st7", "KV first data", "att done", "O stored", "P3 loop end", "P3 reduced", "G drained", "G stored", 0, "KV stored", "h stored", \
  "LN sums", "pre KV bar", "colsum done", "A read", 0, "setup done", "GU bar", "P4 pass2 end", "att blk3", "att blk4", "att blk0", "comb bar", "G flags"
```

```cpp
#include <hip/hip_runtime.h>
#include <hip/hip_bf16.h>
#include <stdint.h>
#pragma clang diagnostic ignored "-Wint-to-pointer-cast"

typedef __bf16 bf8 __attribute__((ext_vector_type(8)));
typedef __bf16 bf2 __attribute__((ext_vector_type(2)));
typedef short s4 __attribute__((ext_vector_type(4)));
typedef short s8 __attribute__((ext_vector_type(8)));
typedef float f2 __attribute__((ext_vector_type(2)));
typedef float f4 __attribute__((ext_vector_type(4)));
typedef float f16 __attribute__((ext_vector_type(16)));
typedef unsigned u2 __attribute__((ext_vector_type(2)));
typedef unsigned u4 __attribute__((ext_vector_type(4)));
typedef unsigned long long u64;
#define LAS __attribute__((address_space(3)))
#define DEV __device__ __forceinline__

constexpr unsigned WS_F1 = 32768, WS_F2 = 65536, WS_F3 = 98304;
constexpr unsigned WS_KV = 1u << 20;
constexpr unsigned WS_O = 5u << 20;
constexpr unsigned WS_H = 7u << 20;
constexpr unsigned WS_ST = 9u << 20;
constexpr unsigned WS_G = 10u << 20;
constexpr unsigned WS_DIAG = 32u << 20;
constexpr unsigned LDS_BYTES = 163840;
constexpr unsigned L_HOLE = 73728;
constexpr unsigned L_OC = L_HOLE;
constexpr unsigned L_A = 98304;
constexpr unsigned L_CSP = 155648, L_CF = L_CSP + 3072;
constexpr int SPIN_CAP = 1 << 14;
constexpr unsigned OOB = 0x40000000u;

#define MFMA16(a, b, c) __builtin_amdgcn_mfma_f32_16x16x32_bf16(__builtin_bit_cast(bf8, a), __builtin_bit_cast(bf8, b), c, 0, 0, 0)
#define MFMA32(a, b, c) __builtin_amdgcn_mfma_f32_32x32x16_bf16(__builtin_bit_cast(bf8, a), __builtin_bit_cast(bf8, b), c, 0, 0, 0)
#define SBAR() __builtin_amdgcn_sched_barrier(0)
#define BAR_VM(N) asm volatile("s_waitcnt vmcnt(%0) lgkmcnt(0)\n\ts_barrier" ::"i"(N) : "memory")
#define BAR_L() asm volatile("s_waitcnt lgkmcnt(0)\n\ts_barrier" ::: "memory")

DEV u4 mk_rsrc(const void* p, unsigned bytes) { const u64 a = (u64)p; return (u4){(unsigned)a, (unsigned)(a >> 32) & 0xffffu, bytes, 0x00020000u}; }
DEV void dma_buf(unsigned lds, unsigned voff, u4 rs, unsigned soff) {
    asm volatile("s_mov_b32 m0, %0\n\ts_nop 0\n\tbuffer_load_dwordx4 %1, %2, %3 offen lds" ::"s"(lds), "v"(voff), "s"(rs), "s"(soff) : "memory"); }
template <unsigned LI, unsigned SI> DEV void dma_bufi(unsigned lds, unsigned voff, u4 rs, unsigned soff) {
    unsigned t; asm volatile("s_add_u32 m0, %1, %5\n\ts_add_u32 %0, %4, %6\n\tbuffer_load_dwordx4 %2, %3, %0 offen lds" : "=&s"(t) : "s"(lds), "v"(voff), "s"(rs), "s"(soff), "i"(LI), "i"(SI) : "memory", "scc"); }
template <unsigned LI> DEV void dma_glbi(unsigned lds, const char* p) {
    asm volatile("s_add_u32 m0, %0, %2\n\ts_nop 0\n\tglobal_load_lds_dwordx4 %1, off" ::"s"(lds), "v"(p), "i"(LI) : "memory", "scc"); }
DEV void dma_glb(unsigned lds, const char* p) {
    asm volatile("s_mov_b32 m0, %0\n\ts_nop 0\n\tglobal_load_lds_dwordx4 %1, off" ::"s"(lds), "v"(p) : "memory"); }
DEV void ld16(u4& d, unsigned voff, u4 rs, unsigned soff) {
    asm volatile("buffer_load_dwordx4 %0, %1, %2, %3 offen sc1" : "=v"(d) : "v"(voff), "s"(rs), "s"(soff) : "memory"); }
DEV void ld16_plain(u4& d, unsigned voff, u4 rs, unsigned soff) {
    asm volatile("buffer_load_dwordx4 %0, %1, %2, %3 offen" : "=v"(d) : "v"(voff), "s"(rs), "s"(soff) : "memory"); }
template <int OFF> DEV void ld16_imm(u4& d, unsigned voff, u4 rs) {
    asm volatile("buffer_load_dwordx4 %0, %1, %2, 0 offen offset:%3" : "=v"(d) : "v"(voff), "s"(rs), "i"(OFF) : "memory"); }
DEV void ld8(u2& d, unsigned voff, u4 rs, unsigned soff) {
    asm volatile("buffer_load_dwordx2 %0, %1, %2, %3 offen sc1" : "=v"(d) : "v"(voff), "s"(rs), "s"(soff) : "memory"); }
DEV void ld16_v(u4& d0, u4& d1, unsigned voff, u4 rs, unsigned soff) {
    asm volatile("s_mov_b64 exec, %5\n\tbuffer_load_dwordx4 %0, %2, %3, %4 offen offset:1024 sc1\n\tbuffer_load_dwordx4 %1, %2, %3, %4 offen offset:1536 sc1\n\ts_mov_b64 exec, -1"
                 : "+v"(d0), "+v"(d1) : "v"(voff), "s"(rs), "s"(soff), "s"(0x0000FFFF0000FFFFull) : "memory"); }
DEV void st16(u4 d, unsigned voff, u4 rs, unsigned soff) {
    asm volatile("buffer_store_dwordx4 %0, %1, %2, %3 offen sc1\n\ts_nop 1" ::"v"(d), "v"(voff), "s"(rs), "s"(soff) : "memory"); }
DEV void st16_plain(u4 d, unsigned voff, u4 rs, unsigned soff) {
    asm volatile("buffer_store_dwordx4 %0, %1, %2, %3 offen\n\ts_nop 1" ::"v"(d), "v"(voff), "s"(rs), "s"(soff) : "memory"); }
DEV void st8(u2 d, unsigned voff, u4 rs, unsigned soff) {
    asm volatile("buffer_store_dwordx2 %0, %1, %2, %3 offen sc1\n\ts_nop 1" ::"v"(d), "v"(voff), "s"(rs), "s"(soff) : "memory"); }
#define WAITV1(N, a) asm volatile("s_waitcnt vmcnt(%1)" : "+v"(a) : "i"(N) : "memory")
#define WAITV1D(ND, NS, a) asm volatile("s_waitcnt vmcnt(%1)\n\ts_cmp_lt_u32 %3, 4\n\ts_cbranch_scc1 .Lw%=\n\ts_waitcnt vmcnt(%2)\n.Lw%=:" : "+v"(a) : "i"(ND), "i"(NS), "s"(w) : "memory", "scc")
#define WAITV3(N, a, b, c) asm volatile("s_waitcnt vmcnt(%3)" : "+v"(a), "+v"(b), "+v"(c) : "i"(N) : "memory")
#define WAITV4(N, a, b, c, d) asm volatile("s_waitcnt vmcnt(%4)" : "+v"(a), "+v"(b), "+v"(c), "+v"(d) : "i"(N) : "memory")

DEV u4 lds16(unsigned a) { return *(LAS const u4*)a; }
DEV f4 lds16f(unsigned a) { return *(LAS const f4*)a; }
DEV float lds4f(unsigned a) { return *(LAS const float*)a; }
DEV void sts16(unsigned a, u4 v) { *(LAS u4*)a = v; }
DEV void sts16f(unsigned a, f4 v) { *(LAS f4*)a = v; }
DEV void sts8f(unsigned a, f2 v) { *(LAS f2*)a = v; }
DEV void sts4f(unsigned a, float v) { *(LAS float*)a = v; }
DEV u4 ldtr(unsigned a0, unsigned a1) {
    const s4 lo = __builtin_amdgcn_ds_read_tr16_b64_v4i16((LAS s4*)a0), hi = __builtin_amdgcn_ds_read_tr16_b64_v4i16((LAS s4*)a1);
    return __builtin_bit_cast(u4, __builtin_shufflevector(lo, hi, 0, 1, 2, 3, 4, 5, 6, 7)); }
DEV unsigned pk2(f2 a) { return __builtin_bit_cast(unsigned, __builtin_convertvector(a, bf2)); }
DEV f2 lo2(f4 a) { return (f2){a[0], a[1]}; }
DEV f2 hi2(f4 a) { return (f2){a[2], a[3]}; }
DEV unsigned pk(float a, float b) { return __builtin_bit_cast(unsigned, __builtin_convertvector((f2){a, b}, bf2)); }
DEV float blo(unsigned v) { return __builtin_bit_cast(float, v << 16); }
DEV float bhi(unsigned v) { return __builtin_bit_cast(float, v & 0xffff0000u); }
DEV float fu(unsigned v) { return __builtin_bit_cast(float, v); }
DEV unsigned uf(float v) { return __builtin_bit_cast(unsigned, v); }
DEV float add_x32(float v) { auto r = __builtin_amdgcn_permlane32_swap(uf(v), uf(v), false, false); const unsigned a = r[0], b = r[1]; return fu(a) + fu(b); }
DEV float add_x16(float v) { auto r = __builtin_amdgcn_permlane16_swap(uf(v), uf(v), false, false); const unsigned a = r[0], b = r[1]; return fu(a) + fu(b); }
DEV float max_x32(float v) { auto r = __builtin_amdgcn_permlane32_swap(uf(v), uf(v), false, false); const unsigned a = r[0], b = r[1]; return __builtin_fmaxf(fu(a), fu(b)); }
template <int C> DEV float add_dpp(float v) { return v + fu(__builtin_amdgcn_update_dpp(0u, uf(v), C, 0xf, 0xf, true)); }
DEV float add_x8(float v) { return v + fu(__builtin_amdgcn_update_dpp(0u, uf(v), 0x128, 0xf, 0xf, true)); }
constexpr u64 diag_mask(int r) { u64 m = 0; for (int l = 0; l < 64; ++l) { const int key = 16 * ((r >> 2) & 1) + 8 * (l >> 5) + 4 * (r >> 3) + (r & 3); if (key <= (l & 31)) m |= 1ull << l; } return m; }
DEV float sel(u64 m, float t, float f) { return __builtin_amdgcn_inverse_ballot_w64(m) ? t : f; }

template <unsigned LDS> DEV void touch(unsigned voff, u4 rs, unsigned soff) {
    asm volatile("s_mov_b32 m0, %3\n\ts_nop 0\n\tbuffer_load_dword %0, %1, %2 offen lds" ::"v"(voff), "s"(rs), "s"(soff), "i"(LDS) : "memory"); }
typedef short sh2 __attribute__((ext_vector_type(2)));
constexpr int kgo(int i) { return i < 8 ? 4 * i : (i - 8) / 3 * 4 + (i - 8) % 3 + 1; }
#define RSLEEP 4
DEV bool any_lane(bool c) { return __builtin_amdgcn_ballot_w64(c) != 0; }
#define UNLIKELY(x) __builtin_expect(!!(x), 0)
#define NRETRY()
#define FRESH16(d, voff, rs, soff) do { if (UNLIKELY(any_lane((d)[3] == ~0u))) { \
        _Pragma("unroll 1") for (int s_ = 0; s_ < SPIN_CAP; ++s_) { NRETRY(); u4 t_; asm volatile("buffer_load_dwordx4 %0, %1, %2, %3 offen sc1\n\ts_waitcnt vmcnt(0)" : "=&v"(t_) : "v"(voff), "s"(rs), "s"(soff) : "memory"); \
            d = t_; if (!any_lane(t_[3] == ~0u)) break; __builtin_amdgcn_s_sleep(2); } } } while (0)
#define POLL(voff) do { \
        _Pragma("unroll 1") for (int s_ = 0; s_ < 4 * SPIN_CAP; ++s_) { u2 t_; asm volatile("buffer_load_dwordx2 %0, %1, %2, 0 offen sc1\n\ts_waitcnt vmcnt(0)" : "=&v"(t_) : "v"(voff), "s"(rsF) : "memory"); \
            if (!any_lane(t_[0] != tag[0] || t_[1] != tag[1])) break; NRETRY(); __builtin_amdgcn_s_sleep(RSLEEP); } } while (0)
#define FLAG(off) st8(tag, l == 0 ? (off) : OOB, rsF, 0)
#define STAMP(i)

__global__ __launch_bounds__(512, 2) void fused_block(const char* x_, const char* wqkv_, const char* wo_, const char* wg_, const char* wu_, const char* wd_, char* out_, char* ws_) {
#if defined(__HIP_DEVICE_COMPILE__)

    typedef u64 u64x8 __attribute__((ext_vector_type(8)));
    u64x8 ka; asm volatile("s_load_dwordx16 %0, %1, 0x0\n\ts_waitcnt lgkmcnt(0)" : "=s"(ka) : "s"(__builtin_amdgcn_kernarg_segment_ptr()) : "memory");

    const char *x = (const char*)ka[0], *wqkv = (const char*)ka[1], *wo = (const char*)ka[2], *wg = (const char*)ka[3], *wu = (const char*)ka[4], *wd = (const char*)ka[5]; char *out = (char*)ka[6], *ws = (char*)ka[7];
    const unsigned l = threadIdx.x & 63, w = __builtin_amdgcn_readfirstlane(threadIdx.x >> 6);
    const unsigned b = blockIdx.x, xc = b & 7, rk = b >> 3;
    const unsigned T64 = 8 * (xc >> 1) + (rk >> 2), hq = 4 * (xc & 1) + (rk & 3);
    STAMP(0);
    const unsigned u8 = l >> 3, l7 = l & 7;
    u4 xr[8];
    const u4 rsX = mk_rsrc(x + T64 * 65536u, 65536u);
    const unsigned vx = (8 * w + u8) * 1024 + l7 * 16;
    {
        const char* pc; unsigned nb; asm volatile("s_getpc_b64 %0\n.Lfb_code0:\n\ts_mov_b32 %1, .Lfb_code1-.Lfb_code0" : "=s"(pc), "=s"(nb));
        const char* p = l < 6 ? wqkv + ((b * 8 + w) * 6 + l) * 128 : pc + (((rk * 8 + w) * 2 + (l & 1)) * 128 < nb ? ((rk * 8 + w) * 2 + (l & 1)) * 128 : 0);
        asm volatile("s_mov_b32 m0, %1\n\ts_nop 0\n\tglobal_load_lds_dword %0, off" ::"v"(p), "s"(L_A + w * 8192) : "memory"); }
    ld16_imm<0>(xr[0], vx, rsX); ld16_imm<128>(xr[1], vx, rsX); ld16_imm<256>(xr[2], vx, rsX); ld16_imm<384>(xr[3], vx, rsX);
#define XB 6
    if (XB > 4) { ld16_imm<512>(xr[4], vx, rsX); ld16_imm<640>(xr[5], vx, rsX); }
    if (XB > 6) { ld16_imm<768>(xr[6], vx, rsX); ld16_imm<896>(xr[7], vx, rsX); }
    const u4 rsW = mk_rsrc(wqkv + hq * 128u, 512u * 3072u - hq * 128u);
    SBAR();
    const unsigned w3 = w & 3, wk2 = w3 * 2048;
    unsigned vw0 = (16 * w3 + u8) * 3072 + 32 * ((l7 >> 1) ^ ((l >> 4) & 1)) + 16 * (l7 & 1), vw1 = (vw0 + 24576) ^ 64;
#define SLOT(i) ((((i) + 1) & 3) * 24576u)
#define SP(i, p) dma_bufi<SLOT(i) + ((p) >> 1) * 8192 + ((p) & 1) * 1024, (i) * 196608u + ((p) >> 1) * 1024u>(wk2, ((p) & 1) ? vw1 : vw0, rsW, 0)
    const unsigned U64 = 4 * xc + (rk >> 3), fb = rk & 7;
    const unsigned g = 2 * (xc >> 1) + (rk >> 4), r = 16 * (xc & 1) + (rk & 15);
    const unsigned j = w & 1, hh = w >> 1;
    const unsigned t16 = l & 15, g4 = l >> 4, h = l >> 5, t32 = l & 31, q4 = t16 >> 2, pp = l & 3, l16 = l * 16, l128 = l * 128;
    const u4 rsF = mk_rsrc(ws, 16u << 20);
    const u4 ones = {0x3f803f80u, 0x3f803f80u, 0x3f803f80u, 0x3f803f80u};
    const unsigned kvb = WS_KV + (4 * hq + hh) * 131072u;
    const unsigned skv = kvb + (2 * T64 + j) * 2048;
    const unsigned sOw = WS_O + (2 * T64 + j) * 32768u + (4 * hq + hh) * 1024;
    u2 tag; u4 xres;
    unsigned vres = (32 * j + 8 * hh + l7) * 1024 + fb * 128 + u8 * 16;
    unsigned xw0 = L_A + (8 * w + u8) * 1024 + 16 * (l7 ^ (8 * (w & 1) + u8)), xw1 = xw0 ^ 128;
    unsigned xa[4];
#pragma unroll
    for (int q = 0; q < 4; ++q) { xa[q] = L_A + (32 * j + t16) * 1024 + 16 * ((4 * q + g4) ^ t16); asm volatile("" : "+v"(xa[q])); }
    unsigned wa = (8 * g4 + q4) * 128 + 32 * (hh ^ ((q4 >> 1) | ((g4 & 1) << 1))) + 8 * pp;
    f4 aq[2] = {}, ak[2] = {}, av[2] = {};
    unsigned vk = (h * 32 + (16 * ((t32 >> 3) & 1) + 8 * ((t32 >> 2) & 1) + 4 * (t32 >> 4) + (t32 & 3))) * 16, vv_ = (h * 16 + t16) * 16;
    unsigned vst = 1024 + ((g4 & 1) * 32 + (g4 >> 1) * 16 + t16) * 16;
    unsigned vrow = u8 * 1024 + 16 * (l7 ^ (((l >> 4) & 1) << 2));
    const char* gp[3];
#pragma unroll
    for (int i = 0; i < 3; ++i) { const unsigned z = 64 * i + l, k = (z * 43691u) >> 19, c = z - 12 * k, m = c >= 6, cc = c - 6 * m; unsigned un = 3 * r + (cc >> 1); un = un > 85 ? 85 : un;
        gp[i] = (m ? wu : wg) + ((16 * w + k) * 2752 + un * 32 + (cc & 1) * 16); asm volatile("" : "+v"(gp[i])); }
    const unsigned wk3 = w * 3072;
#define GU(R, c) dma_glbi<(R) * 24576 + (c) * 1024>(wk3, gp[c] + (R) * 128 * 2752)
#define GUD(R, q) dma_glbi<(R) * 24576 + ((q) / 3) * 12288 + ((q) % 3) * 1024>(wk3, gp[(q) % 3] + ((R) * 128 + ((q) / 3) * 64) * 2752)
    asm volatile("" : "+v"(vw0), "+v"(vw1), "+v"(xw0), "+v"(xw1), "+v"(wa), "+v"(vres), "+v"(vk), "+v"(vv_), "+v"(vrow), "+v"(vst));
    STAMP(39);
    if (XB <= 4) { ld16_imm<512>(xr[4], vx, rsX); ld16_imm<640>(xr[5], vx, rsX); }
    if (XB <= 6) { ld16_imm<768>(xr[6], vx, rsX); ld16_imm<896>(xr[7], vx, rsX); }
#define NO ((void)0)
#define TBPOS 0
#define TOUCH_B() do { const char* tp = (w == 4 ? wo : w == 5 ? wg : w == 6 ? wu : wd) + (w == 4 ? b * 16 + (l & 15) : b * 43 + (l < 43 ? l : 0)) * 128; \
        asm volatile("global_load_dword v255, %0, off" ::"v"(tp) : "memory", "v255"); } while (0)
#if !defined(NOTB) && TBPOS == 0
    if (w >= 4) TOUCH_B();
#define XW 1
#else
#define XW 0
#endif
#define SSTAMP(i)
    {
        f2 xf[32], s1 = {0.f, 0.f}, s2 = {0.f, 0.f};
#define LN_SUM(i, N, D0, D1) do { if (XW && (N) < 8 - (i)) WAITV1(N, xr[i]); else WAITV1D(N, 7 - (i) + XW, xr[i]); _Pragma("unroll") for (int e = 0; e < 4; ++e) { const unsigned d_ = xr[i][e]; f2 v_ = {blo(d_), bhi(d_)}; asm volatile("" : "+v"(v_)); xf[4 * (i) + e] = v_; \
        s1 += v_; s2 += v_ * v_; } asm volatile("" : "+v"(s1), "+v"(s2)); if (w < 4) { D0; D1; } SBAR(); } while (0)
        LN_SUM(0, 7, NO, NO); LN_SUM(1, 6, NO, NO); LN_SUM(2, 5, NO, NO); LN_SUM(3, 4, NO, NO);
        LN_SUM(4, 3, SP(0, 0), SP(0, 1)); LN_SUM(5, 4, SP(0, 2), SP(0, 3)); LN_SUM(6, 5, SP(0, 4), NO); LN_SUM(7, 5, SP(0, 5), NO);
        SSTAMP(34);
        if (w < 4) { SP(1, 0); SP(1, 1); SP(1, 2); SP(1, 3); SP(1, 4); SP(1, 5); }
#if !defined(NOTB) && TBPOS == 1
        else TOUCH_B();
#endif
        const float t1 = add_dpp<0x141>(add_dpp<0x4e>(add_dpp<0xb1>(s1[0] + s1[1]))), t2 = add_dpp<0x141>(add_dpp<0x4e>(add_dpp<0xb1>(s2[0] + s2[1])));
        const float mu = t1 * (1.f / 512), rs = __builtin_amdgcn_rsqf(__builtin_fmaxf(t2 * (1.f / 512) - mu * mu, 0.f) + 1e-5f), mr = -mu * rs;
#pragma unroll
        for (int i = 0; i < 8; ++i) sts16(((i & 1) ? xw1 : xw0) + (i >> 1) * 256, (u4){pk2(xf[4 * i] * rs + mr), pk2(xf[4 * i + 1] * rs + mr), pk2(xf[4 * i + 2] * rs + mr), pk2(xf[4 * i + 3] * rs + mr)});
    }
#define P1_STEP(n, D0, D1, D2) do { constexpr int c_ = (n) & 1, x_ = c_ ^ 1; constexpr bool m_ = (n) < 15; \
        const unsigned xb = xa[((n) + 1) & 3] + (((n) + 1) >> 2) * 256, wb = wa + SLOT(((n) + 1) >> 1) + x_ * 4096; \
        if (m_) X0[x_] = lds16(xb); SBAR(); \
        aq[0] = MFMA16(Wq[c_], X0[c_], aq[0]); if (m_) Wq[x_] = ldtr(wb, wb + 512); SBAR(); \
        aq[1] = MFMA16(Wq[c_], X1[c_], aq[1]); if (m_) X1[x_] = lds16(xb + 16384); D0; SBAR(); \
        ak[0] = MFMA16(Wk[c_], X0[c_], ak[0]); if (m_) Wk[x_] = ldtr(wb + 8192, wb + 8704); SBAR(); \
        ak[1] = MFMA16(Wk[c_], X1[c_], ak[1]); D1; SBAR(); \
        av[0] = MFMA16(X0[c_], Wv[c_], av[0]); if (m_) Wv[x_] = ldtr(wb + 16384, wb + 16896); SBAR(); \
        av[1] = MFMA16(X1[c_], Wv[c_], av[1]); D2; SBAR(); } while (0)
#define P1_FIRST() do { const unsigned wb = wa + SLOT(0); X0[0] = lds16(xa[0]); Wq[0] = ldtr(wb, wb + 512); X1[0] = lds16(xa[0] + 16384); Wk[0] = ldtr(wb + 8192, wb + 8704); Wv[0] = ldtr(wb + 16384, wb + 16896); } while (0)
#define XRES() ld16_plain(xres, vres, mk_rsrc(x + U64 * 65536u, 65536u), 0)
    if (w < 4) {
        u4 X0[2], X1[2], Wq[2], Wk[2], Wv[2];
        SP(2, 0); SP(2, 1); SP(2, 2); SP(2, 3); SP(2, 4); SP(2, 5);
        BAR_VM(12); STAMP(16);  P1_FIRST(); P1_STEP(0, (SP(3, 0), SP(3, 1)), (SP(3, 2), SP(3, 3)), (SP(3, 4), SP(3, 5)));
        BAR_VM(12); SSTAMP(17); P1_STEP(1, SP(4, 0), SP(4, 1), SP(4, 2)); P1_STEP(2, SP(4, 3), SP(4, 4), SP(4, 5));
        BAR_VM(12); SSTAMP(18); P1_STEP(3, SP(5, 0), SP(5, 1), SP(5, 2)); P1_STEP(4, SP(5, 3), SP(5, 4), SP(5, 5));
        BAR_VM(12); SSTAMP(19); P1_STEP(5, SP(6, 0), SP(6, 1), SP(6, 2)); P1_STEP(6, SP(6, 3), SP(6, 4), SP(6, 5));
        BAR_VM(12); SSTAMP(20); P1_STEP(7, SP(7, 0), SP(7, 1), SP(7, 2)); P1_STEP(8, SP(7, 3), SP(7, 4), SP(7, 5));
        BAR_VM(12); SSTAMP(21); P1_STEP(9, XRES(), GUD(1, 0), NO); P1_STEP(10, GUD(1, 1), GUD(1, 2), NO);
        BAR_VM(10); SSTAMP(22); P1_STEP(11, GUD(1, 3), GUD(1, 4), NO); P1_STEP(12, GUD(1, 5), NO, NO);
        BAR_VM(7);  SSTAMP(23); P1_STEP(13, NO, NO, NO); P1_STEP(14, NO, NO, NO); P1_STEP(15, NO, NO, NO);
        WAITV1(6, xres); tag = (u2){0u, 0u};
    } else {
        u4 X0[2], X1[2], Wq[2], Wk[2], Wv[2];
        const u4 bad = {~0u, ~0u, ~0u, ~0u};
#define MK(v, s_) st16(bad, v, rsF, s_)
#if !defined(NOTB) && TBPOS == 2
#define TCH(c) if (w == (c)) TOUCH_B()
#else
#define TCH(c) NO
#endif
        BAR_L(); STAMP(16);  P1_FIRST(); P1_STEP(0, ld8(tag, 0u * l, rsF, 0), MK(l16, skv), TCH(5));
        BAR_L(); SSTAMP(17); P1_STEP(1, NO, MK(l16 + 1024, skv), TCH(6)); P1_STEP(2, NO, MK(l16, skv - 262144), NO);
        BAR_L(); SSTAMP(18); P1_STEP(3, NO, MK(l16 + 1024, skv - 262144), TCH(4)); P1_STEP(4, NO, MK(l16, sOw), NO);
        BAR_L(); SSTAMP(19); P1_STEP(5, NO, MK(l16, sOw - 2048), TCH(7)); P1_STEP(6, XRES(), NO, NO);
        BAR_L(); SSTAMP(20); P1_STEP(7, NO, NO, NO); P1_STEP(8, NO, NO, NO);
        BAR_L(); SSTAMP(21); P1_STEP(9, NO, NO, NO); P1_STEP(10, NO, NO, NO);
        asm volatile("s_waitcnt vmcnt(0)" : "+v"(tag), "+v"(xres) :: "memory");
        BAR_L(); SSTAMP(22);
        { const unsigned lo = tag.x + 1u; tag.y += (lo == 0u); tag.x = lo; }
                             P1_STEP(11, FLAG(w == 4 ? WS_F1 + (T64 * 8 + hq) * 128 : OOB), NO, NO); P1_STEP(12, NO, NO, NO);
        BAR_L(); SSTAMP(23); P1_STEP(13, NO, NO, NO); P1_STEP(14, NO, NO, NO); P1_STEP(15, NO, NO, NO);
    }
    STAMP(1);
    u4 Qf;
    {
        { auto a = __builtin_amdgcn_permlane16_swap(pk2(lo2(ak[0])), pk2(lo2(ak[1])), false, false); auto c = __builtin_amdgcn_permlane16_swap(pk2(hi2(ak[0])), pk2(hi2(ak[1])), false, false);
          st16((u4){a[0], c[0], a[1], c[1]}, l16, rsF, skv); }
        st16((u4){pk2(lo2(av[0])), pk2(hi2(av[0])), pk2(lo2(av[1])), pk2(hi2(av[1]))}, vst, rsF, skv);
        STAMP(32);
        SBAR();
        constexpr float QS = 0.25f * 1.4426950408889634f;
        { auto a = __builtin_amdgcn_permlane16_swap(pk2(lo2(aq[0]) * QS), pk2(lo2(aq[1]) * QS), false, false); auto c = __builtin_amdgcn_permlane16_swap(pk2(hi2(aq[0]) * QS), pk2(hi2(aq[1]) * QS), false, false);
          Qf = (u4){a[0], c[0], a[1], c[1]}; }
        sts16(L_OC + (w ^ 1) * 2304 + l16, Qf);
    }
    const int Tb = 2 * (int)T64 - 8;
    u4 K[5], V0[5], V1[5];
    const int blk[5] = {Tb + 8 + (int)j, Tb + (int)j, Tb + 2 + 3 * (int)j, Tb + 3 + 3 * (int)j, Tb + 4 + 3 * (int)j};
#pragma unroll
    for (int i = 0; i < 5; ++i) { const unsigned o = blk[i] < 0 ? 0u : 0x3f803f80u; V0[i] = (u4){o, o, o, o}; V1[i] = V0[i]; asm volatile("" : "+v"(V0[i]), "+v"(V1[i])); }
    unsigned pm[8];
    { const short uq = (short)(t32 - 8 * h);
#pragma unroll
      for (int i = 0; i < 8; ++i) { const short c = (short)(16 * ((i >> 1) & 1) + 4 * (i >> 2) + 2 * (i & 1)); pm[i] = __builtin_bit_cast(unsigned, ((sh2){uq, uq} - (sh2){c, (short)(c + 1)}) >> (sh2){15, 15}); asm volatile("" : "+v"(pm[i])); } }
    unsigned so[5];
#pragma unroll
    for (int i = 0; i < 5; ++i) so[i] = blk[i] < 0 ? OOB : kvb + (unsigned)blk[i] * 2048;
    if (w == 7) {
        const int Tp = (int)T64 - 1 - (int)(l & 3); const unsigned vc = WS_F1 + ((Tp < 0 ? T64 : (unsigned)Tp) * 8 + hq) * 128; POLL(vc); }
    STAMP(35);
    BAR_VM(0);
    STAMP(2);
#define KVLD(i) do { ld16(K[i], vk, rsF, so[i]); ld16_v(V0[i], V1[i], vv_, rsF, so[i]); } while (0)
#define KVOK(N, i) do { WAITV3(N, K[i], V0[i], V1[i]); \
        if (UNLIKELY(any_lane(max(max(K[i][3], V0[i][3]), V1[i][3]) == ~0u))) { \
            _Pragma("unroll 1") for (int s_ = 0; s_ < SPIN_CAP; ++s_) { NRETRY(); KVLD(i); WAITV3(0, K[i], V0[i], V1[i]); if (!any_lane(max(max(K[i][3], V0[i][3]), V1[i][3]) == ~0u)) break; __builtin_amdgcn_s_sleep(2); } } } while (0)
    KVLD(2); KVLD(3);
    const u4 Qm = Qf, Qo = lds16(L_OC + w * 2304 + l16);
    f16 Om = {}, Oo = {};
    const f16 z16 = {};
    auto pv_tile = [&](const f16& S, const u4& va, const u4& vb, f16& O) {
        float p[16];
#pragma unroll
        for (int e = 0; e < 16; ++e) p[e] = __builtin_amdgcn_exp2f(S[e]);
        const u4 P0 = {pk(p[0], p[1]), pk(p[2], p[3]), pk(p[4], p[5]), pk(p[6], p[7])}, P1 = {pk(p[8], p[9]), pk(p[10], p[11]), pk(p[12], p[13]), pk(p[14], p[15])};
        O = MFMA32(va, P0, O); O = MFMA32(vb, P1, O);
    };
    const u4 rsWo = mk_rsrc(wo + fb * 128u, 512u * 1024u - fb * 128u);
    const unsigned wol = L_A + w * 8192, wos = w * 65536;
    const unsigned vh = (u8 >> 1) * 1024 + ((u8 & 1) * 32 + 8 * hh + l7) * 16, vs = l < 8 ? (8 * hh + l) * 8 : OOB;
    const unsigned sHw = WS_H + (2 * U64 + j) * 32768u + fb * 4096, sSw = WS_ST + (2 * U64 + j) * 2048u + fb * 256;
#define WO_(i) dma_bufi<(i) * 1024, (i) * 8192>(wol, vrow, rsWo, wos)
#define PIN16(a) asm volatile("" : "+v"(a))
#define MEM(...) do { SBAR(); __VA_ARGS__; SBAR(); } while (0)
#define HALF(S, o, va, O) do { float p_[8]; _Pragma("unroll") for (int e = 0; e < 8; ++e) p_[e] = __builtin_amdgcn_exp2f(S[(o) + e]); \
        O = MFMA32(va, ((u4){pk(p_[0], p_[1]), pk(p_[2], p_[3]), pk(p_[4], p_[5]), pk(p_[6], p_[7])}), O); PIN16(O); } while (0)
#define ATT2(i, M0, M1, M2, M3) do { f16 Sa = MFMA32(K[i], Qm, z16), Sb = MFMA32(K[i], Qo, z16); \
        HALF(Sa, 0, V0[i], Om); MEM(M0); HALF(Sb, 0, V0[i], Oo); MEM(M1); HALF(Sa, 8, V1[i], Om); MEM(M2); HALF(Sb, 8, V1[i], Oo); MEM(M3); } while (0)
    KVOK(3, 2);
    STAMP(24);
    MEM(KVLD(4));
    ATT2(2, NO, KVLD(0), NO, KVLD(1));
    KVOK(9, 3);
    SSTAMP(42);
    ATT2(3, WO_(0), WO_(1), (WO_(2), WO_(3)), WO_(4));
    KVOK(11, 4);
    SSTAMP(43);
    { const u4 bad = {~0u, ~0u, ~0u, ~0u};
      ATT2(4, WO_(5), (WO_(6), WO_(7)), st16(bad, vh, rsF, sHw), st8((u2){~0u, ~0u}, vs, rsF, sSw)); }
    KVOK(13, 0);
    KVOK(10, 1);
    SSTAMP(44);
    u2 ck; const unsigned vc1 = WS_F1 + (U64 * 8 + l7) * 128;
    MEM(ld8(ck, w == 7 ? vc1 : OOB, rsF, 0), GU(2, 0), GU(2, 1), GU(2, 2));
    if (j == 0) { const f16 Sb = MFMA32(K[0], Qo, z16); pv_tile(Sb, V0[0], V1[0], Oo); }
    else        { const f16 Sb = MFMA32(K[1], Qo, z16); pv_tile(Sb, V0[1], V1[1], Oo); }
    const unsigned oc = L_OC + w * 2304;
    {
        const f16 Sx = MFMA32(K[0], Qm, z16), Sy = MFMA32(K[1], Qm, z16);
        sts16f(oc + l16, (f4){Oo[0], Oo[1], Oo[2], Oo[3]}); sts16f(oc + 1024 + l16, (f4){Oo[4], Oo[5], Oo[6], Oo[7]}); sts4f(oc + 2048 + l * 4, Oo[8]);
        float p[16];
#pragma unroll
        for (int e = 0; e < 16; ++e) p[e] = __builtin_amdgcn_exp2f(sel(diag_mask(e), Sx[e], Sy[e]));
        unsigned P[8];
#pragma unroll
        for (int e = 0; e < 8; ++e) P[e] = pk(p[2 * e], p[2 * e + 1]);
        const u4 Y0 = {P[0] & pm[0], P[1] & pm[1], P[2] & pm[2], P[3] & pm[3]}, Y1 = {P[4] & pm[4], P[5] & pm[5], P[6] & pm[6], P[7] & pm[7]};
        const u4 X0_ = {P[0] ^ Y0[0], P[1] ^ Y0[1], P[2] ^ Y0[2], P[3] ^ Y0[3]}, X1_ = {P[4] ^ Y1[0], P[5] ^ Y1[1], P[6] ^ Y1[2], P[7] ^ Y1[3]};
        Om = MFMA32(V0[0], X0_, Om); Om = MFMA32(V1[0], X1_, Om); Om = MFMA32(V0[1], Y0, Om); Om = MFMA32(V1[1], Y1, Om);
    }
    SBAR();
    STAMP(25);
    asm volatile("s_waitcnt vmcnt(3) lgkmcnt(0)\n\ts_barrier" : "+v"(ck) :: "memory");
    SSTAMP(45);
    const unsigned op = L_OC + (w ^ 1) * 2304;
    f4 oa = lds16f(op + l16) + (f4){Om[0], Om[1], Om[2], Om[3]}, ob = lds16f(op + 1024 + l16) + (f4){Om[4], Om[5], Om[6], Om[7]};
    float den = Om[8] + lds4f(op + 2048 + l * 4);
    if (UNLIKELY(any_lane(!(den >= 0x1p-60f && den <= 0x1p60f)))) {
        f16 O = {}; float m = -__builtin_inff();
#pragma unroll 1
        for (int kb = 8; kb >= 0; --kb) {
            const int T = 2 * (int)T64 + (int)j - 8 + kb;
            if (T < 0) break;
            u4 Ks, Va = ones, Vb = ones;
            ld16(Ks, vk, rsF, kvb + (unsigned)T * 2048); ld16_v(Va, Vb, vv_, rsF, kvb + (unsigned)T * 2048);
            WAITV3(0, Ks, Va, Vb);
            f16 S = MFMA32(Ks, Qm, z16);
            const u64 all = (kb != 0 && kb != 8) ? ~0ull : 0ull, inv = kb == 0 ? ~0ull : 0ull;
            float mx = m;
#pragma unroll
            for (int e = 0; e < 16; ++e) { S[e] = sel((diag_mask(e) ^ inv) | all, S[e], -__builtin_inff()); mx = __builtin_fmaxf(mx, S[e]); }
            mx = max_x32(mx);
            const float al = __builtin_amdgcn_exp2f(m - mx); m = mx;
#pragma unroll
            for (int e = 0; e < 16; ++e) { O[e] *= al; S[e] -= mx; }
            pv_tile(S, Va, Vb, O);
        }
        oa = (f4){O[0], O[1], O[2], O[3]}; ob = (f4){O[4], O[5], O[6], O[7]}; den = O[8];
    }
    const float rd = __builtin_amdgcn_rcpf(den);
    st16((u4){pk2(lo2(oa) * rd), pk2(hi2(oa) * rd), pk2(lo2(ob) * rd), pk2(hi2(ob) * rd)}, l16, rsF, sOw);
    if (w == 4) FLAG(WS_F2 + (U64 * 8 + fb) * 128);
    STAMP(26);
    const unsigned ta0 = (4 * h + q4) * 128 + 64 * (q4 >> 1) + 32 * (g4 & 1) + 8 * pp, ta1 = ta0 ^ 64;
    f16 acc0 = {}, acc1 = {};
    {
        const unsigned sO = WS_O + (2 * U64 + j) * 32768u + hh * 8192;
        const unsigned oa0 = L_A + hh * 16384 + ta0, oa1 = L_A + hh * 16384 + ta1;
        u4 A0[8], A1[8], B[8];
#pragma unroll
        for (int i = 0; i < 8; ++i) { A0[i] = ldtr(oa0 + i * 2048, oa0 + i * 2048 + 1024); A1[i] = ldtr(oa1 + i * 2048, oa1 + i * 2048 + 1024); }
        if (w == 7 && UNLIKELY(any_lane(ck[0] != tag[0] || ck[1] != tag[1]))) POLL(vc1);
        STAMP(37);
        if (w == 4) BAR_VM(1); else BAR_VM(0);
        STAMP(5);
#pragma unroll
        for (int i = 0; i < 8; ++i) ld16(B[i], l16, rsF, sO + i * 1024);
#define P3_PAIR(i, NW, M0, M1) do { asm volatile("s_waitcnt vmcnt(%2)" : "+v"(B[i]), "+v"(B[(i) + 1]) : "i"(NW) : "memory"); \
            if (UNLIKELY(any_lane(max(B[i][3], B[(i) + 1][3]) == ~0u))) { \
                _Pragma("unroll 1") for (int s_ = 0; s_ < SPIN_CAP; ++s_) { NRETRY(); __builtin_amdgcn_s_sleep(3); asm volatile("s_waitcnt vmcnt(0)" ::: "memory"); unsigned m_ = 0; \
                    _Pragma("unroll") for (int q = (i); q < 8; ++q) ld16(B[q], l16, rsF, sO + q * 1024); \
                    _Pragma("unroll") for (int q = (i); q < 8; ++q) { WAITV1(7 - q, B[q]); m_ = max(m_, B[q][3]); } \
                    if (!any_lane(m_ == ~0u)) break; } } if ((i) == 0) STAMP(4); \
            acc0 = MFMA32(A0[i], B[i], acc0); acc1 = MFMA32(A1[i], B[i], acc1); M0; SBAR(); acc0 = MFMA32(A0[(i) + 1], B[(i) + 1], acc0); acc1 = MFMA32(A1[(i) + 1], B[(i) + 1], acc1); M1; SBAR(); } while (0)
        P3_PAIR(0, 6, NO, NO); P3_PAIR(2, 4, GU(0, 0), GU(0, 1)); P3_PAIR(4, 4, GU(0, 2), GU(3, 0)); P3_PAIR(6, 4, GU(3, 1), GU(3, 2));
    }
    const unsigned vc2 = WS_F2 + (g * 32 + t32) * 128;
    ld8(ck, w == 7 ? vc2 : OOB, rsF, 0);
    STAMP(27);
    const unsigned rw0 = L_A + (hh * 2 + j) * 8192 + h * 512 + t32 * 16, rw1 = rw0 ^ 128, rr = L_A + j * 8192 + u8 * 1024 + (((8 * hh + l7) ^ ((u8 & 1) << 3)) * 16);
    auto reduce = [&](f4& lo, f4& hi) {
#pragma unroll
        for (int u = 0; u < 4; ++u) {
            sts16f(((u & 1) ? rw1 : rw0) + u * 1024, (f4){acc0[4 * u], acc0[4 * u + 1], acc0[4 * u + 2], acc0[4 * u + 3]});
            sts16f(((u & 1) ? rw1 : rw0) + (4 + u) * 1024, (f4){acc1[4 * u], acc1[4 * u + 1], acc1[4 * u + 2], acc1[4 * u + 3]});
        }
        BAR_L();
        lo = (lds16f(rr) + lds16f(rr + 16384)) + (lds16f(rr + 32768) + lds16f(rr + 49152));
        hi = (lds16f(rr + 512) + lds16f(rr + 16896)) + (lds16f(rr + 33280) + lds16f(rr + 49664));
    };
    u4 Hres;
    const unsigned sG = WS_G + (8 * g + w) * 88064u + 3 * r * 1024;
#define SG(u) (3 * r + (u) < 86 ? sG + (u) * 1024 : OOB)
    const unsigned ga = (8 * h + q4) * 192 + (g4 & 1) * 96 + 8 * pp;
    f16 cs[3] = {};
    {
        f4 lo, hi; reduce(lo, hi);
        STAMP(28);
        Hres = (u4){pk2(lo2(lo) + (f2){blo(xres[0]), bhi(xres[0])}), pk2(hi2(lo) + (f2){blo(xres[1]), bhi(xres[1])}), pk2(lo2(hi) + (f2){blo(xres[2]), bhi(xres[2])}), pk2(hi2(hi) + (f2){blo(xres[3]), bhi(xres[3])})};
        st16(Hres, vh, rsF, sHw);
        f2 s1 = {0.f, 0.f}, s2 = {0.f, 0.f};
#pragma unroll
        for (int e = 0; e < 4; ++e) { const f2 a = {blo(Hres[e]), bhi(Hres[e])}; s1 += a; s2 += a * a; }
        const float t1 = add_x32(add_x16(add_x8(s1[0] + s1[1]))), t2 = add_x32(add_x16(add_x8(s2[0] + s2[1])));
        st8((u2){uf(t1), uf(t2)}, vs, rsF, sSw);
        STAMP(33);
        asm volatile("s_waitcnt vmcnt(2) lgkmcnt(0)\n\ts_barrier" : "+v"(ck) :: "memory");
        STAMP(40);
        const unsigned o = ga + w * 12288;
        u4 a[12];
#pragma unroll
        for (int i = 0; i < 12; ++i) a[i] = ldtr(o + (i / 3) * 3072 + (i % 3) * 32, o + (i / 3) * 3072 + (i % 3) * 32 + 768);
        SBAR();
#pragma unroll
        for (int i = 0; i < 12; ++i) cs[i % 3] = MFMA32(a[i], ones, cs[i % 3]);
        asm volatile("" : "+v"(cs[0]), "+v"(cs[1]), "+v"(cs[2]));
    }
    STAMP(36);
    const u4 rsWd = mk_rsrc(wd + fb * 128u, 1376u * 1024u - fb * 128u);
    if (t32 == 0) {
#pragma unroll
        for (int u = 0; u < 3; ++u)
#pragma unroll
            for (int qd = 0; qd < 4; ++qd) sts16f(L_CSP + w * 384 + u * 128 + qd * 32 + h * 16, (f4){cs[u][4 * qd], cs[u][4 * qd + 1], cs[u][4 * qd + 2], cs[u][4 * qd + 3]});
    }
    if (w == 7 && UNLIKELY(any_lane(ck[0] != tag[0] || ck[1] != tag[1]))) POLL(vc2);
    BAR_VM(0);
    STAMP(6);
    const unsigned d2l = hh * 6144 + j * 3072, d2s = (352 * hh + 112 + 24 * j) * 1024;
#define WD2(R, i) dma_bufi<(R) * 24576 + (i) * 1024, (R) * 49152 + (i) * 8192>(d2l, vrow, rsWd, d2s)
    {
        const unsigned sH = WS_H + (8 * g + w) * 32768u, sS = WS_ST + (8 * g + w) * 2048u;
#define PD4 6
        constexpr int PD = PD4;
        u4 Hb[32], A0[3], A1[3], A2[4];
        u2 st[4];
        f16 ag[3] = {};
        const unsigned cf = L_CF + w * 384, vstat = h * 1024 + t32 * 8;
#pragma unroll
        for (int i = 0; i < PD; ++i) ld16(Hb[kgo(i)], l16, rsF, sH + kgo(i) * 1024);
        if (l < 48) { f2 s = {0.f, 0.f};
#pragma unroll
            for (int i = 0; i < 8; ++i) s += *(LAS const f2*)(L_CSP + i * 384 + l * 8);
            sts8f(cf + l * 8, s); }
        A0[0] = ldtr(ga, ga + 768); A1[0] = ldtr(ga + 32, ga + 800); A0[1] = ldtr(ga + kgo(1) * 3072, ga + kgo(1) * 3072 + 768); A1[1] = ldtr(ga + kgo(1) * 3072 + 32, ga + kgo(1) * 3072 + 800);
        unsigned mx = 0;
#define P4_STEP(i) do { constexpr int c = (i) % 3, n = ((i) + 2) % 3, k = kgo(i); const unsigned o = ga + ((i) < 30 ? kgo((i) + 2) * 3072 : 0); \
            if ((i) + PD < 32) ld16(Hb[kgo((i) + PD < 32 ? (i) + PD : 0)], l16, rsF, sH + kgo((i) + PD) * 1024); \
            else if ((i) + PD < 36) ld8(st[((i) + PD - 32) & 3], vstat, rsF, sS + ((i) + PD - 32) * 256); \
            WAITV1((i) + PD < 36 ? PD : 35 - (i), Hb[k]); \
            if ((i) < 8) FRESH16(Hb[k], l16, rsF, sH + k * 1024); else { mx = max(mx, Hb[k][3]); asm volatile("" : "+v"(mx)); } if ((i) == 0) STAMP(7); SBAR(); \
            ag[0] = MFMA32(A0[c], Hb[k], ag[0]); if ((i) < 30) A0[n] = ldtr(o, o + 768); else A2[((i) - 30) & 1] = ldtr(ga + 64 + (((i) - 30) & 1) * 3072, ga + 832 + (((i) - 30) & 1) * 3072); SBAR(); \
            ag[1] = MFMA32(A1[c], Hb[k], ag[1]); if ((i) < 30) A1[n] = ldtr(o + 32, o + 800); else if ((i) == 31) A2[2] = ldtr(ga + 64 + 6144, ga + 832 + 6144); SBAR(); } while (0)
#define P4_4(i) P4_STEP(i); P4_STEP((i) + 1); P4_STEP((i) + 2); P4_STEP((i) + 3)
        P4_4(0); P4_4(4); P4_4(8); P4_4(12); P4_4(16); P4_4(20); P4_4(24); P4_4(28);
        WAITV4(0, st[0], st[1], st[2], st[3]);
        if (UNLIKELY(any_lane(max(max(mx, max(st[0][1], st[1][1])), max(st[2][1], st[3][1])) == ~0u))) {
#pragma unroll
            for (int i = 0; i < 32; ++i) FRESH16(Hb[i], l16, rsF, sH + i * 1024);
#pragma unroll 1
            for (int s_ = 0; s_ < SPIN_CAP; ++s_) { NRETRY();
#pragma unroll
                for (int i = 0; i < 4; ++i) ld8(st[i], vstat, rsF, sS + i * 256);
                WAITV4(0, st[0], st[1], st[2], st[3]);
                if (!any_lane(max(max(st[0][1], st[1][1]), max(st[2][1], st[3][1])) == ~0u)) break;
            }
            ag[0] = z16; ag[1] = z16;
#pragma unroll
            for (int i = 0; i < 32; ++i) { const unsigned o = ga + kgo(i) * 3072; ag[0] = MFMA32(ldtr(o, o + 768), Hb[kgo(i)], ag[0]); ag[1] = MFMA32(ldtr(o + 32, o + 800), Hb[kgo(i)], ag[1]); }
        }
        STAMP(8);
        const float t1 = add_x32(fu(st[0].x) + fu(st[1].x) + fu(st[2].x) + fu(st[3].x)), t2 = add_x32(fu(st[0].y) + fu(st[1].y) + fu(st[2].y) + fu(st[3].y));
        float nm = t1 * (-1.f / 512), ve = __builtin_fmaxf(t2 * (1.f / 512) - nm * nm, 0.f) + 1e-5f;
        const float rs = __builtin_amdgcn_rsqf(ve);
        float ka = rs * -2.3022081986f, kb = rs * rs * rs * -0.1029432397f;
        asm volatile("" : "+v"(nm), "+v"(ve), "+v"(ka), "+v"(kb));
        const unsigned d1l = L_A + hh * 14336 + 7 * j * 1024, d1s = (352 * hh + 56 * j) * 1024;
#define WD1(i) dma_bufi<(i) * 1024, (i) * 8192>(d1l, vrow, rsWd, d1s)
        f4 cg, cu; float g0, g1, u0, u1, z0, z1; u4 Gy;
#define P2_(a, b) asm volatile("" : "+v"(a), "+v"(b))
#define GE_C(u, qd) do { cg = lds16f(cf + (u) * 128 + (qd) * 32 + h * 16); cu = lds16f(cf + (u) * 128 + (2 + (qd)) * 32 + h * 16); } while (0)
#define GE_0(u, pr) do { g0 = __builtin_fmaf(cg[2 * ((pr) & 1)], nm, ag[u][2 * (pr)]); u0 = __builtin_fmaf(cu[2 * ((pr) & 1)], nm, ag[u][8 + 2 * (pr)]); z0 = g0 * g0; P2_(g0, u0); \
            g1 = __builtin_fmaf(cg[2 * ((pr) & 1) + 1], nm, ag[u][2 * (pr) + 1]); P2_(z0, g1); u1 = __builtin_fmaf(cu[2 * ((pr) & 1) + 1], nm, ag[u][9 + 2 * (pr)]); z1 = g1 * g1; P2_(u1, z1); } while (0)
#define GE_1() do { z0 = __builtin_fmaf(z0, kb, ka); P2_(z0, z1); z1 = __builtin_fmaf(z1, kb, ka); z0 *= g0; P2_(z0, z1); z1 *= g1; z0 = __builtin_amdgcn_exp2f(z0); P2_(z0, z1); z1 = __builtin_amdgcn_exp2f(z1); P2_(z0, z1); } while (0)
#define GE_2() do { u0 *= g0; z0 = __builtin_fmaf(z0, ve, ve); P2_(u0, z0); u1 *= g1; z1 = __builtin_fmaf(z1, ve, ve); z0 = __builtin_amdgcn_rcpf(z0); P2_(u1, z0); P2_(z0, z1); } while (0)
#define GE_3(pr) do { z1 = __builtin_amdgcn_rcpf(z1); u0 *= z0; P2_(u0, z1); u1 *= z1; P2_(u0, u1); unsigned y_ = pk(u0, u1); asm volatile("" : "+v"(y_)); Gy[pr] = y_; } while (0)
        GE_C(0, 0);
#define P4B_STEP(i) do { constexpr int c = (i) & 3, n = ((i) + 3) & 3, p = (i) >> 2, u = p >> 2, pr = p & 3; const unsigned o = ga + ((i) + 3) * 3072 + 64; \
            if ((i) == 14) BAR_L(); SBAR(); \
            ag[2] = MFMA32(A2[c], Hb[i], ag[2]); if ((i) < 29) A2[n] = ldtr(o, o + 768); SBAR(); \
            if (((i) & 3) == 0) GE_0(u, pr); if (((i) & 3) == 1) { GE_1(); if ((pr & 1) && p < 7) GE_C((p + 1) >> 2, (((p + 1) >> 1) & 1)); } if (((i) & 3) == 2) GE_2(); \
            if (((i) & 3) == 3) { GE_3(pr); if (pr == 3) st16(Gy, l16, rsF, SG(u)); } \
            if ((i) < 14 && !((i) & 1)) WD1(((i) >> 1) & 7); \
            if ((i) >= 17 && (i) < 29 && ((i) & 1)) WD2((((i) + 7) >> 1) / 3 & 1, (((i) + 7) >> 1) % 3); SBAR(); } while (0)
#define P4B_4(i) P4B_STEP(i); P4B_STEP((i) + 1); P4B_STEP((i) + 2); P4B_STEP((i) + 3)
        P4B_4(0); P4B_4(4); P4B_4(8); P4B_4(12); P4B_4(16); P4B_4(20); P4B_4(24); P4B_4(28);
        STAMP(41);
#pragma unroll
        for (int pr = 0; pr < 4; ++pr) {
            if (!(pr & 1)) GE_C(2, pr >> 1);
            const f2 a = (f2){ag[2][2 * pr], ag[2][2 * pr + 1]} + ((pr & 1) ? hi2(cg) : lo2(cg)) * nm, c = (f2){ag[2][8 + 2 * pr], ag[2][9 + 2 * pr]} + ((pr & 1) ? hi2(cu) : lo2(cu)) * nm;
            const f2 z = a * (a * a * kb + ka), d = (f2){__builtin_amdgcn_exp2f(z[0]), __builtin_amdgcn_exp2f(z[1])} * ve + ve;
            Gy[pr] = pk2(a * c * (f2){__builtin_amdgcn_rcpf(d[0]), __builtin_amdgcn_rcpf(d[1])});
        }
        st16(Gy, l16, rsF, SG(2));
        STAMP(30);
    }
    BAR_VM(9);
    STAMP(9);
    {
        u4 D0[7], D1[7];
#pragma unroll
        for (int i = 0; i < 7; ++i) { const unsigned o = L_A + hh * 14336 + i * 2048; D0[i] = ldtr(o + ta0, o + ta0 + 1024); D1[i] = ldtr(o + ta1, o + ta1 + 1024); }
        WD2(2, 0); WD2(2, 1); WD2(2, 2); WD2(3, 0); WD2(3, 1); WD2(3, 2);
        const unsigned d3l = L_A + hh * 6144 + 3 * j * 1024, d3s = (352 * hh + 304 + 24 * j) * 1024;
#define WD3(i) dma_bufi<(i) * 1024, (i) * 8192>(d3l, vrow, rsWd, d3s)
        BAR_VM(6);
        STAMP(29);
        if (w == 4) FLAG(WS_F3 + (g * 32 + r) * 128);
#define PSLEEP 6
        if (w == 7) { const unsigned vc = WS_F3 + (xc * 32 + t32) * 128; __builtin_amdgcn_s_sleep(PSLEEP); POLL(vc); }
        if (w == 6 && b == 0) {
#pragma unroll 1
            for (unsigned i = 0; i < 4; ++i) { const unsigned vc = WS_F1 + (64 * i + l) * 128; POLL(vc); }
            FLAG(0u);
        }
        BAR_L();
        STAMP(46);
        constexpr int PD = 6;
        u4 Gb[22], A0[2], A1[2];
        const unsigned gs = WS_G + (2 * U64 + j) * 88064u + 22 * hh * 1024, gz = hh == 3 ? OOB : gs, o0 = hh * 6144 + ta0, o1 = hh * 6144 + ta1, p0 = L_A + hh * 6144 + ta0, p1 = L_A + hh * 6144 + ta1;
#pragma unroll
        for (int i = 0; i < PD; ++i) ld16(Gb[i], l16, rsF, gs + i * 1024);
        WD3(0); WD3(1); WD3(2);
        acc0 = z16; acc1 = z16;
#define P5_STEP(i) do { constexpr int c = (i) & 1, n = c ^ 1; \
            if ((i) + PD < 22) ld16(Gb[(i) + PD < 22 ? (i) + PD : 0], l16, rsF, ((i) + PD >= 20 ? gz : gs) + ((i) + PD) * 1024); \
            WAITV1(((i) + PD < 22 ? PD : 21 - (i)) + ((i) < PD ? 3 : 0), Gb[i]); if ((i) == 0) STAMP(10); \
            if ((i) == 6) BAR_L(); SBAR(); \
            constexpr int m_ = (i) >= 6 ? (i) - 6 : 0; constexpr unsigned q_ = (m_ / 3) * 24576 + (m_ % 3) * 2048; \
            const unsigned a0_ = (i) < 18 ? o0 + q_ : p0 + ((i) - 18) * 2048, a1_ = (i) < 18 ? o1 + q_ : p1 + ((i) - 18) * 2048; \
            acc0 = MFMA32((i) < 7 ? D0[(i) < 7 ? (i) : 0] : A0[c], Gb[i], acc0); if ((i) >= 6 && (i) < 21) A0[n] = ldtr(a0_, a0_ + 1024); SBAR(); \
            acc1 = MFMA32((i) < 7 ? D1[(i) < 7 ? (i) : 0] : A1[c], Gb[i], acc1); if ((i) >= 6 && (i) < 21) A1[n] = ldtr(a1_, a1_ + 1024); SBAR(); } while (0)
        P5_STEP(0); P5_STEP(1); P5_STEP(2); P5_STEP(3); P5_STEP(4); P5_STEP(5); P5_STEP(6); P5_STEP(7); P5_STEP(8); P5_STEP(9); P5_STEP(10);
        P5_STEP(11); P5_STEP(12); P5_STEP(13); P5_STEP(14); P5_STEP(15); P5_STEP(16); P5_STEP(17); P5_STEP(18); P5_STEP(19); P5_STEP(20); P5_STEP(21);
        }
    STAMP(11);
    BAR_L();
    {
        f4 lo, hi; reduce(lo, hi);
        const u4 Ores = {pk2(lo2(lo) + (f2){blo(Hres[0]), bhi(Hres[0])}), pk2(hi2(lo) + (f2){blo(Hres[1]), bhi(Hres[1])}), pk2(lo2(hi) + (f2){blo(Hres[2]), bhi(Hres[2])}), pk2(hi2(hi) + (f2){blo(Hres[3]), bhi(Hres[3])})};
        st16_plain(Ores, vres, mk_rsrc(out + U64 * 65536u, 65536u), 0);
    }
    asm volatile(".Lfb_code1:");
    STAMP(12);
#endif
}

#define WSOFF 0
extern "C" void kernel_launch(void* const* d_in, const int* in_sizes, int n_in, void* d_out, int out_size, void* d_ws, size_t ws_size, hipStream_t stream) {
    (void)in_sizes; (void)n_in; (void)out_size; (void)ws_size;
    (void)hipFuncSetAttribute((const void*)fused_block, hipFuncAttributeMaxDynamicSharedMemorySize, LDS_BYTES);
    fused_block<<<256, 512, LDS_BYTES, stream>>>((const char*)d_in[0], (const char*)d_in[1], (const char*)d_in[2], (const char*)d_in[4], (const char*)d_in[5], (const char*)d_in[6],
                                                 (char*)d_out, (char*)d_ws + WSOFF);
}
#define LASTSTAMP 12
#define STAMP_NAMES "start", "P1 loop end", "KV drained", 0, "O first data", "O drained", "h drained", "h first data", "P4 pass1 end", "P4 end bar", "G first data", "P5 loop end", "end", 0, 0, 0, \
  "st0", "st1", "st2", "st3", "st4", "st5", "st6", "st7", "KV first data", "att done", "O stored", "P3 loop end", "P3 reduced", "G drained", "G stored", 0, "KV stored", "h stored", \
  "LN sums", "pre KV bar", "colsum done", "A read", 0, "setup done", "GU bar", "P4 pass2 end", "att blk3", "att blk4", "att blk0", "comb bar", "G flags"
```

```cpp
#define XB 8
#include <hip/hip_runtime.h>
#include <hip/hip_bf16.h>
#include <stdint.h>
#pragma clang diagnostic ignored "-Wint-to-pointer-cast"

typedef __bf16 bf8 __attribute__((ext_vector_type(8)));
typedef __bf16 bf2 __attribute__((ext_vector_type(2)));
typedef short s4 __attribute__((ext_vector_type(4)));
typedef short s8 __attribute__((ext_vector_type(8)));
typedef float f2 __attribute__((ext_vector_type(2)));
typedef float f4 __attribute__((ext_vector_type(4)));
typedef float f16 __attribute__((ext_vector_type(16)));
typedef unsigned u2 __attribute__((ext_vector_type(2)));
typedef unsigned u4 __attribute__((ext_vector_type(4)));
typedef unsigned long long u64;
#define LAS __attribute__((address_space(3)))
#define DEV __device__ __forceinline__

constexpr unsigned WS_F1 = 32768, WS_F2 = 65536, WS_F3 = 98304, WS_F4 = 131072;
constexpr unsigned WS_KV = 1u << 20;
constexpr unsigned WS_O = 5u << 20;
constexpr unsigned WS_H = 7u << 20;
constexpr unsigned WS_ST = 9u << 20;
constexpr unsigned WS_G = 10u << 20;
constexpr unsigned WS_DIAG = 32u << 20;
constexpr unsigned LDS_BYTES = 163840;
constexpr unsigned L_HOLE = 73728;
constexpr unsigned L_OC = L_HOLE;
constexpr unsigned L_A = 98304;
constexpr unsigned L_CSP = 155648, L_CF = L_CSP + 3072;
constexpr int SPIN_CAP = 1 << 14;
constexpr unsigned OOB = 0x40000000u;

#define MFMA16(a, b, c) __builtin_amdgcn_mfma_f32_16x16x32_bf16(__builtin_bit_cast(bf8, a), __builtin_bit_cast(bf8, b), c, 0, 0, 0)
#define MFMA32(a, b, c) __builtin_amdgcn_mfma_f32_32x32x16_bf16(__builtin_bit_cast(bf8, a), __builtin_bit_cast(bf8, b), c, 0, 0, 0)
#define SBAR() __builtin_amdgcn_sched_barrier(0)
#define BAR_VM(N) asm volatile("s_waitcnt vmcnt(%0) lgkmcnt(0)\n\ts_barrier" ::"i"(N) : "memory")
#define BAR_L() asm volatile("s_waitcnt lgkmcnt(0)\n\ts_barrier" ::: "memory")

DEV u4 mk_rsrc(const void* p, unsigned bytes) { const u64 a = (u64)p; return (u4){(unsigned)a, (unsigned)(a >> 32) & 0xffffu, bytes, 0x00020000u}; }
DEV void dma_buf(unsigned lds, unsigned voff, u4 rs, unsigned soff) {
    asm volatile("s_mov_b32 m0, %0\n\ts_nop 0\n\tbuffer_load_dwordx4 %1, %2, %3 offen lds" ::"s"(lds), "v"(voff), "s"(rs), "s"(soff) : "memory"); }
template <unsigned LI, unsigned SI> DEV void dma_bufi(unsigned lds, unsigned voff, u4 rs, unsigned soff) {
    unsigned t; asm volatile("s_add_u32 m0, %1, %5\n\ts_add_u32 %0, %4, %6\n\tbuffer_load_dwordx4 %2, %3, %0 offen lds" : "=&s"(t) : "s"(lds), "v"(voff), "s"(rs), "s"(soff), "i"(LI), "i"(SI) : "memory", "scc"); }
template <unsigned LI> DEV void dma_glbi(unsigned lds, const char* p) {
    asm volatile("s_add_u32 m0, %0, %2\n\ts_nop 0\n\tglobal_load_lds_dwordx4 %1, off" ::"s"(lds), "v"(p), "i"(LI) : "memory", "scc"); }
DEV void dma_glb(unsigned lds, const char* p) {
    asm volatile("s_mov_b32 m0, %0\n\ts_nop 0\n\tglobal_load_lds_dwordx4 %1, off" ::"s"(lds), "v"(p) : "memory"); }
DEV void ld16(u4& d, unsigned voff, u4 rs, unsigned soff) {
    asm volatile("buffer_load_dwordx4 %0, %1, %2, %3 offen sc1" : "=v"(d) : "v"(voff), "s"(rs), "s"(soff) : "memory"); }
DEV void ld16_plain(u4& d, unsigned voff, u4 rs, unsigned soff) {
    asm volatile("buffer_load_dwordx4 %0, %1, %2, %3 offen" : "=v"(d) : "v"(voff), "s"(rs), "s"(soff) : "memory"); }
template <int OFF> DEV void ld16_imm(u4& d, unsigned voff, u4 rs) {
    asm volatile("buffer_load_dwordx4 %0, %1, %2, 0 offen offset:%3" : "=v"(d) : "v"(voff), "s"(rs), "i"(OFF) : "memory"); }
DEV void ld8(u2& d, unsigned voff, u4 rs, unsigned soff) {
    asm volatile("buffer_load_dwordx2 %0, %1, %2, %3 offen sc1" : "=v"(d) : "v"(voff), "s"(rs), "s"(soff) : "memory"); }
DEV void ld16_v(u4& d0, u4& d1, unsigned voff, u4 rs, unsigned soff) {
    asm volatile("s_mov_b64 exec, %5\n\tbuffer_load_dwordx4 %0, %2, %3, %4 offen offset:1024 sc1\n\tbuffer_load_dwordx4 %1, %2, %3, %4 offen offset:1536 sc1\n\ts_mov_b64 exec, -1"
                 : "+v"(d0), "+v"(d1) : "v"(voff), "s"(rs), "s"(soff), "s"(0x0000FFFF0000FFFFull) : "memory"); }
DEV void st16(u4 d, unsigned voff, u4 rs, unsigned soff) {
    asm volatile("buffer_store_dwordx4 %0, %1, %2, %3 offen sc1\n\ts_nop 1" ::"v"(d), "v"(voff), "s"(rs), "s"(soff) : "memory"); }
DEV void st16_plain(u4 d, unsigned voff, u4 rs, unsigned soff) {
    asm volatile("buffer_store_dwordx4 %0, %1, %2, %3 offen\n\ts_nop 1" ::"v"(d), "v"(voff), "s"(rs), "s"(soff) : "memory"); }
DEV void st8(u2 d, unsigned voff, u4 rs, unsigned soff) {
    asm volatile("buffer_store_dwordx2 %0, %1, %2, %3 offen sc1\n\ts_nop 1" ::"v"(d), "v"(voff), "s"(rs), "s"(soff) : "memory"); }
#define WAITV1(N, a) asm volatile("s_waitcnt vmcnt(%1)" : "+v"(a) : "i"(N) : "memory")
#define WAITV1D(ND, NS, a) asm volatile("s_waitcnt vmcnt(%1)\n\ts_cmp_lt_u32 %3, 4\n\ts_cbranch_scc1 .Lw%=\n\ts_waitcnt vmcnt(%2)\n.Lw%=:" : "+v"(a) : "i"(ND), "i"(NS), "s"(w) : "memory", "scc")
#define WAITV3(N, a, b, c) asm volatile("s_waitcnt vmcnt(%3)" : "+v"(a), "+v"(b), "+v"(c) : "i"(N) : "memory")
#define WAITV4(N, a, b, c, d) asm volatile("s_waitcnt vmcnt(%4)" : "+v"(a), "+v"(b), "+v"(c), "+v"(d) : "i"(N) : "memory")

DEV u4 lds16(unsigned a) { return *(LAS const u4*)a; }
DEV f4 lds16f(unsigned a) { return *(LAS const f4*)a; }
DEV float lds4f(unsigned a) { return *(LAS const float*)a; }
DEV void sts16(unsigned a, u4 v) { *(LAS u4*)a = v; }
DEV void sts16f(unsigned a, f4 v) { *(LAS f4*)a = v; }
DEV void sts8f(unsigned a, f2 v) { *(LAS f2*)a = v; }
DEV void sts4f(unsigned a, float v) { *(LAS float*)a = v; }
DEV u4 ldtr(unsigned a0, unsigned a1) {
    const s4 lo = __builtin_amdgcn_ds_read_tr16_b64_v4i16((LAS s4*)a0), hi = __builtin_amdgcn_ds_read_tr16_b64_v4i16((LAS s4*)a1);
    return __builtin_bit_cast(u4, __builtin_shufflevector(lo, hi, 0, 1, 2, 3, 4, 5, 6, 7)); }
DEV unsigned pk2(f2 a) { return __builtin_bit_cast(unsigned, __builtin_convertvector(a, bf2)); }
DEV f2 lo2(f4 a) { return (f2){a[0], a[1]}; }
DEV f2 hi2(f4 a) { return (f2){a[2], a[3]}; }
DEV unsigned pk(float a, float b) { return __builtin_bit_cast(unsigned, __builtin_convertvector((f2){a, b}, bf2)); }
DEV float blo(unsigned v) { return __builtin_bit_cast(float, v << 16); }
DEV float bhi(unsigned v) { return __builtin_bit_cast(float, v & 0xffff0000u); }
DEV float fu(unsigned v) { return __builtin_bit_cast(float, v); }
DEV unsigned uf(float v) { return __builtin_bit_cast(unsigned, v); }
DEV float add_x32(float v) { auto r = __builtin_amdgcn_permlane32_swap(uf(v), uf(v), false, false); const unsigned a = r[0], b = r[1]; return fu(a) + fu(b); }
DEV float add_x16(float v) { auto r = __builtin_amdgcn_permlane16_swap(uf(v), uf(v), false, false); const unsigned a = r[0], b = r[1]; return fu(a) + fu(b); }
DEV float max_x32(float v) { auto r = __builtin_amdgcn_permlane32_swap(uf(v), uf(v), false, false); const unsigned a = r[0], b = r[1]; return __builtin_fmaxf(fu(a), fu(b)); }
template <int C> DEV float add_dpp(float v) { return v + fu(__builtin_amdgcn_update_dpp(0u, uf(v), C, 0xf, 0xf, true)); }
DEV float add_x8(float v) { return v + fu(__builtin_amdgcn_update_dpp(0u, uf(v), 0x128, 0xf, 0xf, true)); }
constexpr u64 diag_mask(int r) { u64 m = 0; for (int l = 0; l < 64; ++l) { const int key = 16 * ((r >> 2) & 1) + 8 * (l >> 5) + 4 * (r >> 3) + (r & 3); if (key <= (l & 31)) m |= 1ull << l; } return m; }
DEV float sel(u64 m, float t, float f) { return __builtin_amdgcn_inverse_ballot_w64(m) ? t : f; }

template <unsigned LDS> DEV void touch(unsigned voff, u4 rs, unsigned soff) {
    asm volatile("s_mov_b32 m0, %3\n\ts_nop 0\n\tbuffer_load_dword %0, %1, %2 offen lds" ::"v"(voff), "s"(rs), "s"(soff), "i"(LDS) : "memory"); }
typedef short sh2 __attribute__((ext_vector_type(2)));
constexpr int kgo(int i) { return i < 8 ? 4 * i : (i - 8) / 3 * 4 + (i - 8) % 3 + 1; }
#define RSLEEP 4
DEV bool any_lane(bool c) { return __builtin_amdgcn_ballot_w64(c) != 0; }
#define UNLIKELY(x) __builtin_expect(!!(x), 0)
#define NRETRY()
#define FRESH16(d, voff, rs, soff) do { if (UNLIKELY(any_lane((d)[3] == ~0u))) { \
        _Pragma("unroll 1") for (int s_ = 0; s_ < SPIN_CAP; ++s_) { NRETRY(); u4 t_; asm volatile("buffer_load_dwordx4 %0, %1, %2, %3 offen sc1\n\ts_waitcnt vmcnt(0)" : "=&v"(t_) : "v"(voff), "s"(rs), "s"(soff) : "memory"); \
            d = t_; if (!any_lane(t_[3] == ~0u)) break; __builtin_amdgcn_s_sleep(2); } } } while (0)
#define POLL(voff) do { \
        _Pragma("unroll 1") for (int s_ = 0; s_ < 4 * SPIN_CAP; ++s_) { u2 t_; asm volatile("buffer_load_dwordx2 %0, %1, %2, 0 offen sc1\n\ts_waitcnt vmcnt(0)" : "=&v"(t_) : "v"(voff), "s"(rsF) : "memory"); \
            if (!any_lane(t_[0] != tag[0] || t_[1] != tag[1])) break; NRETRY(); __builtin_amdgcn_s_sleep(RSLEEP); } } while (0)
#define FLAG(off) st8(tag, l == 0 ? (off) : OOB, rsF, 0)
#define STAMP(i)

__global__ __launch_bounds__(512, 2) void fused_block(const char* x_, const char* wqkv_, const char* wo_, const char* wg_, const char* wu_, const char* wd_, char* out_, char* ws_) {
#if defined(__HIP_DEVICE_COMPILE__)

    typedef u64 u64x8 __attribute__((ext_vector_type(8)));
    u64x8 ka; asm volatile("s_load_dwordx16 %0, %1, 0x0\n\ts_waitcnt lgkmcnt(0)" : "=s"(ka) : "s"(__builtin_amdgcn_kernarg_segment_ptr()) : "memory");

    const char *x = (const char*)ka[0], *wqkv = (const char*)ka[1], *wo = (const char*)ka[2], *wg = (const char*)ka[3], *wu = (const char*)ka[4], *wd = (const char*)ka[5]; char *out = (char*)ka[6], *ws = (char*)ka[7];
    const unsigned l = threadIdx.x & 63, w = __builtin_amdgcn_readfirstlane(threadIdx.x >> 6);
    const unsigned b = blockIdx.x, xc = b & 7, rk = b >> 3;
    const unsigned T64 = 8 * (xc >> 1) + (rk >> 2), hq = 4 * (xc & 1) + (rk & 3);
    STAMP(0);
    const unsigned u8 = l >> 3, l7 = l & 7;
    u4 xr[8];
    const u4 rsX = mk_rsrc(x + T64 * 65536u, 65536u);
    const unsigned vx = (8 * w + u8) * 1024 + l7 * 16;
    ld16_imm<0>(xr[0], vx, rsX); ld16_imm<128>(xr[1], vx, rsX); ld16_imm<256>(xr[2], vx, rsX); ld16_imm<384>(xr[3], vx, rsX);
    if (XB > 4) { ld16_imm<512>(xr[4], vx, rsX); ld16_imm<640>(xr[5], vx, rsX); }
    if (XB > 6) { ld16_imm<768>(xr[6], vx, rsX); ld16_imm<896>(xr[7], vx, rsX); }
    const u4 rsW = mk_rsrc(wqkv + hq * 128u, 512u * 3072u - hq * 128u);
    SBAR();
    const unsigned w3 = w & 3, wk2 = w3 * 2048;
    unsigned vw0 = (16 * w3 + u8) * 3072 + 32 * ((l7 >> 1) ^ ((l >> 4) & 1)) + 16 * (l7 & 1), vw1 = (vw0 + 24576) ^ 64;
#define SLOT(i) ((((i) + 1) & 3) * 24576u)
#define SP(i, p) dma_bufi<SLOT(i) + ((p) >> 1) * 8192 + ((p) & 1) * 1024, (i) * 196608u + ((p) >> 1) * 1024u>(wk2, ((p) & 1) ? vw1 : vw0, rsW, 0)
    const unsigned U64 = 4 * xc + (rk >> 3), fb = rk & 7;
    const unsigned g = 2 * (xc >> 1) + (rk >> 4), r = 16 * (xc & 1) + (rk & 15);
    const unsigned j = w & 1, hh = w >> 1;
    const unsigned t16 = l & 15, g4 = l >> 4, h = l >> 5, t32 = l & 31, q4 = t16 >> 2, pp = l & 3, l16 = l * 16, l128 = l * 128;
    const u4 rsF = mk_rsrc(ws, 16u << 20);
    const u4 ones = {0x3f803f80u, 0x3f803f80u, 0x3f803f80u, 0x3f803f80u};
    const unsigned kvb = WS_KV + (4 * hq + hh) * 131072u;
    const unsigned skv = kvb + (2 * T64 + j) * 2048;
    const unsigned sOw = WS_O + (2 * T64 + j) * 32768u + (4 * hq + hh) * 1024;
    u2 tag; u4 xres;
    unsigned vres = (32 * j + 8 * hh + l7) * 1024 + fb * 128 + u8 * 16;
    unsigned xw0 = L_A + (8 * w + u8) * 1024 + 16 * (l7 ^ (8 * (w & 1) + u8)), xw1 = xw0 ^ 128;
    unsigned xa[4];
#pragma unroll
    for (int q = 0; q < 4; ++q) { xa[q] = L_A + (32 * j + t16) * 1024 + 16 * ((4 * q + g4) ^ t16); asm volatile("" : "+v"(xa[q])); }
    unsigned wa = (8 * g4 + q4) * 128 + 32 * (hh ^ ((q4 >> 1) | ((g4 & 1) << 1))) + 8 * pp;
    f4 aq[2] = {}, ak[2] = {}, av[2] = {};
    unsigned vk = (h * 32 + (16 * ((t32 >> 3) & 1) + 8 * ((t32 >> 2) & 1) + 4 * (t32 >> 4) + (t32 & 3))) * 16, vv_ = (h * 16 + t16) * 16;
    unsigned vst = 1024 + ((g4 & 1) * 32 + (g4 >> 1) * 16 + t16) * 16;
    unsigned vrow = u8 * 1024 + 16 * (l7 ^ (((l >> 4) & 1) << 2));
    const char* gp[3];
#pragma unroll
    for (int i = 0; i < 3; ++i) { const unsigned z = 64 * i + l, k = (z * 43691u) >> 19, c = z - 12 * k, m = c >= 6, cc = c - 6 * m; unsigned un = 3 * r + (cc >> 1); un = un > 85 ? 85 : un;
        gp[i] = (m ? wu : wg) + ((16 * w + k) * 2752 + un * 32 + (cc & 1) * 16); asm volatile("" : "+v"(gp[i])); }
    const unsigned wk3 = w * 3072;
#define GU(R, c) dma_glbi<(R) * 24576 + (c) * 1024>(wk3, gp[c] + (R) * 128 * 2752)
#define GUD(R, q) dma_glbi<(R) * 24576 + ((q) / 3) * 12288 + ((q) % 3) * 1024>(wk3, gp[(q) % 3] + ((R) * 128 + ((q) / 3) * 64) * 2752)
    asm volatile("" : "+v"(vw0), "+v"(vw1), "+v"(xw0), "+v"(xw1), "+v"(wa), "+v"(vres), "+v"(vk), "+v"(vv_), "+v"(vrow), "+v"(vst));
    STAMP(39);
    if (XB <= 4) { ld16_imm<512>(xr[4], vx, rsX); ld16_imm<640>(xr[5], vx, rsX); }
    if (XB <= 6) { ld16_imm<768>(xr[6], vx, rsX); ld16_imm<896>(xr[7], vx, rsX); }
#define TAMODE 0
    {
        const char* pc; unsigned nb; asm volatile("s_getpc_b64 %0\n.Lfb_code0:\n\ts_mov_b32 %1, .Lfb_code1-.Lfb_code0" : "=s"(pc), "=s"(nb));
        const unsigned co = ((rk * 8 + w) * 2 + (l & 1)) * 128;
        const char* p = TAMODE == 1 ? (l < 24 ? wqkv + (hq * 128 + (w * 64 + (T64 & 7) * 8 + l7) * 3072 + u8 * 1024) : pc + (co < nb ? co : 0))
                                    : (l < 6 ? wqkv + ((b * 8 + w) * 6 + l) * 128 : pc + (co < nb ? co : 0));
        asm volatile("global_load_dword v254, %0, off" ::"v"(p) : "memory", "v254"); }
#define NO ((void)0)
#define TBPOS 0
#define TOUCH_B() do { const char* tp = (w == 4 ? wo : w == 5 ? wg : w == 6 ? wu : wd) + (w == 4 ? b * 16 + (l & 15) : b * 43 + (l < 43 ? l : 0)) * 128; \
        asm volatile("global_load_dword v255, %0, off" ::"v"(tp) : "memory", "v255"); } while (0)
#if !defined(NOTB) && TBPOS == 0
    if (w >= 4) TOUCH_B();
#define XW 1
#else
#define XW 0
#endif
#define SSTAMP(i)
    {
        f2 xf[32], s1 = {0.f, 0.f}, s2 = {0.f, 0.f};
#define LN_SUM(i, N, D0, D1) do { if (XW && (N) < 8 - (i)) WAITV1((N) + 1, xr[i]); else WAITV1D((N) + 1, 8 - (i) + XW, xr[i]); _Pragma("unroll") for (int e = 0; e < 4; ++e) { const unsigned d_ = xr[i][e]; f2 v_ = {blo(d_), bhi(d_)}; asm volatile("" : "+v"(v_)); xf[4 * (i) + e] = v_; \
        s1 += v_; s2 += v_ * v_; } asm volatile("" : "+v"(s1), "+v"(s2)); if (w < 4) { D0; D1; } SBAR(); } while (0)
        LN_SUM(0, 7, NO, NO); LN_SUM(1, 6, NO, NO); LN_SUM(2, 5, NO, NO); LN_SUM(3, 4, NO, NO);
        LN_SUM(4, 3, SP(0, 0), SP(0, 1)); LN_SUM(5, 4, SP(0, 2), SP(0, 3)); LN_SUM(6, 5, SP(0, 4), NO); LN_SUM(7, 5, SP(0, 5), NO);
        SSTAMP(34);
        if (w < 4) { SP(1, 0); SP(1, 1); SP(1, 2); SP(1, 3); SP(1, 4); SP(1, 5); }
#if !defined(NOTB) && TBPOS == 1
        else TOUCH_B();
#endif
        const float t1 = add_dpp<0x141>(add_dpp<0x4e>(add_dpp<0xb1>(s1[0] + s1[1]))), t2 = add_dpp<0x141>(add_dpp<0x4e>(add_dpp<0xb1>(s2[0] + s2[1])));
        const float mu = t1 * (1.f / 512), rs = __builtin_amdgcn_rsqf(__builtin_fmaxf(t2 * (1.f / 512) - mu * mu, 0.f) + 1e-5f), mr = -mu * rs;
#pragma unroll
        for (int i = 0; i < 8; ++i) sts16(((i & 1) ? xw1 : xw0) + (i >> 1) * 256, (u4){pk2(xf[4 * i] * rs + mr), pk2(xf[4 * i + 1] * rs + mr), pk2(xf[4 * i + 2] * rs + mr), pk2(xf[4 * i + 3] * rs + mr)});
    }
#define P1_STEP(n, D0, D1, D2) do { constexpr int c_ = (n) & 1, x_ = c_ ^ 1; constexpr bool m_ = (n) < 15; \
        const unsigned xb = xa[((n) + 1) & 3] + (((n) + 1) >> 2) * 256, wb = wa + SLOT(((n) + 1) >> 1) + x_ * 4096; \
        if (m_) X0[x_] = lds16(xb); SBAR(); \
        aq[0] = MFMA16(Wq[c_], X0[c_], aq[0]); if (m_) Wq[x_] = ldtr(wb, wb + 512); SBAR(); \
        aq[1] = MFMA16(Wq[c_], X1[c_], aq[1]); if (m_) X1[x_] = lds16(xb + 16384); D0; SBAR(); \
        ak[0] = MFMA16(Wk[c_], X0[c_], ak[0]); if (m_) Wk[x_] = ldtr(wb + 8192, wb + 8704); SBAR(); \
        ak[1] = MFMA16(Wk[c_], X1[c_], ak[1]); D1; SBAR(); \
        av[0] = MFMA16(X0[c_], Wv[c_], av[0]); if (m_) Wv[x_] = ldtr(wb + 16384, wb + 16896); SBAR(); \
        av[1] = MFMA16(X1[c_], Wv[c_], av[1]); D2; SBAR(); } while (0)
#define P1_FIRST() do { const unsigned wb = wa + SLOT(0); X0[0] = lds16(xa[0]); Wq[0] = ldtr(wb, wb + 512); X1[0] = lds16(xa[0] + 16384); Wk[0] = ldtr(wb + 8192, wb + 8704); Wv[0] = ldtr(wb + 16384, wb + 16896); } while (0)
#define XRES() ld16_plain(xres, vres, mk_rsrc(x + U64 * 65536u, 65536u), 0)
    if (w < 4) {
        u4 X0[2], X1[2], Wq[2], Wk[2], Wv[2];
        SP(2, 0); SP(2, 1); SP(2, 2); SP(2, 3); SP(2, 4); SP(2, 5);
        BAR_VM(12); STAMP(16);  P1_FIRST(); P1_STEP(0, (SP(3, 0), SP(3, 1)), (SP(3, 2), SP(3, 3)), (SP(3, 4), SP(3, 5)));
        BAR_VM(12); SSTAMP(17); P1_STEP(1, SP(4, 0), SP(4, 1), SP(4, 2)); P1_STEP(2, SP(4, 3), SP(4, 4), SP(4, 5));
        BAR_VM(12); SSTAMP(18); P1_STEP(3, SP(5, 0), SP(5, 1), SP(5, 2)); P1_STEP(4, SP(5, 3), SP(5, 4), SP(5, 5));
        BAR_VM(12); SSTAMP(19); P1_STEP(5, SP(6, 0), SP(6, 1), SP(6, 2)); P1_STEP(6, SP(6, 3), SP(6, 4), SP(6, 5));
        BAR_VM(12); SSTAMP(20); P1_STEP(7, SP(7, 0), SP(7, 1), SP(7, 2)); P1_STEP(8, SP(7, 3), SP(7, 4), SP(7, 5));
        BAR_VM(12); SSTAMP(21); P1_STEP(9, XRES(), GUD(1, 0), NO); P1_STEP(10, GUD(1, 1), GUD(1, 2), NO);
        BAR_VM(10); SSTAMP(22); P1_STEP(11, GUD(1, 3), GUD(1, 4), NO); P1_STEP(12, GUD(1, 5), NO, NO);
        BAR_VM(7);  SSTAMP(23); P1_STEP(13, NO, NO, NO); P1_STEP(14, NO, NO, NO); P1_STEP(15, NO, NO, NO);
        WAITV1(6, xres); tag = (u2){0u, 0u};
    } else {
        u4 X0[2], X1[2], Wq[2], Wk[2], Wv[2];
        const u4 bad = {~0u, ~0u, ~0u, ~0u};
#define MK(v, s_) st16(bad, v, rsF, s_)
#if !defined(NOTB) && TBPOS == 2
#define TCH(c) if (w == (c)) TOUCH_B()
#else
#define TCH(c) NO
#endif
        BAR_L(); STAMP(16);  P1_FIRST(); P1_STEP(0, ld8(tag, 0u * l, rsF, 0), MK(l16, skv), TCH(5));
        BAR_L(); SSTAMP(17); P1_STEP(1, NO, MK(l16 + 1024, skv), TCH(6)); P1_STEP(2, NO, MK(l16, skv - 262144), NO);
        BAR_L(); SSTAMP(18); P1_STEP(3, NO, MK(l16 + 1024, skv - 262144), TCH(4)); P1_STEP(4, NO, MK(l16, sOw), NO);
        BAR_L(); SSTAMP(19); P1_STEP(5, NO, MK(l16, sOw - 2048), TCH(7)); P1_STEP(6, XRES(), NO, NO);
        BAR_L(); SSTAMP(20); P1_STEP(7, NO, NO, NO); P1_STEP(8, NO, NO, NO);
        BAR_L(); SSTAMP(21); P1_STEP(9, NO, NO, NO); P1_STEP(10, NO, NO, NO);
        asm volatile("s_waitcnt vmcnt(0)" : "+v"(tag), "+v"(xres) :: "memory");
        BAR_L(); SSTAMP(22);
        { const unsigned lo = tag.x + 1u; tag.y += (lo == 0u); tag.x = lo; }
                             P1_STEP(11, FLAG(w == 4 ? WS_F1 + (T64 * 8 + hq) * 128 : OOB), NO, NO); P1_STEP(12, NO, NO, NO);
        BAR_L(); SSTAMP(23); P1_STEP(13, NO, NO, NO); P1_STEP(14, NO, NO, NO); P1_STEP(15, NO, NO, NO);
    }
    STAMP(1);
    u4 Qf;
    {
        { auto a = __builtin_amdgcn_permlane16_swap(pk2(lo2(ak[0])), pk2(lo2(ak[1])), false, false); auto c = __builtin_amdgcn_permlane16_swap(pk2(hi2(ak[0])), pk2(hi2(ak[1])), false, false);
          st16((u4){a[0], c[0], a[1], c[1]}, l16, rsF, skv); }
        st16((u4){pk2(lo2(av[0])), pk2(hi2(av[0])), pk2(lo2(av[1])), pk2(hi2(av[1]))}, vst, rsF, skv);
        STAMP(32);
        SBAR();
        constexpr float QS = 0.25f * 1.4426950408889634f;
        { auto a = __builtin_amdgcn_permlane16_swap(pk2(lo2(aq[0]) * QS), pk2(lo2(aq[1]) * QS), false, false); auto c = __builtin_amdgcn_permlane16_swap(pk2(hi2(aq[0]) * QS), pk2(hi2(aq[1]) * QS), false, false);
          Qf = (u4){a[0], c[0], a[1], c[1]}; }
        sts16(L_OC + (w ^ 1) * 2304 + l16, Qf);
    }
    const int Tb = 2 * (int)T64 - 8;
    u4 K[5], V0[5], V1[5];
    const int blk[5] = {Tb + 8 + (int)j, Tb + (int)j, Tb + 2 + 3 * (int)j, Tb + 3 + 3 * (int)j, Tb + 4 + 3 * (int)j};
#pragma unroll
    for (int i = 0; i < 5; ++i) { const unsigned o = blk[i] < 0 ? 0u : 0x3f803f80u; V0[i] = (u4){o, o, o, o}; V1[i] = V0[i]; asm volatile("" : "+v"(V0[i]), "+v"(V1[i])); }
    unsigned pm[8];
    { const short uq = (short)(t32 - 8 * h);
#pragma unroll
      for (int i = 0; i < 8; ++i) { const short c = (short)(16 * ((i >> 1) & 1) + 4 * (i >> 2) + 2 * (i & 1)); pm[i] = __builtin_bit_cast(unsigned, ((sh2){uq, uq} - (sh2){c, (short)(c + 1)}) >> (sh2){15, 15}); asm volatile("" : "+v"(pm[i])); } }
    unsigned so[5];
#pragma unroll
    for (int i = 0; i < 5; ++i) so[i] = blk[i] < 0 ? OOB : kvb + (unsigned)blk[i] * 2048;
    if (w == 7) {
        const int Tp = (int)T64 - 1 - (int)(l & 3); const unsigned vc = WS_F1 + ((Tp < 0 ? T64 : (unsigned)Tp) * 8 + hq) * 128; POLL(vc); }
    STAMP(35);
    BAR_VM(0);
    STAMP(2);
#define KVLD(i) do { ld16(K[i], vk, rsF, so[i]); ld16_v(V0[i], V1[i], vv_, rsF, so[i]); } while (0)
#define KVOK(N, i) do { WAITV3(N, K[i], V0[i], V1[i]); \
        if (UNLIKELY(any_lane(max(max(K[i][3], V0[i][3]), V1[i][3]) == ~0u))) { \
            _Pragma("unroll 1") for (int s_ = 0; s_ < SPIN_CAP; ++s_) { NRETRY(); KVLD(i); WAITV3(0, K[i], V0[i], V1[i]); if (!any_lane(max(max(K[i][3], V0[i][3]), V1[i][3]) == ~0u)) break; __builtin_amdgcn_s_sleep(2); } } } while (0)
    KVLD(2); KVLD(3);
    const u4 Qm = Qf, Qo = lds16(L_OC + w * 2304 + l16);
    f16 Om = {}, Oo = {};
    const f16 z16 = {};
    auto pv_tile = [&](const f16& S, const u4& va, const u4& vb, f16& O) {
        float p[16];
#pragma unroll
        for (int e = 0; e < 16; ++e) p[e] = __builtin_amdgcn_exp2f(S[e]);
        const u4 P0 = {pk(p[0], p[1]), pk(p[2], p[3]), pk(p[4], p[5]), pk(p[6], p[7])}, P1 = {pk(p[8], p[9]), pk(p[10], p[11]), pk(p[12], p[13]), pk(p[14], p[15])};
        O = MFMA32(va, P0, O); O = MFMA32(vb, P1, O);
    };
    const u4 rsWo = mk_rsrc(wo + fb * 128u, 512u * 1024u - fb * 128u);
    const unsigned wol = L_A + w * 8192, wos = w * 65536;
    const unsigned vh = (u8 >> 1) * 1024 + ((u8 & 1) * 32 + 8 * hh + l7) * 16, vs = l < 8 ? (8 * hh + l) * 8 : OOB;
    const unsigned sHw = WS_H + (2 * U64 + j) * 32768u + fb * 4096, sSw = WS_ST + (2 * U64 + j) * 2048u + fb * 256;
    const unsigned sGa = r < 29 ? WS_G + (8 * g + w) * 90112u + r * 2048 : OOB, sGb = r < 28 ? WS_G + (8 * g + w) * 90112u + (60 + r) * 1024 : OOB;
#define WO_(i) dma_bufi<(i) * 1024, (i) * 8192>(wol, vrow, rsWo, wos)
#define PIN16(a) asm volatile("" : "+v"(a))
#define MEM(...) do { SBAR(); __VA_ARGS__; SBAR(); } while (0)
#define HALF(S, o, va, O) do { float p_[8]; _Pragma("unroll") for (int e = 0; e < 8; ++e) p_[e] = __builtin_amdgcn_exp2f(S[(o) + e]); \
        u4 q_ = {pk(p_[0], p_[1]), pk(p_[2], p_[3]), pk(p_[4], p_[5]), pk(p_[6], p_[7])}; asm volatile("" : "+v"(q_)); O = MFMA32(va, q_, O); PIN16(O); SBAR(); } while (0)
#define SCORE(S, k, q) do { S = MFMA32(k, q, z16); PIN16(S); SBAR(); } while (0)
    f16 Sa, Sb, Sc;
    u2 ck; const unsigned vc1 = WS_F1 + (U64 * 8 + l7) * 128;
    const u4 bad = {~0u, ~0u, ~0u, ~0u};
    KVOK(3, 2);
    STAMP(24);
    SCORE(Sa, K[2], Qm); SCORE(Sb, K[2], Qo); MEM(KVLD(4));
    HALF(Sa, 0, V0[2], Om); MEM(KVLD(0)); HALF(Sa, 8, V1[2], Om); MEM(KVLD(1)); KVOK(9, 3);
    SCORE(Sa, K[3], Qm); HALF(Sb, 0, V0[2], Oo); MEM(WO_(0)); HALF(Sb, 8, V1[2], Oo); MEM(WO_(1));
    SSTAMP(42);
    SCORE(Sb, K[3], Qo); HALF(Sa, 0, V0[3], Om); MEM(WO_(2)); HALF(Sa, 8, V1[3], Om); MEM(WO_(3)); KVOK(10, 4);
    SCORE(Sa, K[4], Qm); HALF(Sb, 0, V0[3], Oo); MEM(WO_(4)); HALF(Sb, 8, V1[3], Oo); MEM(WO_(5));
    SSTAMP(43);
    SCORE(Sb, K[4], Qo); HALF(Sa, 0, V0[4], Om); MEM(WO_(6)); HALF(Sa, 8, V1[4], Om); MEM(WO_(7)); KVOK(11, 0); KVOK(8, 1);
    if (j == 0) { SCORE(Sa, K[0], Qo); HALF(Sb, 0, V0[4], Oo); MEM(st16(bad, vh, rsF, sHw)); HALF(Sb, 8, V1[4], Oo); MEM(st8((u2){~0u, ~0u}, vs, rsF, sSw), st16(bad, l16, rsF, sGb));
                  SCORE(Sb, K[0], Qm); SCORE(Sc, K[1], Qm); HALF(Sa, 0, V0[0], Oo); HALF(Sa, 8, V1[0], Oo); }
    else        { SCORE(Sa, K[1], Qo); HALF(Sb, 0, V0[4], Oo); MEM(st16(bad, vh, rsF, sHw)); HALF(Sb, 8, V1[4], Oo); MEM(st8((u2){~0u, ~0u}, vs, rsF, sSw), st16(bad, l16, rsF, sGb));
                  SCORE(Sb, K[0], Qm); SCORE(Sc, K[1], Qm); HALF(Sa, 0, V0[1], Oo); HALF(Sa, 8, V1[1], Oo); }
    SSTAMP(44);
    MEM(ld8(ck, w == 7 ? vc1 : OOB, rsF, 0), GU(2, 0), GU(2, 1), GU(2, 2));
    const unsigned oc = L_OC + w * 2304;
    {
        const f16 Sx = Sb, Sy = Sc;
        float p[16];
#pragma unroll
        for (int e = 0; e < 8; ++e) p[e] = __builtin_amdgcn_exp2f(sel(diag_mask(e), Sx[e], Sy[e]));
        SBAR();
        sts16f(oc + l16, (f4){Oo[0], Oo[1], Oo[2], Oo[3]}); sts16f(oc + 1024 + l16, (f4){Oo[4], Oo[5], Oo[6], Oo[7]}); sts4f(oc + 2048 + l * 4, Oo[8]);
#pragma unroll
        for (int e = 8; e < 16; ++e) p[e] = __builtin_amdgcn_exp2f(sel(diag_mask(e), Sx[e], Sy[e]));
        unsigned P[8];
#pragma unroll
        for (int e = 0; e < 8; ++e) P[e] = pk(p[2 * e], p[2 * e + 1]);
        const u4 Y0 = {P[0] & pm[0], P[1] & pm[1], P[2] & pm[2], P[3] & pm[3]}, Y1 = {P[4] & pm[4], P[5] & pm[5], P[6] & pm[6], P[7] & pm[7]};
        const u4 X0_ = {P[0] ^ Y0[0], P[1] ^ Y0[1], P[2] ^ Y0[2], P[3] ^ Y0[3]}, X1_ = {P[4] ^ Y1[0], P[5] ^ Y1[1], P[6] ^ Y1[2], P[7] ^ Y1[3]};
        Om = MFMA32(V0[0], X0_, Om); Om = MFMA32(V1[0], X1_, Om); Om = MFMA32(V0[1], Y0, Om); Om = MFMA32(V1[1], Y1, Om);
    }
    SBAR();
    STAMP(25);
    asm volatile("s_waitcnt vmcnt(3) lgkmcnt(0)\n\ts_barrier" : "+v"(ck) :: "memory");
    SSTAMP(45);
    const unsigned op = L_OC + (w ^ 1) * 2304;
    f4 oa = lds16f(op + l16) + (f4){Om[0], Om[1], Om[2], Om[3]}, ob = lds16f(op + 1024 + l16) + (f4){Om[4], Om[5], Om[6], Om[7]};
    float den = Om[8] + lds4f(op + 2048 + l * 4);
    if (UNLIKELY(any_lane(!(den >= 0x1p-60f && den <= 0x1p60f)))) {
        f16 O = {}; float m = -__builtin_inff();
#pragma unroll 1
        for (int kb = 8; kb >= 0; --kb) {
            const int T = 2 * (int)T64 + (int)j - 8 + kb;
            if (T < 0) break;
            u4 Ks, Va = ones, Vb = ones;
            ld16(Ks, vk, rsF, kvb + (unsigned)T * 2048); ld16_v(Va, Vb, vv_, rsF, kvb + (unsigned)T * 2048);
            WAITV3(0, Ks, Va, Vb);
            f16 S = MFMA32(Ks, Qm, z16);
            const u64 all = (kb != 0 && kb != 8) ? ~0ull : 0ull, inv = kb == 0 ? ~0ull : 0ull;
            float mx = m;
#pragma unroll
            for (int e = 0; e < 16; ++e) { S[e] = sel((diag_mask(e) ^ inv) | all, S[e], -__builtin_inff()); mx = __builtin_fmaxf(mx, S[e]); }
            mx = max_x32(mx);
            const float al = __builtin_amdgcn_exp2f(m - mx); m = mx;
#pragma unroll
            for (int e = 0; e < 16; ++e) { O[e] *= al; S[e] -= mx; }
            pv_tile(S, Va, Vb, O);
        }
        oa = (f4){O[0], O[1], O[2], O[3]}; ob = (f4){O[4], O[5], O[6], O[7]}; den = O[8];
    }
    const float rd = __builtin_amdgcn_rcpf(den);
    st16((u4){pk2(lo2(oa) * rd), pk2(hi2(oa) * rd), pk2(lo2(ob) * rd), pk2(hi2(ob) * rd)}, l16, rsF, sOw);
    FLAG(w == 4 ? WS_F2 + (U64 * 8 + fb) * 128 : w == 5 ? WS_F3 + (g * 32 + r) * 128 : OOB);
    STAMP(26);
    const unsigned ta0 = (4 * h + q4) * 128 + 64 * (q4 >> 1) + 32 * (g4 & 1) + 8 * pp, ta1 = ta0 ^ 64;
    f16 acc0 = {}, acc1 = {};
    {
        const unsigned sO = WS_O + (2 * U64 + j) * 32768u + hh * 8192;
        const unsigned oa0 = L_A + hh * 16384 + ta0, oa1 = L_A + hh * 16384 + ta1;
        u4 A0[8], A1[8], B[8];
#pragma unroll
        for (int i = 0; i < 8; ++i) { A0[i] = ldtr(oa0 + i * 2048, oa0 + i * 2048 + 1024); A1[i] = ldtr(oa1 + i * 2048, oa1 + i * 2048 + 1024); }
        if (w == 7 && UNLIKELY(any_lane(ck[0] != tag[0] || ck[1] != tag[1]))) POLL(vc1);
        STAMP(37);
        BAR_VM(1);
        STAMP(5);
#pragma unroll
        for (int i = 0; i < 8; ++i) ld16(B[i], l16, rsF, sO + i * 1024);
#define P3_PAIR(i, NW, M0, M1) do { asm volatile("s_waitcnt vmcnt(%2)" : "+v"(B[i]), "+v"(B[(i) + 1]) : "i"(NW) : "memory"); \
            if (UNLIKELY(any_lane(max(B[i][3], B[(i) + 1][3]) == ~0u))) { \
                _Pragma("unroll 1") for (int s_ = 0; s_ < SPIN_CAP; ++s_) { NRETRY(); __builtin_amdgcn_s_sleep(3); asm volatile("s_waitcnt vmcnt(0)" ::: "memory"); unsigned m_ = 0; \
                    _Pragma("unroll") for (int q = (i); q < 8; ++q) ld16(B[q], l16, rsF, sO + q * 1024); \
                    _Pragma("unroll") for (int q = (i); q < 8; ++q) { WAITV1(7 - q, B[q]); m_ = max(m_, B[q][3]); } \
                    if (!any_lane(m_ == ~0u)) break; } } if ((i) == 0) STAMP(4); \
            acc0 = MFMA32(A0[i], B[i], acc0); acc1 = MFMA32(A1[i], B[i], acc1); M0; SBAR(); acc0 = MFMA32(A0[(i) + 1], B[(i) + 1], acc0); acc1 = MFMA32(A1[(i) + 1], B[(i) + 1], acc1); M1; SBAR(); } while (0)
        P3_PAIR(0, 6, NO, NO); P3_PAIR(2, 4, GU(0, 0), GU(0, 1)); P3_PAIR(4, 4, GU(0, 2), GU(3, 0)); P3_PAIR(6, 4, GU(3, 1), GU(3, 2));
    }
    const unsigned vc2 = WS_F2 + (g * 32 + t32) * 128;
    ld8(ck, w == 7 ? vc2 : OOB, rsF, 0);
    STAMP(27);
    const unsigned rw0 = L_A + (hh * 2 + j) * 8192 + h * 512 + t32 * 16, rw1 = rw0 ^ 128, rr = L_A + j * 8192 + u8 * 1024 + (((8 * hh + l7) ^ ((u8 & 1) << 3)) * 16);
    auto reduce = [&](f4& lo, f4& hi) {
#pragma unroll
        for (int u = 0; u < 4; ++u) {
            sts16f(((u & 1) ? rw1 : rw0) + u * 1024, (f4){acc0[4 * u], acc0[4 * u + 1], acc0[4 * u + 2], acc0[4 * u + 3]});
            sts16f(((u & 1) ? rw1 : rw0) + (4 + u) * 1024, (f4){acc1[4 * u], acc1[4 * u + 1], acc1[4 * u + 2], acc1[4 * u + 3]});
        }
        BAR_L();
        lo = (lds16f(rr) + lds16f(rr + 16384)) + (lds16f(rr + 32768) + lds16f(rr + 49152));
        hi = (lds16f(rr + 512) + lds16f(rr + 16896)) + (lds16f(rr + 33280) + lds16f(rr + 49664));
    };
    u4 Hres;
    const unsigned ga = (8 * h + q4) * 192 + (g4 & 1) * 96 + 8 * pp;
    f16 cs[3] = {};
    {
        f4 lo, hi; reduce(lo, hi);
        STAMP(28);
        Hres = (u4){pk2(lo2(lo) + (f2){blo(xres[0]), bhi(xres[0])}), pk2(hi2(lo) + (f2){blo(xres[1]), bhi(xres[1])}), pk2(lo2(hi) + (f2){blo(xres[2]), bhi(xres[2])}), pk2(hi2(hi) + (f2){blo(xres[3]), bhi(xres[3])})};
        st16(Hres, vh, rsF, sHw);
        f2 s1 = {0.f, 0.f}, s2 = {0.f, 0.f};
#pragma unroll
        for (int e = 0; e < 4; ++e) { const f2 a = {blo(Hres[e]), bhi(Hres[e])}; s1 += a; s2 += a * a; }
        const float t1 = add_x32(add_x16(add_x8(s1[0] + s1[1]))), t2 = add_x32(add_x16(add_x8(s2[0] + s2[1])));
        st8((u2){uf(t1), uf(t2)}, vs, rsF, sSw);
        STAMP(33);
        asm volatile("s_waitcnt vmcnt(2) lgkmcnt(0)\n\ts_barrier" : "+v"(ck) :: "memory");
        STAMP(40);
        const unsigned o = ga + w * 12288;
        u4 a[12];
#pragma unroll
        for (int i = 0; i < 12; ++i) a[i] = ldtr(o + (i / 3) * 3072 + (i % 3) * 32, o + (i / 3) * 3072 + (i % 3) * 32 + 768);
        SBAR();
#pragma unroll
        for (int i = 0; i < 12; ++i) cs[i % 3] = MFMA32(a[i], ones, cs[i % 3]);
        asm volatile("" : "+v"(cs[0]), "+v"(cs[1]), "+v"(cs[2]));
    }
    STAMP(36);
    const u4 rsWd = mk_rsrc(wd + fb * 128u, 1376u * 1024u - fb * 128u);
    if (t32 == 0) {
#pragma unroll
        for (int u = 0; u < 3; ++u)
#pragma unroll
            for (int qd = 0; qd < 4; ++qd) sts16f(L_CSP + w * 384 + u * 128 + qd * 32 + h * 16, (f4){cs[u][4 * qd], cs[u][4 * qd + 1], cs[u][4 * qd + 2], cs[u][4 * qd + 3]});
    }
    if (w == 7 && UNLIKELY(any_lane(ck[0] != tag[0] || ck[1] != tag[1]))) POLL(vc2);
    if (w == 5 && b == 0) {
#pragma unroll 1
        for (int s_ = 0; s_ < 4 * SPIN_CAP; ++s_) { u2 f0, f1, f2_, f3; const unsigned vc = WS_F1 + l * 128;
            ld8(f0, vc, rsF, 0); ld8(f1, vc, rsF, 8192); ld8(f2_, vc, rsF, 16384); ld8(f3, vc, rsF, 24576); WAITV4(0, f0, f1, f2_, f3);
            if (!any_lane(((f0[0] ^ tag[0]) | (f1[0] ^ tag[0]) | (f2_[0] ^ tag[0]) | (f3[0] ^ tag[0]) | (f0[1] ^ tag[1]) | (f1[1] ^ tag[1]) | (f2_[1] ^ tag[1]) | (f3[1] ^ tag[1])) != 0u)) break; }
        FLAG(0u);
    }
    BAR_VM(0);
    STAMP(6);
    const unsigned a0 = 15 * hh, a1 = a0 + 1, jo = j * 8192;
    const unsigned wde = (48 * (a0 >> 1) + 16 * (a0 & 1)) * 1024 + jo, wdo = (48 * (a1 >> 1) + 16 * (a1 & 1)) * 1024 + jo, wdt = (336 * hh + 32) * 1024 + jo;
    const unsigned wla = L_A + hh * 14336 + j * 1024, wlb = hh * 12288 + j * 1024, wlc = 49152 + hh * 4096 + j * 1024;
#define WD(i) dma_bufi<((i) < 14 ? (i) % 7 : (i) < 20 ? (i) - 14 : (i) - 20) * 2048u, ((i) < 15 ? (i) >> 1 : (i) - 15) * 49152u>((i) < 14 ? wla : (i) < 20 ? wlb : wlc, vrow, rsWd, (i) >= 15 ? wdt : ((i) & 1) ? wdo : wde)
    u4 D0[7], D1[7];
    {
        const unsigned sH = WS_H + (8 * g + w) * 32768u, sS = WS_ST + (8 * g + w) * 2048u;
#define PD4 6
        constexpr int PD = PD4;
        u4 Hb[32], A0[3], A1[3], A2[4];
        u2 st[4];
        f16 ag[3] = {};
        const unsigned cf = L_CF + w * 384, vstat = h * 1024 + t32 * 8;
#pragma unroll
        for (int i = 0; i < PD; ++i) ld16(Hb[kgo(i)], l16, rsF, sH + kgo(i) * 1024);
        if (l < 48) { f2 s = {0.f, 0.f};
#pragma unroll
            for (int i = 0; i < 8; ++i) s += *(LAS const f2*)(L_CSP + i * 384 + l * 8);
            sts8f(cf + l * 8, s); }
        A0[0] = ldtr(ga, ga + 768); A1[0] = ldtr(ga + 32, ga + 800); A0[1] = ldtr(ga + kgo(1) * 3072, ga + kgo(1) * 3072 + 768); A1[1] = ldtr(ga + kgo(1) * 3072 + 32, ga + kgo(1) * 3072 + 800);
        unsigned mx = 0;
#define P4_STEP(i) do { constexpr int c = (i) % 3, n = ((i) + 2) % 3, k = kgo(i); const unsigned o = ga + ((i) < 30 ? kgo((i) + 2) * 3072 : 0); \
            if ((i) + PD < 32) ld16(Hb[kgo((i) + PD < 32 ? (i) + PD : 0)], l16, rsF, sH + kgo((i) + PD) * 1024); \
            else if ((i) + PD < 36) ld8(st[((i) + PD - 32) & 3], vstat, rsF, sS + ((i) + PD - 32) * 256); \
            WAITV1((i) + PD < 36 ? PD : 35 - (i), Hb[k]); \
            if ((i) < 8) FRESH16(Hb[k], l16, rsF, sH + k * 1024); else { mx = max(mx, Hb[k][3]); asm volatile("" : "+v"(mx)); } if ((i) == 0) STAMP(7); SBAR(); \
            ag[0] = MFMA32(A0[c], Hb[k], ag[0]); if ((i) < 30) A0[n] = ldtr(o, o + 768); else A2[((i) - 30) & 1] = ldtr(ga + 64 + (((i) - 30) & 1) * 3072, ga + 832 + (((i) - 30) & 1) * 3072); SBAR(); \
            ag[1] = MFMA32(A1[c], Hb[k], ag[1]); if ((i) < 30) A1[n] = ldtr(o + 32, o + 800); else if ((i) == 31) A2[2] = ldtr(ga + 64 + 6144, ga + 832 + 6144); SBAR(); } while (0)
#define P4_4(i) P4_STEP(i); P4_STEP((i) + 1); P4_STEP((i) + 2); P4_STEP((i) + 3)
        P4_4(0); P4_4(4); P4_4(8); P4_4(12); P4_4(16); P4_4(20); P4_4(24); P4_4(28);
        WAITV4(0, st[0], st[1], st[2], st[3]);
        if (UNLIKELY(any_lane(max(max(mx, max(st[0][1], st[1][1])), max(st[2][1], st[3][1])) == ~0u))) {
#pragma unroll
            for (int i = 0; i < 32; ++i) FRESH16(Hb[i], l16, rsF, sH + i * 1024);
#pragma unroll 1
            for (int s_ = 0; s_ < SPIN_CAP; ++s_) { NRETRY();
#pragma unroll
                for (int i = 0; i < 4; ++i) ld8(st[i], vstat, rsF, sS + i * 256);
                WAITV4(0, st[0], st[1], st[2], st[3]);
                if (!any_lane(max(max(st[0][1], st[1][1]), max(st[2][1], st[3][1])) == ~0u)) break;
            }
            ag[0] = z16; ag[1] = z16;
#pragma unroll
            for (int i = 0; i < 32; ++i) { const unsigned o = ga + kgo(i) * 3072; ag[0] = MFMA32(ldtr(o, o + 768), Hb[kgo(i)], ag[0]); ag[1] = MFMA32(ldtr(o + 32, o + 800), Hb[kgo(i)], ag[1]); }
        }
        STAMP(8);
        const float t1 = add_x32(fu(st[0].x) + fu(st[1].x) + fu(st[2].x) + fu(st[3].x)), t2 = add_x32(fu(st[0].y) + fu(st[1].y) + fu(st[2].y) + fu(st[3].y));
        const float nm = t1 * (-1.f / 512), ve = __builtin_fmaxf(t2 * (1.f / 512) - nm * nm, 0.f) + 1e-5f, rs = __builtin_amdgcn_rsqf(ve);
        const float ka_ = rs * -2.3022081986f, kb_ = rs * rs * rs * -0.1029432397f;
#define G_UNIT(u, so_, M0, M1) do { u4 y; _Pragma("unroll") for (int qd = 0; qd < 2; ++qd) { \
                const f4 cg = lds16f(cf + (u) * 128 + qd * 32 + h * 16), cu = lds16f(cf + (u) * 128 + (2 + qd) * 32 + h * 16); \
                _Pragma("unroll") for (int e = 0; e < 2; ++e) { \
                    const f2 gt = (f2){ag[u][4 * qd + 2 * e], ag[u][4 * qd + 2 * e + 1]} + (e ? hi2(cg) : lo2(cg)) * nm, up = (f2){ag[u][8 + 4 * qd + 2 * e], ag[u][9 + 4 * qd + 2 * e]} + (e ? hi2(cu) : lo2(cu)) * nm; \
                    const f2 z = gt * (gt * gt * kb_ + ka_), d = (f2){__builtin_amdgcn_exp2f(z[0]), __builtin_amdgcn_exp2f(z[1])} * ve + ve; \
                    unsigned y_ = pk2(gt * up * (f2){__builtin_amdgcn_rcpf(d[0]), __builtin_amdgcn_rcpf(d[1])}); asm volatile("" : "+v"(y_)); y[2 * qd + e] = y_; } \
                if (qd == 0) MEM(M0); } \
            MEM(st16(y, l16, rsF, so_); M1); } while (0)
        MEM(WD(0), WD(1), WD(2));
        G_UNIT(0, sGa, (WD(3), WD(4), WD(5), WD(6)), NO); G_UNIT(1, sGa + 1024, NO, NO);
        STAMP(47);
        const unsigned da0 = L_A + hh * 14336 + ta0, da1 = L_A + hh * 14336 + ta1;
        const unsigned vc3 = WS_F3 + ((U64 >> 2) * 32 + t32) * 128, vc4 = vc3 + (WS_F4 - WS_F3);
#define B1 6
#define WDS(i, m) if ((i) == B1 + 8 + (B1 + 21 <= 31 ? (m) : (m) * (24 - B1) / 13)) WD(7 + (m))
#define P4B_STEP(i) do { constexpr int c = (i) & 3, n = ((i) + 3) & 3; const unsigned o = ga + ((i) + 3) * 3072 + 64; \
            if ((i) == B1) SSTAMP(48); if ((i) == B1 + 1) SSTAMP(49); if ((i) == B1 + 8) SSTAMP(50); if ((i) == B1 + 9) SSTAMP(51); if ((i) == B1 + 15) SSTAMP(52); if ((i) == B1 + 21) SSTAMP(53); \
            if ((i) == B1) BAR_VM(2);         \
            if ((i) == B1 + 8) { BAR_VM(0); FLAG(w == 4 ? WS_F4 + (g * 32 + r) * 128 : OOB); }        SBAR(); \
            ag[2] = MFMA32(A2[c], Hb[i], ag[2]); if ((i) < 29) A2[n] = ldtr(o, o + 768); \
            if ((i) >= B1 && (i) < B1 + 7) { constexpr int q = ((i) + 14 - B1) % 7; D0[q] = ldtr(da0 + q * 2048, da0 + q * 2048 + 1024); D1[q] = ldtr(da1 + q * 2048, da1 + q * 2048 + 1024); } SBAR(); \
            WDS(i, 0); WDS(i, 1); WDS(i, 2); WDS(i, 3); WDS(i, 4); WDS(i, 5); WDS(i, 6); WDS(i, 7); WDS(i, 8); WDS(i, 9); WDS(i, 10); WDS(i, 11); WDS(i, 12); \
            if ((i) == (B1 + 21 < 31 ? B1 + 21 : 31)) ld8(ck, w == 7 ? vc3 : w == 6 ? vc4 : OOB, rsF, 0); SBAR(); } while (0)
#define P4B_4(i) P4B_STEP(i); P4B_STEP((i) + 1); P4B_STEP((i) + 2); P4B_STEP((i) + 3)
        P4B_4(0); P4B_4(4); P4B_4(8); P4B_4(12); P4B_4(16); P4B_4(20); P4B_4(24); P4B_4(28);
        STAMP(41);
        G_UNIT(2, sGb, NO, NO);
        STAMP(30);
        WAITV1(1, ck);
        if (w >= 6 && UNLIKELY(any_lane(ck[0] != tag[0] || ck[1] != tag[1]))) { const unsigned vc = w == 7 ? vc3 : vc4; POLL(vc); }
    }
    BAR_L();
    STAMP(9);
    {
        constexpr int PD = 6;
        u4 Gb[22], A0[2], A1[2];
        const unsigned gs = WS_G + (2 * U64 + j) * 90112u + 15 * hh * 1024, gz = hh == 3 ? OOB : gs, gt = WS_G + (2 * U64 + j) * 90112u + (60 + 7 * hh - 15) * 1024;
        const unsigned o0 = L_A + hh * 14336 + ta0, o1 = L_A + hh * 14336 + ta1, p0 = hh * 12288 + ta0, p1 = hh * 12288 + ta1, q0 = 49152 + hh * 4096 + ta0, q1 = 49152 + hh * 4096 + ta1;
#define GOFF(i) (((i) >= 15 ? gt : (i) >= 13 ? gz : gs) + (i) * 1024)
#pragma unroll
        for (int i = 0; i < PD; ++i) ld16(Gb[i], l16, rsF, GOFF(i));
        WD(20); WD(21);
        acc0 = z16; acc1 = z16;
#define P5_STEP(i) do { constexpr int c = (i) & 1, n = c ^ 1; \
            if ((i) + PD < 22) ld16(Gb[(i) + PD < 22 ? (i) + PD : 0], l16, rsF, GOFF((i) + PD)); \
            WAITV1(((i) + PD < 22 ? PD : 21 - (i)) + ((i) < PD ? 2 : 0), Gb[i]); if ((i) >= 15) FRESH16(Gb[i], l16, rsF, GOFF(i)); if ((i) == 0) STAMP(10); \
            if ((i) == 12) BAR_L(); SBAR(); \
            const unsigned a0_ = (i) < 13 ? o0 + ((i) - 6) * 2048 : (i) < 19 ? p0 + ((i) - 13) * 2048 : q0 + ((i) - 19) * 2048, a1_ = (i) < 13 ? o1 + ((i) - 6) * 2048 : (i) < 19 ? p1 + ((i) - 13) * 2048 : q1 + ((i) - 19) * 2048; \
            acc0 = MFMA32((i) < 7 ? D0[(i) < 7 ? (i) : 0] : A0[c], Gb[i], acc0); if ((i) >= 6 && (i) < 21) A0[n] = ldtr(a0_, a0_ + 1024); SBAR(); \
            acc1 = MFMA32((i) < 7 ? D1[(i) < 7 ? (i) : 0] : A1[c], Gb[i], acc1); if ((i) >= 6 && (i) < 21) A1[n] = ldtr(a1_, a1_ + 1024); SBAR(); } while (0)
        P5_STEP(0); P5_STEP(1); P5_STEP(2); P5_STEP(3); P5_STEP(4); P5_STEP(5); P5_STEP(6); P5_STEP(7); P5_STEP(8); P5_STEP(9); P5_STEP(10);
        P5_STEP(11); P5_STEP(12); P5_STEP(13); P5_STEP(14); P5_STEP(15); P5_STEP(16); P5_STEP(17); P5_STEP(18); P5_STEP(19); P5_STEP(20); P5_STEP(21);
    }
    STAMP(11);
    BAR_L();
    {
        f4 lo, hi; reduce(lo, hi);
        const u4 Ores = {pk2(lo2(lo) + (f2){blo(Hres[0]), bhi(Hres[0])}), pk2(hi2(lo) + (f2){blo(Hres[1]), bhi(Hres[1])}), pk2(lo2(hi) + (f2){blo(Hres[2]), bhi(Hres[2])}), pk2(hi2(hi) + (f2){blo(Hres[3]), bhi(Hres[3])})};
        st16_plain(Ores, vres, mk_rsrc(out + U64 * 65536u, 65536u), 0);
    }
    asm volatile(".Lfb_code1:");
    STAMP(12);
#endif
}

#define WSOFF 0
extern "C" void kernel_launch(void* const* d_in, const int* in_sizes, int n_in, void* d_out, int out_size, void* d_ws, size_t ws_size, hipStream_t stream) {
    (void)in_sizes; (void)n_in; (void)out_size; (void)ws_size;
    (void)hipFuncSetAttribute((const void*)fused_block, hipFuncAttributeMaxDynamicSharedMemorySize, LDS_BYTES);
    fused_block<<<256, 512, LDS_BYTES, stream>>>((const char*)d_in[0], (const char*)d_in[1], (const char*)d_in[2], (const char*)d_in[4], (const char*)d_in[5], (const char*)d_in[6],
                                                 (char*)d_out, (char*)d_ws + WSOFF);
}
#define LASTSTAMP 12
#define STAMP_NAMES "start", "P1 loop end", "KV drained", 0, "O first data", "O drained", "h drained", "h first data", "P4 pass1 end", "P4 end bar", "G first data", "P5 loop end", "end", 0, 0, 0, \
  "st0", "st1", "st2", "st3", "st4", "st5", "st6", "st7", "KV first data", "att done", "O stored", "P3 loop end", "P3 reduced", 0, "G stored", 0, "KV stored", "h stored", \
  "LN sums", "pre KV bar", "colsum done", "A read", 0, "setup done", "GU bar", "P4 pass2 end", "att blk3", "att blk4", "att blk0", "comb bar", 0, "G01 stored", "p2 pre B1", "p2 s7", "p2 pre B2", "p2 s15", "p2 s21", "p2 s27"
```

```cpp
#define XB 8
#include <hip/hip_runtime.h>
#include <hip/hip_bf16.h>
#include <stdint.h>
#pragma clang diagnostic ignored "-Wint-to-pointer-cast"

typedef __bf16 bf8 __attribute__((ext_vector_type(8)));
typedef __bf16 bf2 __attribute__((ext_vector_type(2)));
typedef short s4 __attribute__((ext_vector_type(4)));
typedef short s8 __attribute__((ext_vector_type(8)));
typedef float f2 __attribute__((ext_vector_type(2)));
typedef float f4 __attribute__((ext_vector_type(4)));
typedef float f16 __attribute__((ext_vector_type(16)));
typedef unsigned u2 __attribute__((ext_vector_type(2)));
typedef unsigned u4 __attribute__((ext_vector_type(4)));
typedef unsigned long long u64;
#define LAS __attribute__((address_space(3)))
#define DEV __device__ __forceinline__

constexpr unsigned WS_F1 = 32768, WS_F2 = 65536, WS_F3 = 98304, WS_F4 = 131072;
constexpr unsigned WS_KV = 1u << 20;
constexpr unsigned WS_O = 5u << 20;
constexpr unsigned WS_H = 7u << 20;
constexpr unsigned WS_ST = 9u << 20;
constexpr unsigned WS_G = 10u << 20;
constexpr unsigned WS_DIAG = 32u << 20;
constexpr unsigned LDS_BYTES = 163840;
constexpr unsigned L_HOLE = 73728;
constexpr unsigned L_OC = L_HOLE;
constexpr unsigned L_A = 98304;
constexpr unsigned L_CSP = 155648, L_CF = L_CSP + 3072;
constexpr int SPIN_CAP = 1 << 14;
constexpr unsigned OOB = 0x40000000u;

#define MFMA16(a, b, c) __builtin_amdgcn_mfma_f32_16x16x32_bf16(__builtin_bit_cast(bf8, a), __builtin_bit_cast(bf8, b), c, 0, 0, 0)
#define MFMA32(a, b, c) __builtin_amdgcn_mfma_f32_32x32x16_bf16(__builtin_bit_cast(bf8, a), __builtin_bit_cast(bf8, b), c, 0, 0, 0)
#define SBAR() __builtin_amdgcn_sched_barrier(0)
#define BAR_VM(N) asm volatile("s_waitcnt vmcnt(%0) lgkmcnt(0)\n\ts_barrier" ::"i"(N) : "memory")
#define BAR_L() asm volatile("s_waitcnt lgkmcnt(0)\n\ts_barrier" ::: "memory")

DEV u4 mk_rsrc(const void* p, unsigned bytes) { const u64 a = (u64)p; return (u4){(unsigned)a, (unsigned)(a >> 32) & 0xffffu, bytes, 0x00020000u}; }
DEV void dma_buf(unsigned lds, unsigned voff, u4 rs, unsigned soff) {
    asm volatile("s_mov_b32 m0, %0\n\ts_nop 0\n\tbuffer_load_dwordx4 %1, %2, %3 offen lds" ::"s"(lds), "v"(voff), "s"(rs), "s"(soff) : "memory"); }
template <unsigned LI, unsigned SI> DEV void dma_bufi(unsigned lds, unsigned voff, u4 rs, unsigned soff) {
    unsigned t; asm volatile("s_add_u32 m0, %1, %5\n\ts_add_u32 %0, %4, %6\n\tbuffer_load_dwordx4 %2, %3, %0 offen lds" : "=&s"(t) : "s"(lds), "v"(voff), "s"(rs), "s"(soff), "i"(LI), "i"(SI) : "memory", "scc"); }
template <unsigned LI> DEV void dma_glbi(unsigned lds, const char* p) {
    asm volatile("s_add_u32 m0, %0, %2\n\ts_nop 0\n\tglobal_load_lds_dwordx4 %1, off" ::"s"(lds), "v"(p), "i"(LI) : "memory", "scc"); }
DEV void dma_glb(unsigned lds, const char* p) {
    asm volatile("s_mov_b32 m0, %0\n\ts_nop 0\n\tglobal_load_lds_dwordx4 %1, off" ::"s"(lds), "v"(p) : "memory"); }
DEV void ld16(u4& d, unsigned voff, u4 rs, unsigned soff) {
    asm volatile("buffer_load_dwordx4 %0, %1, %2, %3 offen sc1" : "=v"(d) : "v"(voff), "s"(rs), "s"(soff) : "memory"); }
DEV void ld16_plain(u4& d, unsigned voff, u4 rs, unsigned soff) {
    asm volatile("buffer_load_dwordx4 %0, %1, %2, %3 offen" : "=v"(d) : "v"(voff), "s"(rs), "s"(soff) : "memory"); }
template <int OFF> DEV void ld16_imm(u4& d, unsigned voff, u4 rs) {
    asm volatile("buffer_load_dwordx4 %0, %1, %2, 0 offen offset:%3" : "=v"(d) : "v"(voff), "s"(rs), "i"(OFF) : "memory"); }
DEV void ld8(u2& d, unsigned voff, u4 rs, unsigned soff) {
    asm volatile("buffer_load_dwordx2 %0, %1, %2, %3 offen sc1" : "=v"(d) : "v"(voff), "s"(rs), "s"(soff) : "memory"); }
DEV void ld16_v(u4& d0, u4& d1, unsigned voff, u4 rs, unsigned soff) {
    asm volatile("s_mov_b64 exec, %5\n\tbuffer_load_dwordx4 %0, %2, %3, %4 offen offset:1024 sc1\n\tbuffer_load_dwordx4 %1, %2, %3, %4 offen offset:1536 sc1\n\ts_mov_b64 exec, -1"
                 : "+v"(d0), "+v"(d1) : "v"(voff), "s"(rs), "s"(soff), "s"(0x0000FFFF0000FFFFull) : "memory"); }
DEV void st16(u4 d, unsigned voff, u4 rs, unsigned soff) {
    asm volatile("buffer_store_dwordx4 %0, %1, %2, %3 offen sc1\n\ts_nop 1" ::"v"(d), "v"(voff), "s"(rs), "s"(soff) : "memory"); }
DEV void st16_plain(u4 d, unsigned voff, u4 rs, unsigned soff) {
    asm volatile("buffer_store_dwordx4 %0, %1, %2, %3 offen\n\ts_nop 1" ::"v"(d), "v"(voff), "s"(rs), "s"(soff) : "memory"); }
DEV void st8(u2 d, unsigned voff, u4 rs, unsigned soff) {
    asm volatile("buffer_store_dwordx2 %0, %1, %2, %3 offen sc1\n\ts_nop 1" ::"v"(d), "v"(voff), "s"(rs), "s"(soff) : "memory"); }
#define WAITV1(N, a) asm volatile("s_waitcnt vmcnt(%1)" : "+v"(a) : "i"(N) : "memory")
#define WAITV1D(ND, NS, a) asm volatile("s_waitcnt vmcnt(%1)\n\ts_cmp_lt_u32 %3, 4\n\ts_cbranch_scc1 .Lw%=\n\ts_waitcnt vmcnt(%2)\n.Lw%=:" : "+v"(a) : "i"(ND), "i"(NS), "s"(w) : "memory", "scc")
#define WAITV3(N, a, b, c) asm volatile("s_waitcnt vmcnt(%3)" : "+v"(a), "+v"(b), "+v"(c) : "i"(N) : "memory")
#define WAITV4(N, a, b, c, d) asm volatile("s_waitcnt vmcnt(%4)" : "+v"(a), "+v"(b), "+v"(c), "+v"(d) : "i"(N) : "memory")

DEV u4 lds16(unsigned a) { return *(LAS const u4*)a; }
DEV f4 lds16f(unsigned a) { return *(LAS const f4*)a; }
DEV float lds4f(unsigned a) { return *(LAS const float*)a; }
DEV void sts16(unsigned a, u4 v) { *(LAS u4*)a = v; }
DEV void sts16f(unsigned a, f4 v) { *(LAS f4*)a = v; }
DEV void sts8f(unsigned a, f2 v) { *(LAS f2*)a = v; }
DEV void sts4f(unsigned a, float v) { *(LAS float*)a = v; }
DEV u4 ldtr(unsigned a0, unsigned a1) {
    const s4 lo = __builtin_amdgcn_ds_read_tr16_b64_v4i16((LAS s4*)a0), hi = __builtin_amdgcn_ds_read_tr16_b64_v4i16((LAS s4*)a1);
    return __builtin_bit_cast(u4, __builtin_shufflevector(lo, hi, 0, 1, 2, 3, 4, 5, 6, 7)); }
DEV unsigned pk2(f2 a) { return __builtin_bit_cast(unsigned, __builtin_convertvector(a, bf2)); }
DEV f2 lo2(f4 a) { return (f2){a[0], a[1]}; }
DEV f2 hi2(f4 a) { return (f2){a[2], a[3]}; }
DEV unsigned pk(float a, float b) { return __builtin_bit_cast(unsigned, __builtin_convertvector((f2){a, b}, bf2)); }
DEV float blo(unsigned v) { return __builtin_bit_cast(float, v << 16); }
DEV float bhi(unsigned v) { return __builtin_bit_cast(float, v & 0xffff0000u); }
DEV float fu(unsigned v) { return __builtin_bit_cast(float, v); }
DEV unsigned uf(float v) { return __builtin_bit_cast(unsigned, v); }
DEV float add_x32(float v) { auto r = __builtin_amdgcn_permlane32_swap(uf(v), uf(v), false, false); const unsigned a = r[0], b = r[1]; return fu(a) + fu(b); }
DEV float add_x16(float v) { auto r = __builtin_amdgcn_permlane16_swap(uf(v), uf(v), false, false); const unsigned a = r[0], b = r[1]; return fu(a) + fu(b); }
DEV float max_x32(float v) { auto r = __builtin_amdgcn_permlane32_swap(uf(v), uf(v), false, false); const unsigned a = r[0], b = r[1]; return __builtin_fmaxf(fu(a), fu(b)); }
template <int C> DEV float add_dpp(float v) { return v + fu(__builtin_amdgcn_update_dpp(0u, uf(v), C, 0xf, 0xf, true)); }
DEV float add_x8(float v) { return v + fu(__builtin_amdgcn_update_dpp(0u, uf(v), 0x128, 0xf, 0xf, true)); }
constexpr u64 diag_mask(int r) { u64 m = 0; for (int l = 0; l < 64; ++l) { const int key = 16 * ((r >> 2) & 1) + 8 * (l >> 5) + 4 * (r >> 3) + (r & 3); if (key <= (l & 31)) m |= 1ull << l; } return m; }
DEV float sel(u64 m, float t, float f) { return __builtin_amdgcn_inverse_ballot_w64(m) ? t : f; }

template <unsigned LDS> DEV void touch(unsigned voff, u4 rs, unsigned soff) {
    asm volatile("s_mov_b32 m0, %3\n\ts_nop 0\n\tbuffer_load_dword %0, %1, %2 offen lds" ::"v"(voff), "s"(rs), "s"(soff), "i"(LDS) : "memory"); }
typedef short sh2 __attribute__((ext_vector_type(2)));
constexpr int kgo(int i) { return i < 8 ? 4 * i : (i - 8) / 3 * 4 + (i - 8) % 3 + 1; }
#define RSLEEP 4
DEV bool any_lane(bool c) { return __builtin_amdgcn_ballot_w64(c) != 0; }
#define UNLIKELY(x) __builtin_expect(!!(x), 0)
#define NRETRY()
#define FRESH16(d, voff, rs, soff) do { if (UNLIKELY(any_lane((d)[3] == ~0u))) { \
        _Pragma("unroll 1") for (int s_ = 0; s_ < SPIN_CAP; ++s_) { NRETRY(); u4 t_; asm volatile("buffer_load_dwordx4 %0, %1, %2, %3 offen sc1\n\ts_waitcnt vmcnt(0)" : "=&v"(t_) : "v"(voff), "s"(rs), "s"(soff) : "memory"); \
            d = t_; if (!any_lane(t_[3] == ~0u)) break; __builtin_amdgcn_s_sleep(2); } } } while (0)
#define POLL(voff) do { \
        _Pragma("unroll 1") for (int s_ = 0; s_ < 4 * SPIN_CAP; ++s_) { u2 t_; asm volatile("buffer_load_dwordx2 %0, %1, %2, 0 offen sc1\n\ts_waitcnt vmcnt(0)" : "=&v"(t_) : "v"(voff), "s"(rsF) : "memory"); \
            if (!any_lane(t_[0] != tag[0] || t_[1] != tag[1])) break; NRETRY(); __builtin_amdgcn_s_sleep(RSLEEP); } } while (0)
#define FLAG(off) st8(tag, l == 0 ? (off) : OOB, rsF, 0)
#define STAMP(i) do { if ((i) == 30) asm volatile("s_memrealtime %0\n\ts_waitcnt lgkmcnt(0)" : "=s"(pt0)::"memory"); if ((i) == 12) asm volatile("s_memrealtime %0\n\ts_waitcnt lgkmcnt(0)" : "=s"(pt1)::"memory"); } while (0)

__global__ __launch_bounds__(512, 2) void fused_block(const char* x_, const char* wqkv_, const char* wo_, const char* wg_, const char* wu_, const char* wd_, char* out_, char* ws_) {
#if defined(__HIP_DEVICE_COMPILE__)

    typedef u64 u64x8 __attribute__((ext_vector_type(8)));
    u64x8 ka; asm volatile("s_load_dwordx16 %0, %1, 0x0\n\ts_waitcnt lgkmcnt(0)" : "=s"(ka) : "s"(__builtin_amdgcn_kernarg_segment_ptr()) : "memory");

    const char *x = (const char*)ka[0], *wqkv = (const char*)ka[1], *wo = (const char*)ka[2], *wg = (const char*)ka[3], *wu = (const char*)ka[4], *wd = (const char*)ka[5]; char *out = (char*)ka[6], *ws = (char*)ka[7];
    const unsigned l = threadIdx.x & 63, w = __builtin_amdgcn_readfirstlane(threadIdx.x >> 6);
    const unsigned b = blockIdx.x, xc = b & 7, rk = b >> 3;
    const unsigned T64 = 8 * (xc >> 1) + (rk >> 2), hq = 4 * (xc & 1) + (rk & 3);
    u64 pt0 = 0, pt1 = 0;
    STAMP(0);
    const unsigned u8 = l >> 3, l7 = l & 7;
    u4 xr[8];
    const u4 rsX = mk_rsrc(x + T64 * 65536u, 65536u);
    const unsigned vx = (8 * w + u8) * 1024 + l7 * 16;
    ld16_imm<0>(xr[0], vx, rsX); ld16_imm<128>(xr[1], vx, rsX); ld16_imm<256>(xr[2], vx, rsX); ld16_imm<384>(xr[3], vx, rsX);
    if (XB > 4) { ld16_imm<512>(xr[4], vx, rsX); ld16_imm<640>(xr[5], vx, rsX); }
    if (XB > 6) { ld16_imm<768>(xr[6], vx, rsX); ld16_imm<896>(xr[7], vx, rsX); }
    const u4 rsW = mk_rsrc(wqkv + hq * 128u, 512u * 3072u - hq * 128u);
    SBAR();
    const unsigned w3 = w & 3, wk2 = w3 * 2048;
    unsigned vw0 = (16 * w3 + u8) * 3072 + 32 * ((l7 >> 1) ^ ((l >> 4) & 1)) + 16 * (l7 & 1), vw1 = (vw0 + 24576) ^ 64;
#define SLOT(i) ((((i) + 1) & 3) * 24576u)
#define SP(i, p) dma_bufi<SLOT(i) + ((p) >> 1) * 8192 + ((p) & 1) * 1024, (i) * 196608u + ((p) >> 1) * 1024u>(wk2, ((p) & 1) ? vw1 : vw0, rsW, 0)
    const unsigned U64 = 4 * xc + (rk >> 3), fb = rk & 7;
    const unsigned g = 2 * (xc >> 1) + (rk >> 4), r = 16 * (xc & 1) + (rk & 15);
    const unsigned j = w & 1, hh = w >> 1;
    const unsigned t16 = l & 15, g4 = l >> 4, h = l >> 5, t32 = l & 31, q4 = t16 >> 2, pp = l & 3, l16 = l * 16, l128 = l * 128;
    const u4 rsF = mk_rsrc(ws, 16u << 20);
    const u4 ones = {0x3f803f80u, 0x3f803f80u, 0x3f803f80u, 0x3f803f80u};
    const unsigned kvb = WS_KV + (4 * hq + hh) * 131072u;
    const unsigned skv = kvb + (2 * T64 + j) * 2048;
    const unsigned sOw = WS_O + (2 * T64 + j) * 32768u + (4 * hq + hh) * 1024;
    u2 tag; u4 xres;
    unsigned vres = (32 * j + 8 * hh + l7) * 1024 + fb * 128 + u8 * 16;
    unsigned xw0 = L_A + (8 * w + u8) * 1024 + 16 * (l7 ^ (8 * (w & 1) + u8)), xw1 = xw0 ^ 128;
    unsigned xa[4];
#pragma unroll
    for (int q = 0; q < 4; ++q) { xa[q] = L_A + (32 * j + t16) * 1024 + 16 * ((4 * q + g4) ^ t16); asm volatile("" : "+v"(xa[q])); }
    unsigned wa = (8 * g4 + q4) * 128 + 32 * (hh ^ ((q4 >> 1) | ((g4 & 1) << 1))) + 8 * pp;
    f4 aq[2] = {}, ak[2] = {}, av[2] = {};
    unsigned vk = (h * 32 + (16 * ((t32 >> 3) & 1) + 8 * ((t32 >> 2) & 1) + 4 * (t32 >> 4) + (t32 & 3))) * 16, vv_ = (h * 16 + t16) * 16;
    unsigned vst = 1024 + ((g4 & 1) * 32 + (g4 >> 1) * 16 + t16) * 16;
    unsigned vrow = u8 * 1024 + 16 * (l7 ^ (((l >> 4) & 1) << 2));
    const char* gp[3];
#pragma unroll
    for (int i = 0; i < 3; ++i) { const unsigned z = 64 * i + l, k = (z * 43691u) >> 19, c = z - 12 * k, m = c >= 6, cc = c - 6 * m; unsigned un = 3 * r + (cc >> 1); un = un > 85 ? 85 : un;
        gp[i] = (m ? wu : wg) + ((16 * w + k) * 2752 + un * 32 + (cc & 1) * 16); asm volatile("" : "+v"(gp[i])); }
    const unsigned wk3 = w * 3072;
#define GU(R, c) dma_glbi<(R) * 24576 + (c) * 1024>(wk3, gp[c] + (R) * 128 * 2752)
#define GUD(R, q) dma_glbi<(R) * 24576 + ((q) / 3) * 12288 + ((q) % 3) * 1024>(wk3, gp[(q) % 3] + ((R) * 128 + ((q) / 3) * 64) * 2752)
    asm volatile("" : "+v"(vw0), "+v"(vw1), "+v"(xw0), "+v"(xw1), "+v"(wa), "+v"(vres), "+v"(vk), "+v"(vv_), "+v"(vrow), "+v"(vst));
    STAMP(39);
    if (XB <= 4) { ld16_imm<512>(xr[4], vx, rsX); ld16_imm<640>(xr[5], vx, rsX); }
    if (XB <= 6) { ld16_imm<768>(xr[6], vx, rsX); ld16_imm<896>(xr[7], vx, rsX); }
#define TAMODE 0
    {
        const char* pc; unsigned nb; asm volatile("s_getpc_b64 %0\n.Lfb_code0:\n\ts_mov_b32 %1, .Lfb_code1-.Lfb_code0" : "=s"(pc), "=s"(nb));
        const unsigned co = ((rk * 8 + w) * 2 + (l & 1)) * 128;
        const char* p = TAMODE == 1 ? (l < 24 ? wqkv + (hq * 128 + (w * 64 + (T64 & 7) * 8 + l7) * 3072 + u8 * 1024) : pc + (co < nb ? co : 0))
                                    : (l < 6 ? wqkv + ((b * 8 + w) * 6 + l) * 128 : pc + (co < nb ? co : 0));
        asm volatile("global_load_dword v254, %0, off" ::"v"(p) : "memory", "v254"); }
#define NO ((void)0)
#define TBPOS 0
#define TOUCH_B() do { const char* tp = (w == 4 ? wo : w == 5 ? wg : w == 6 ? wu : wd) + (w == 4 ? b * 16 + (l & 15) : b * 43 + (l < 43 ? l : 0)) * 128; \
        asm volatile("global_load_dword v255, %0, off" ::"v"(tp) : "memory", "v255"); } while (0)
#if !defined(NOTB) && TBPOS == 0
    if (w >= 4) TOUCH_B();
#define XW 1
#else
#define XW 0
#endif
#define SSTAMP(i)
    {
        f2 xf[32], s1 = {0.f, 0.f}, s2 = {0.f, 0.f};
#define LN_SUM(i, N, D0, D1) do { if (XW && (N) < 8 - (i)) WAITV1((N) + 1, xr[i]); else WAITV1D((N) + 1, 8 - (i) + XW, xr[i]); _Pragma("unroll") for (int e = 0; e < 4; ++e) { const unsigned d_ = xr[i][e]; f2 v_ = {blo(d_), bhi(d_)}; asm volatile("" : "+v"(v_)); xf[4 * (i) + e] = v_; \
        s1 += v_; s2 += v_ * v_; } asm volatile("" : "+v"(s1), "+v"(s2)); if (w < 4) { D0; D1; } SBAR(); } while (0)
        LN_SUM(0, 7, NO, NO); LN_SUM(1, 6, NO, NO); LN_SUM(2, 5, NO, NO); LN_SUM(3, 4, NO, NO);
        LN_SUM(4, 3, SP(0, 0), SP(0, 1)); LN_SUM(5, 4, SP(0, 2), SP(0, 3)); LN_SUM(6, 5, SP(0, 4), NO); LN_SUM(7, 5, SP(0, 5), NO);
        SSTAMP(34);
        if (w < 4) { SP(1, 0); SP(1, 1); SP(1, 2); SP(1, 3); SP(1, 4); SP(1, 5); }
#if !defined(NOTB) && TBPOS == 1
        else TOUCH_B();
#endif
        const float t1 = add_dpp<0x141>(add_dpp<0x4e>(add_dpp<0xb1>(s1[0] + s1[1]))), t2 = add_dpp<0x141>(add_dpp<0x4e>(add_dpp<0xb1>(s2[0] + s2[1])));
        const float mu = t1 * (1.f / 512), rs = __builtin_amdgcn_rsqf(__builtin_fmaxf(t2 * (1.f / 512) - mu * mu, 0.f) + 1e-5f), mr = -mu * rs;
#pragma unroll
        for (int i = 0; i < 8; ++i) sts16(((i & 1) ? xw1 : xw0) + (i >> 1) * 256, (u4){pk2(xf[4 * i] * rs + mr), pk2(xf[4 * i + 1] * rs + mr), pk2(xf[4 * i + 2] * rs + mr), pk2(xf[4 * i + 3] * rs + mr)});
    }
#define P1_STEP(n, D0, D1, D2) do { constexpr int c_ = (n) & 1, x_ = c_ ^ 1; constexpr bool m_ = (n) < 15; \
        const unsigned xb = xa[((n) + 1) & 3] + (((n) + 1) >> 2) * 256, wb = wa + SLOT(((n) + 1) >> 1) + x_ * 4096; \
        if (m_) X0[x_] = lds16(xb); SBAR(); \
        aq[0] = MFMA16(Wq[c_], X0[c_], aq[0]); if (m_) Wq[x_] = ldtr(wb, wb + 512); SBAR(); \
        aq[1] = MFMA16(Wq[c_], X1[c_], aq[1]); if (m_) X1[x_] = lds16(xb + 16384); D0; SBAR(); \
        ak[0] = MFMA16(Wk[c_], X0[c_], ak[0]); if (m_) Wk[x_] = ldtr(wb + 8192, wb + 8704); SBAR(); \
        ak[1] = MFMA16(Wk[c_], X1[c_], ak[1]); D1; SBAR(); \
        av[0] = MFMA16(X0[c_], Wv[c_], av[0]); if (m_) Wv[x_] = ldtr(wb + 16384, wb + 16896); SBAR(); \
        av[1] = MFMA16(X1[c_], Wv[c_], av[1]); D2; SBAR(); } while (0)
#define P1_FIRST() do { const unsigned wb = wa + SLOT(0); X0[0] = lds16(xa[0]); Wq[0] = ldtr(wb, wb + 512); X1[0] = lds16(xa[0] + 16384); Wk[0] = ldtr(wb + 8192, wb + 8704); Wv[0] = ldtr(wb + 16384, wb + 16896); } while (0)
#define XRES() ld16_plain(xres, vres, mk_rsrc(x + U64 * 65536u, 65536u), 0)
    if (w < 4) {
        u4 X0[2], X1[2], Wq[2], Wk[2], Wv[2];
        SP(2, 0); SP(2, 1); SP(2, 2); SP(2, 3); SP(2, 4); SP(2, 5);
        BAR_VM(12); STAMP(16);  P1_FIRST(); P1_STEP(0, (SP(3, 0), SP(3, 1)), (SP(3, 2), SP(3, 3)), (SP(3, 4), SP(3, 5)));
        BAR_VM(12); SSTAMP(17); P1_STEP(1, SP(4, 0), SP(4, 1), SP(4, 2)); P1_STEP(2, SP(4, 3), SP(4, 4), SP(4, 5));
        BAR_VM(12); SSTAMP(18); P1_STEP(3, SP(5, 0), SP(5, 1), SP(5, 2)); P1_STEP(4, SP(5, 3), SP(5, 4), SP(5, 5));
        BAR_VM(12); SSTAMP(19); P1_STEP(5, SP(6, 0), SP(6, 1), SP(6, 2)); P1_STEP(6, SP(6, 3), SP(6, 4), SP(6, 5));
        BAR_VM(12); SSTAMP(20); P1_STEP(7, SP(7, 0), SP(7, 1), SP(7, 2)); P1_STEP(8, SP(7, 3), SP(7, 4), SP(7, 5));
        BAR_VM(12); SSTAMP(21); P1_STEP(9, XRES(), GUD(1, 0), NO); P1_STEP(10, GUD(1, 1), GUD(1, 2), NO);
        BAR_VM(10); SSTAMP(22); P1_STEP(11, GUD(1, 3), GUD(1, 4), NO); P1_STEP(12, GUD(1, 5), NO, NO);
        BAR_VM(7);  SSTAMP(23); P1_STEP(13, NO, NO, NO); P1_STEP(14, NO, NO, NO); P1_STEP(15, NO, NO, NO);
        WAITV1(6, xres); tag = (u2){0u, 0u};
    } else {
        u4 X0[2], X1[2], Wq[2], Wk[2], Wv[2];
        const u4 bad = {~0u, ~0u, ~0u, ~0u};
#define MK(v, s_) st16(bad, v, rsF, s_)
#if !defined(NOTB) && TBPOS == 2
#define TCH(c) if (w == (c)) TOUCH_B()
#else
#define TCH(c) NO
#endif
        BAR_L(); STAMP(16);  P1_FIRST(); P1_STEP(0, ld8(tag, 0u * l, rsF, 0), MK(l16, skv), TCH(5));
        BAR_L(); SSTAMP(17); P1_STEP(1, NO, MK(l16 + 1024, skv), TCH(6)); P1_STEP(2, NO, MK(l16, skv - 262144), NO);
        BAR_L(); SSTAMP(18); P1_STEP(3, NO, MK(l16 + 1024, skv - 262144), TCH(4)); P1_STEP(4, NO, MK(l16, sOw), NO);
        BAR_L(); SSTAMP(19); P1_STEP(5, NO, MK(l16, sOw - 2048), TCH(7)); P1_STEP(6, XRES(), NO, NO);
        BAR_L(); SSTAMP(20); P1_STEP(7, NO, NO, NO); P1_STEP(8, NO, NO, NO);
        BAR_L(); SSTAMP(21); P1_STEP(9, NO, NO, NO); P1_STEP(10, NO, NO, NO);
        asm volatile("s_waitcnt vmcnt(0)" : "+v"(tag), "+v"(xres) :: "memory");
        BAR_L(); SSTAMP(22);
        { const unsigned lo = tag.x + 1u; tag.y += (lo == 0u); tag.x = lo; }
                             P1_STEP(11, FLAG(w == 4 ? WS_F1 + (T64 * 8 + hq) * 128 : OOB), NO, NO); P1_STEP(12, NO, NO, NO);
        BAR_L(); SSTAMP(23); P1_STEP(13, NO, NO, NO); P1_STEP(14, NO, NO, NO); P1_STEP(15, NO, NO, NO);
    }
    STAMP(1);
    u4 Qf;
    {
        { auto a = __builtin_amdgcn_permlane16_swap(pk2(lo2(ak[0])), pk2(lo2(ak[1])), false, false); auto c = __builtin_amdgcn_permlane16_swap(pk2(hi2(ak[0])), pk2(hi2(ak[1])), false, false);
          st16((u4){a[0], c[0], a[1], c[1]}, l16, rsF, skv); }
        st16((u4){pk2(lo2(av[0])), pk2(hi2(av[0])), pk2(lo2(av[1])), pk2(hi2(av[1]))}, vst, rsF, skv);
        STAMP(32);
        SBAR();
        constexpr float QS = 0.25f * 1.4426950408889634f;
        { auto a = __builtin_amdgcn_permlane16_swap(pk2(lo2(aq[0]) * QS), pk2(lo2(aq[1]) * QS), false, false); auto c = __builtin_amdgcn_permlane16_swap(pk2(hi2(aq[0]) * QS), pk2(hi2(aq[1]) * QS), false, false);
          Qf = (u4){a[0], c[0], a[1], c[1]}; }
        sts16(L_OC + (w ^ 1) * 2304 + l16, Qf);
    }
    const int Tb = 2 * (int)T64 - 8;
    u4 K[5], V0[5], V1[5];
    const int blk[5] = {Tb + 8 + (int)j, Tb + (int)j, Tb + 2 + 3 * (int)j, Tb + 3 + 3 * (int)j, Tb + 4 + 3 * (int)j};
#pragma unroll
    for (int i = 0; i < 5; ++i) { const unsigned o = blk[i] < 0 ? 0u : 0x3f803f80u; V0[i] = (u4){o, o, o, o}; V1[i] = V0[i]; asm volatile("" : "+v"(V0[i]), "+v"(V1[i])); }
    unsigned pm[8];
    { const short uq = (short)(t32 - 8 * h);
#pragma unroll
      for (int i = 0; i < 8; ++i) { const short c = (short)(16 * ((i >> 1) & 1) + 4 * (i >> 2) + 2 * (i & 1)); pm[i] = __builtin_bit_cast(unsigned, ((sh2){uq, uq} - (sh2){c, (short)(c + 1)}) >> (sh2){15, 15}); asm volatile("" : "+v"(pm[i])); } }
    unsigned so[5];
#pragma unroll
    for (int i = 0; i < 5; ++i) so[i] = blk[i] < 0 ? OOB : kvb + (unsigned)blk[i] * 2048;
    if (w == 7) {
        const int Tp = (int)T64 - 1 - (int)(l & 3); const unsigned vc = WS_F1 + ((Tp < 0 ? T64 : (unsigned)Tp) * 8 + hq) * 128; POLL(vc); }
    STAMP(35);
    BAR_VM(0);
    STAMP(2);
#define KVLD(i) do { ld16(K[i], vk, rsF, so[i]); ld16_v(V0[i], V1[i], vv_, rsF, so[i]); } while (0)
#define KVOK(N, i) do { WAITV3(N, K[i], V0[i], V1[i]); \
        if (UNLIKELY(any_lane(max(max(K[i][3], V0[i][3]), V1[i][3]) == ~0u))) { \
            _Pragma("unroll 1") for (int s_ = 0; s_ < SPIN_CAP; ++s_) { NRETRY(); KVLD(i); WAITV3(0, K[i], V0[i], V1[i]); if (!any_lane(max(max(K[i][3], V0[i][3]), V1[i][3]) == ~0u)) break; __builtin_amdgcn_s_sleep(2); } } } while (0)
    KVLD(2); KVLD(3);
    const u4 Qm = Qf, Qo = lds16(L_OC + w * 2304 + l16);
    f16 Om = {}, Oo = {};
    const f16 z16 = {};
    auto pv_tile = [&](const f16& S, const u4& va, const u4& vb, f16& O) {
        float p[16];
#pragma unroll
        for (int e = 0; e < 16; ++e) p[e] = __builtin_amdgcn_exp2f(S[e]);
        const u4 P0 = {pk(p[0], p[1]), pk(p[2], p[3]), pk(p[4], p[5]), pk(p[6], p[7])}, P1 = {pk(p[8], p[9]), pk(p[10], p[11]), pk(p[12], p[13]), pk(p[14], p[15])};
        O = MFMA32(va, P0, O); O = MFMA32(vb, P1, O);
    };
    const u4 rsWo = mk_rsrc(wo + fb * 128u, 512u * 1024u - fb * 128u);
    const unsigned wol = L_A + w * 8192, wos = w * 65536;
    const unsigned vh = (u8 >> 1) * 1024 + ((u8 & 1) * 32 + 8 * hh + l7) * 16, vs = l < 8 ? (8 * hh + l) * 8 : OOB;
    const unsigned sHw = WS_H + (2 * U64 + j) * 32768u + fb * 4096, sSw = WS_ST + (2 * U64 + j) * 2048u + fb * 256;
    const unsigned sGa = r < 29 ? WS_G + (8 * g + w) * 90112u + r * 2048 : OOB, sGb = r < 28 ? WS_G + (8 * g + w) * 90112u + (60 + r) * 1024 : OOB;
#define WO_(i) dma_bufi<(i) * 1024, (i) * 8192>(wol, vrow, rsWo, wos)
#define PIN16(a) asm volatile("" : "+v"(a))
#define MEM(...) do { SBAR(); __VA_ARGS__; SBAR(); } while (0)
#define HALF(S, o, va, O) do { float p_[8]; _Pragma("unroll") for (int e = 0; e < 8; ++e) p_[e] = __builtin_amdgcn_exp2f(S[(o) + e]); \
        u4 q_ = {pk(p_[0], p_[1]), pk(p_[2], p_[3]), pk(p_[4], p_[5]), pk(p_[6], p_[7])}; asm volatile("" : "+v"(q_)); O = MFMA32(va, q_, O); PIN16(O); SBAR(); } while (0)
#define SCORE(S, k, q) do { S = MFMA32(k, q, z16); PIN16(S); SBAR(); } while (0)
    f16 Sa, Sb, Sc;
    u2 ck; const unsigned vc1 = WS_F1 + (U64 * 8 + l7) * 128;
    const u4 bad = {~0u, ~0u, ~0u, ~0u};
    KVOK(3, 2);
    STAMP(24);
    SCORE(Sa, K[2], Qm); SCORE(Sb, K[2], Qo); MEM(KVLD(4));
    HALF(Sa, 0, V0[2], Om); MEM(KVLD(0)); HALF(Sa, 8, V1[2], Om); MEM(KVLD(1)); KVOK(9, 3);
    SCORE(Sa, K[3], Qm); HALF(Sb, 0, V0[2], Oo); MEM(WO_(0)); HALF(Sb, 8, V1[2], Oo); MEM(WO_(1));
    SSTAMP(42);
    SCORE(Sb, K[3], Qo); HALF(Sa, 0, V0[3], Om); MEM(WO_(2)); HALF(Sa, 8, V1[3], Om); MEM(WO_(3)); KVOK(10, 4);
    SCORE(Sa, K[4], Qm); HALF(Sb, 0, V0[3], Oo); MEM(WO_(4)); HALF(Sb, 8, V1[3], Oo); MEM(WO_(5));
    SSTAMP(43);
    SCORE(Sb, K[4], Qo); HALF(Sa, 0, V0[4], Om); MEM(WO_(6)); HALF(Sa, 8, V1[4], Om); MEM(WO_(7)); KVOK(11, 0); KVOK(8, 1);
    if (j == 0) { SCORE(Sa, K[0], Qo); HALF(Sb, 0, V0[4], Oo); MEM(st16(bad, vh, rsF, sHw)); HALF(Sb, 8, V1[4], Oo); MEM(st8((u2){~0u, ~0u}, vs, rsF, sSw), st16(bad, l16, rsF, sGb));
                  SCORE(Sb, K[0], Qm); SCORE(Sc, K[1], Qm); HALF(Sa, 0, V0[0], Oo); HALF(Sa, 8, V1[0], Oo); }
    else        { SCORE(Sa, K[1], Qo); HALF(Sb, 0, V0[4], Oo); MEM(st16(bad, vh, rsF, sHw)); HALF(Sb, 8, V1[4], Oo); MEM(st8((u2){~0u, ~0u}, vs, rsF, sSw), st16(bad, l16, rsF, sGb));
                  SCORE(Sb, K[0], Qm); SCORE(Sc, K[1], Qm); HALF(Sa, 0, V0[1], Oo); HALF(Sa, 8, V1[1], Oo); }
    SSTAMP(44);
    MEM(ld8(ck, w == 7 ? vc1 : OOB, rsF, 0), GU(2, 0), GU(2, 1), GU(2, 2));
    const unsigned oc = L_OC + w * 2304;
    {
        const f16 Sx = Sb, Sy = Sc;
        float p[16];
#pragma unroll
        for (int e = 0; e < 8; ++e) p[e] = __builtin_amdgcn_exp2f(sel(diag_mask(e), Sx[e], Sy[e]));
        SBAR();
        sts16f(oc + l16, (f4){Oo[0], Oo[1], Oo[2], Oo[3]}); sts16f(oc + 1024 + l16, (f4){Oo[4], Oo[5], Oo[6], Oo[7]}); sts4f(oc + 2048 + l * 4, Oo[8]);
#pragma unroll
        for (int e = 8; e < 16; ++e) p[e] = __builtin_amdgcn_exp2f(sel(diag_mask(e), Sx[e], Sy[e]));
        unsigned P[8];
#pragma unroll
        for (int e = 0; e < 8; ++e) P[e] = pk(p[2 * e], p[2 * e + 1]);
        const u4 Y0 = {P[0] & pm[0], P[1] & pm[1], P[2] & pm[2], P[3] & pm[3]}, Y1 = {P[4] & pm[4], P[5] & pm[5], P[6] & pm[6], P[7] & pm[7]};
        const u4 X0_ = {P[0] ^ Y0[0], P[1] ^ Y0[1], P[2] ^ Y0[2], P[3] ^ Y0[3]}, X1_ = {P[4] ^ Y1[0], P[5] ^ Y1[1], P[6] ^ Y1[2], P[7] ^ Y1[3]};
        Om = MFMA32(V0[0], X0_, Om); Om = MFMA32(V1[0], X1_, Om); Om = MFMA32(V0[1], Y0, Om); Om = MFMA32(V1[1], Y1, Om);
    }
    SBAR();
    STAMP(25);
    asm volatile("s_waitcnt vmcnt(3) lgkmcnt(0)\n\ts_barrier" : "+v"(ck) :: "memory");
    SSTAMP(45);
    const unsigned op = L_OC + (w ^ 1) * 2304;
    f4 oa = lds16f(op + l16) + (f4){Om[0], Om[1], Om[2], Om[3]}, ob = lds16f(op + 1024 + l16) + (f4){Om[4], Om[5], Om[6], Om[7]};
    float den = Om[8] + lds4f(op + 2048 + l * 4);
    if (UNLIKELY(any_lane(!(den >= 0x1p-60f && den <= 0x1p60f)))) {
        f16 O = {}; float m = -__builtin_inff();
#pragma unroll 1
        for (int kb = 8; kb >= 0; --kb) {
            const int T = 2 * (int)T64 + (int)j - 8 + kb;
            if (T < 0) break;
            u4 Ks, Va = ones, Vb = ones;
            ld16(Ks, vk, rsF, kvb + (unsigned)T * 2048); ld16_v(Va, Vb, vv_, rsF, kvb + (unsigned)T * 2048);
            WAITV3(0, Ks, Va, Vb);
            f16 S = MFMA32(Ks, Qm, z16);
            const u64 all = (kb != 0 && kb != 8) ? ~0ull : 0ull, inv = kb == 0 ? ~0ull : 0ull;
            float mx = m;
#pragma unroll
            for (int e = 0; e < 16; ++e) { S[e] = sel((diag_mask(e) ^ inv) | all, S[e], -__builtin_inff()); mx = __builtin_fmaxf(mx, S[e]); }
            mx = max_x32(mx);
            const float al = __builtin_amdgcn_exp2f(m - mx); m = mx;
#pragma unroll
            for (int e = 0; e < 16; ++e) { O[e] *= al; S[e] -= mx; }
            pv_tile(S, Va, Vb, O);
        }
        oa = (f4){O[0], O[1], O[2], O[3]}; ob = (f4){O[4], O[5], O[6], O[7]}; den = O[8];
    }
    const float rd = __builtin_amdgcn_rcpf(den);
    st16((u4){pk2(lo2(oa) * rd), pk2(hi2(oa) * rd), pk2(lo2(ob) * rd), pk2(hi2(ob) * rd)}, l16, rsF, sOw);
    FLAG(w == 4 ? WS_F2 + (U64 * 8 + fb) * 128 : w == 5 ? WS_F3 + (g * 32 + r) * 128 : OOB);
    STAMP(26);
    const unsigned ta0 = (4 * h + q4) * 128 + 64 * (q4 >> 1) + 32 * (g4 & 1) + 8 * pp, ta1 = ta0 ^ 64;
    f16 acc0 = {}, acc1 = {};
    {
        const unsigned sO = WS_O + (2 * U64 + j) * 32768u + hh * 8192;
        const unsigned oa0 = L_A + hh * 16384 + ta0, oa1 = L_A + hh * 16384 + ta1;
        u4 A0[8], A1[8], B[8];
#pragma unroll
        for (int i = 0; i < 8; ++i) { A0[i] = ldtr(oa0 + i * 2048, oa0 + i * 2048 + 1024); A1[i] = ldtr(oa1 + i * 2048, oa1 + i * 2048 + 1024); }
        if (w == 7 && UNLIKELY(any_lane(ck[0] != tag[0] || ck[1] != tag[1]))) POLL(vc1);
        STAMP(37);
        BAR_VM(1);
        STAMP(5);
#pragma unroll
        for (int i = 0; i < 8; ++i) ld16(B[i], l16, rsF, sO + i * 1024);
#define P3_PAIR(i, NW, M0, M1) do { asm volatile("s_waitcnt vmcnt(%2)" : "+v"(B[i]), "+v"(B[(i) + 1]) : "i"(NW) : "memory"); \
            if (UNLIKELY(any_lane(max(B[i][3], B[(i) + 1][3]) == ~0u))) { \
                _Pragma("unroll 1") for (int s_ = 0; s_ < SPIN_CAP; ++s_) { NRETRY(); __builtin_amdgcn_s_sleep(3); asm volatile("s_waitcnt vmcnt(0)" ::: "memory"); unsigned m_ = 0; \
                    _Pragma("unroll") for (int q = (i); q < 8; ++q) ld16(B[q], l16, rsF, sO + q * 1024); \
                    _Pragma("unroll") for (int q = (i); q < 8; ++q) { WAITV1(7 - q, B[q]); m_ = max(m_, B[q][3]); } \
                    if (!any_lane(m_ == ~0u)) break; } } if ((i) == 0) STAMP(4); \
            acc0 = MFMA32(A0[i], B[i], acc0); acc1 = MFMA32(A1[i], B[i], acc1); M0; SBAR(); acc0 = MFMA32(A0[(i) + 1], B[(i) + 1], acc0); acc1 = MFMA32(A1[(i) + 1], B[(i) + 1], acc1); M1; SBAR(); } while (0)
        P3_PAIR(0, 6, NO, NO); P3_PAIR(2, 4, GU(0, 0), GU(0, 1)); P3_PAIR(4, 4, GU(0, 2), GU(3, 0)); P3_PAIR(6, 4, GU(3, 1), GU(3, 2));
    }
    const unsigned vc2 = WS_F2 + (g * 32 + t32) * 128;
    ld8(ck, w == 7 ? vc2 : OOB, rsF, 0);
    STAMP(27);
    const unsigned rw0 = L_A + (hh * 2 + j) * 8192 + h * 512 + t32 * 16, rw1 = rw0 ^ 128, rr = L_A + j * 8192 + u8 * 1024 + (((8 * hh + l7) ^ ((u8 & 1) << 3)) * 16);
    auto reduce = [&](f4& lo, f4& hi) {
#pragma unroll
        for (int u = 0; u < 4; ++u) {
            sts16f(((u & 1) ? rw1 : rw0) + u * 1024, (f4){acc0[4 * u], acc0[4 * u + 1], acc0[4 * u + 2], acc0[4 * u + 3]});
            sts16f(((u & 1) ? rw1 : rw0) + (4 + u) * 1024, (f4){acc1[4 * u], acc1[4 * u + 1], acc1[4 * u + 2], acc1[4 * u + 3]});
        }
        BAR_L();
        lo = (lds16f(rr) + lds16f(rr + 16384)) + (lds16f(rr + 32768) + lds16f(rr + 49152));
        hi = (lds16f(rr + 512) + lds16f(rr + 16896)) + (lds16f(rr + 33280) + lds16f(rr + 49664));
    };
    u4 Hres;
    const unsigned ga = (8 * h + q4) * 192 + (g4 & 1) * 96 + 8 * pp;
    f16 cs[3] = {};
    {
        f4 lo, hi; reduce(lo, hi);
        STAMP(28);
        Hres = (u4){pk2(lo2(lo) + (f2){blo(xres[0]), bhi(xres[0])}), pk2(hi2(lo) + (f2){blo(xres[1]), bhi(xres[1])}), pk2(lo2(hi) + (f2){blo(xres[2]), bhi(xres[2])}), pk2(hi2(hi) + (f2){blo(xres[3]), bhi(xres[3])})};
        st16(Hres, vh, rsF, sHw);
        f2 s1 = {0.f, 0.f}, s2 = {0.f, 0.f};
#pragma unroll
        for (int e = 0; e < 4; ++e) { const f2 a = {blo(Hres[e]), bhi(Hres[e])}; s1 += a; s2 += a * a; }
        const float t1 = add_x32(add_x16(add_x8(s1[0] + s1[1]))), t2 = add_x32(add_x16(add_x8(s2[0] + s2[1])));
        st8((u2){uf(t1), uf(t2)}, vs, rsF, sSw);
        STAMP(33);
        asm volatile("s_waitcnt vmcnt(2) lgkmcnt(0)\n\ts_barrier" : "+v"(ck) :: "memory");
        STAMP(40);
        const unsigned o = ga + w * 12288;
        u4 a[12];
#pragma unroll
        for (int i = 0; i < 12; ++i) a[i] = ldtr(o + (i / 3) * 3072 + (i % 3) * 32, o + (i / 3) * 3072 + (i % 3) * 32 + 768);
        SBAR();
#pragma unroll
        for (int i = 0; i < 12; ++i) cs[i % 3] = MFMA32(a[i], ones, cs[i % 3]);
        asm volatile("" : "+v"(cs[0]), "+v"(cs[1]), "+v"(cs[2]));
    }
    STAMP(36);
    const u4 rsWd = mk_rsrc(wd + fb * 128u, 1376u * 1024u - fb * 128u);
    if (t32 == 0) {
#pragma unroll
        for (int u = 0; u < 3; ++u)
#pragma unroll
            for (int qd = 0; qd < 4; ++qd) sts16f(L_CSP + w * 384 + u * 128 + qd * 32 + h * 16, (f4){cs[u][4 * qd], cs[u][4 * qd + 1], cs[u][4 * qd + 2], cs[u][4 * qd + 3]});
    }
    if (w == 7 && UNLIKELY(any_lane(ck[0] != tag[0] || ck[1] != tag[1]))) POLL(vc2);
    if (w == 5 && b == 0) {
#pragma unroll 1
        for (int s_ = 0; s_ < 4 * SPIN_CAP; ++s_) { u2 f0, f1, f2_, f3; const unsigned vc = WS_F1 + l * 128;
            ld8(f0, vc, rsF, 0); ld8(f1, vc, rsF, 8192); ld8(f2_, vc, rsF, 16384); ld8(f3, vc, rsF, 24576); WAITV4(0, f0, f1, f2_, f3);
            if (!any_lane(((f0[0] ^ tag[0]) | (f1[0] ^ tag[0]) | (f2_[0] ^ tag[0]) | (f3[0] ^ tag[0]) | (f0[1] ^ tag[1]) | (f1[1] ^ tag[1]) | (f2_[1] ^ tag[1]) | (f3[1] ^ tag[1])) != 0u)) break; }
        FLAG(0u);
    }
    BAR_VM(0);
    STAMP(6);
    const unsigned a0 = 15 * hh, a1 = a0 + 1, jo = j * 8192;
    const unsigned wde = (48 * (a0 >> 1) + 16 * (a0 & 1)) * 1024 + jo, wdo = (48 * (a1 >> 1) + 16 * (a1 & 1)) * 1024 + jo, wdt = (336 * hh + 32) * 1024 + jo;
    const unsigned wla = L_A + hh * 14336 + j * 1024, wlb = hh * 12288 + j * 1024, wlc = 49152 + hh * 4096 + j * 1024;
#define WD(i) dma_bufi<((i) < 14 ? (i) % 7 : (i) < 20 ? (i) - 14 : (i) - 20) * 2048u, ((i) < 15 ? (i) >> 1 : (i) - 15) * 49152u>((i) < 14 ? wla : (i) < 20 ? wlb : wlc, vrow, rsWd, (i) >= 15 ? wdt : ((i) & 1) ? wdo : wde)
    u4 D0[7], D1[7];
    {
        const unsigned sH = WS_H + (8 * g + w) * 32768u, sS = WS_ST + (8 * g + w) * 2048u;
#define PD4 6
        constexpr int PD = PD4;
        u4 Hb[32], A0[3], A1[3], A2[4];
        u2 st[4];
        f16 ag[3] = {};
        const unsigned cf = L_CF + w * 384, vstat = h * 1024 + t32 * 8;
#pragma unroll
        for (int i = 0; i < PD; ++i) ld16(Hb[kgo(i)], l16, rsF, sH + kgo(i) * 1024);
        if (l < 48) { f2 s = {0.f, 0.f};
#pragma unroll
            for (int i = 0; i < 8; ++i) s += *(LAS const f2*)(L_CSP + i * 384 + l * 8);
            sts8f(cf + l * 8, s); }
        A0[0] = ldtr(ga, ga + 768); A1[0] = ldtr(ga + 32, ga + 800); A0[1] = ldtr(ga + kgo(1) * 3072, ga + kgo(1) * 3072 + 768); A1[1] = ldtr(ga + kgo(1) * 3072 + 32, ga + kgo(1) * 3072 + 800);
        unsigned mx = 0;
#define P4_STEP(i) do { constexpr int c = (i) % 3, n = ((i) + 2) % 3, k = kgo(i); const unsigned o = ga + ((i) < 30 ? kgo((i) + 2) * 3072 : 0); \
            if ((i) + PD < 32) ld16(Hb[kgo((i) + PD < 32 ? (i) + PD : 0)], l16, rsF, sH + kgo((i) + PD) * 1024); \
            else if ((i) + PD < 36) ld8(st[((i) + PD - 32) & 3], vstat, rsF, sS + ((i) + PD - 32) * 256); \
            WAITV1((i) + PD < 36 ? PD : 35 - (i), Hb[k]); \
            if ((i) < 8) FRESH16(Hb[k], l16, rsF, sH + k * 1024); else { mx = max(mx, Hb[k][3]); asm volatile("" : "+v"(mx)); } if ((i) == 0) STAMP(7); SBAR(); \
            ag[0] = MFMA32(A0[c], Hb[k], ag[0]); if ((i) < 30) A0[n] = ldtr(o, o + 768); else A2[((i) - 30) & 1] = ldtr(ga + 64 + (((i) - 30) & 1) * 3072, ga + 832 + (((i) - 30) & 1) * 3072); SBAR(); \
            ag[1] = MFMA32(A1[c], Hb[k], ag[1]); if ((i) < 30) A1[n] = ldtr(o + 32, o + 800); else if ((i) == 31) A2[2] = ldtr(ga + 64 + 6144, ga + 832 + 6144); SBAR(); } while (0)
#define P4_4(i) P4_STEP(i); P4_STEP((i) + 1); P4_STEP((i) + 2); P4_STEP((i) + 3)
        P4_4(0); P4_4(4); P4_4(8); P4_4(12); P4_4(16); P4_4(20); P4_4(24); P4_4(28);
        WAITV4(0, st[0], st[1], st[2], st[3]);
        if (UNLIKELY(any_lane(max(max(mx, max(st[0][1], st[1][1])), max(st[2][1], st[3][1])) == ~0u))) {
#pragma unroll
            for (int i = 0; i < 32; ++i) FRESH16(Hb[i], l16, rsF, sH + i * 1024);
#pragma unroll 1
            for (int s_ = 0; s_ < SPIN_CAP; ++s_) { NRETRY();
#pragma unroll
                for (int i = 0; i < 4; ++i) ld8(st[i], vstat, rsF, sS + i * 256);
                WAITV4(0, st[0], st[1], st[2], st[3]);
                if (!any_lane(max(max(st[0][1], st[1][1]), max(st[2][1], st[3][1])) == ~0u)) break;
            }
            ag[0] = z16; ag[1] = z16;
#pragma unroll
            for (int i = 0; i < 32; ++i) { const unsigned o = ga + kgo(i) * 3072; ag[0] = MFMA32(ldtr(o, o + 768), Hb[kgo(i)], ag[0]); ag[1] = MFMA32(ldtr(o + 32, o + 800), Hb[kgo(i)], ag[1]); }
        }
        STAMP(8);
        const float t1 = add_x32(fu(st[0].x) + fu(st[1].x) + fu(st[2].x) + fu(st[3].x)), t2 = add_x32(fu(st[0].y) + fu(st[1].y) + fu(st[2].y) + fu(st[3].y));
        const float nm = t1 * (-1.f / 512), ve = __builtin_fmaxf(t2 * (1.f / 512) - nm * nm, 0.f) + 1e-5f, rs = __builtin_amdgcn_rsqf(ve);
        const float ka_ = rs * -2.3022081986f, kb_ = rs * rs * rs * -0.1029432397f;
#define G_UNIT(u, so_, M0, M1) do { u4 y; _Pragma("unroll") for (int qd = 0; qd < 2; ++qd) { \
                const f4 cg = lds16f(cf + (u) * 128 + qd * 32 + h * 16), cu = lds16f(cf + (u) * 128 + (2 + qd) * 32 + h * 16); \
                _Pragma("unroll") for (int e = 0; e < 2; ++e) { \
                    const f2 gt = (f2){ag[u][4 * qd + 2 * e], ag[u][4 * qd + 2 * e + 1]} + (e ? hi2(cg) : lo2(cg)) * nm, up = (f2){ag[u][8 + 4 * qd + 2 * e], ag[u][9 + 4 * qd + 2 * e]} + (e ? hi2(cu) : lo2(cu)) * nm; \
                    const f2 z = gt * (gt * gt * kb_ + ka_), d = (f2){__builtin_amdgcn_exp2f(z[0]), __builtin_amdgcn_exp2f(z[1])} * ve + ve; \
                    unsigned y_ = pk2(gt * up * (f2){__builtin_amdgcn_rcpf(d[0]), __builtin_amdgcn_rcpf(d[1])}); asm volatile("" : "+v"(y_)); y[2 * qd + e] = y_; } \
                if (qd == 0) MEM(M0); } \
            MEM(st16(y, l16, rsF, so_); M1); } while (0)
        MEM(WD(0), WD(1), WD(2));
        G_UNIT(0, sGa, (WD(3), WD(4), WD(5), WD(6)), NO); G_UNIT(1, sGa + 1024, NO, NO);
        STAMP(47);
        const unsigned da0 = L_A + hh * 14336 + ta0, da1 = L_A + hh * 14336 + ta1;
        const unsigned vc3 = WS_F3 + ((U64 >> 2) * 32 + t32) * 128, vc4 = vc3 + (WS_F4 - WS_F3);
#define B1 6
#define WDS(i, m) if ((i) == B1 + 8 + (B1 + 21 <= 31 ? (m) : (m) * (24 - B1) / 13)) WD(7 + (m))
#define P4B_STEP(i) do { constexpr int c = (i) & 3, n = ((i) + 3) & 3; const unsigned o = ga + ((i) + 3) * 3072 + 64; \
            if ((i) == B1) SSTAMP(48); if ((i) == B1 + 1) SSTAMP(49); if ((i) == B1 + 8) SSTAMP(50); if ((i) == B1 + 9) SSTAMP(51); if ((i) == B1 + 15) SSTAMP(52); if ((i) == B1 + 21) SSTAMP(53); \
            if ((i) == B1) BAR_VM(2);         \
            if ((i) == B1 + 8) { BAR_VM(0); FLAG(w == 4 ? WS_F4 + (g * 32 + r) * 128 : OOB); }        SBAR(); \
            ag[2] = MFMA32(A2[c], Hb[i], ag[2]); if ((i) < 29) A2[n] = ldtr(o, o + 768); \
            if ((i) >= B1 && (i) < B1 + 7) { constexpr int q = ((i) + 14 - B1) % 7; D0[q] = ldtr(da0 + q * 2048, da0 + q * 2048 + 1024); D1[q] = ldtr(da1 + q * 2048, da1 + q * 2048 + 1024); } SBAR(); \
            WDS(i, 0); WDS(i, 1); WDS(i, 2); WDS(i, 3); WDS(i, 4); WDS(i, 5); WDS(i, 6); WDS(i, 7); WDS(i, 8); WDS(i, 9); WDS(i, 10); WDS(i, 11); WDS(i, 12); \
            if ((i) == (B1 + 21 < 31 ? B1 + 21 : 31)) ld8(ck, w == 7 ? vc3 : w == 6 ? vc4 : OOB, rsF, 0); SBAR(); } while (0)
#define P4B_4(i) P4B_STEP(i); P4B_STEP((i) + 1); P4B_STEP((i) + 2); P4B_STEP((i) + 3)
        P4B_4(0); P4B_4(4); P4B_4(8); P4B_4(12); P4B_4(16); P4B_4(20); P4B_4(24); P4B_4(28);
        STAMP(41);
        G_UNIT(2, sGb, NO, NO);
        STAMP(30);
        WAITV1(1, ck);
        if (w >= 6 && UNLIKELY(any_lane(ck[0] != tag[0] || ck[1] != tag[1]))) { const unsigned vc = w == 7 ? vc3 : vc4; POLL(vc); }
    }
    BAR_L();
    STAMP(9);
    {
        constexpr int PD = 6;
        u4 Gb[22], A0[2], A1[2];
        const unsigned gs = WS_G + (2 * U64 + j) * 90112u + 15 * hh * 1024, gz = hh == 3 ? OOB : gs, gt = WS_G + (2 * U64 + j) * 90112u + (60 + 7 * hh - 15) * 1024;
        const unsigned o0 = L_A + hh * 14336 + ta0, o1 = L_A + hh * 14336 + ta1, p0 = hh * 12288 + ta0, p1 = hh * 12288 + ta1, q0 = 49152 + hh * 4096 + ta0, q1 = 49152 + hh * 4096 + ta1;
#define GOFF(i) (((i) >= 15 ? gt : (i) >= 13 ? gz : gs) + (i) * 1024)
#pragma unroll
        for (int i = 0; i < PD; ++i) ld16(Gb[i], l16, rsF, GOFF(i));
        WD(20); WD(21);
        acc0 = z16; acc1 = z16;
#define P5_STEP(i) do { constexpr int c = (i) & 1, n = c ^ 1; \
            if ((i) + PD < 22) ld16(Gb[(i) + PD < 22 ? (i) + PD : 0], l16, rsF, GOFF((i) + PD)); \
            WAITV1(((i) + PD < 22 ? PD : 21 - (i)) + ((i) < PD ? 2 : 0), Gb[i]); if ((i) >= 15) FRESH16(Gb[i], l16, rsF, GOFF(i)); if ((i) == 0) STAMP(10); \
            if ((i) == 12) BAR_L(); SBAR(); \
            const unsigned a0_ = (i) < 13 ? o0 + ((i) - 6) * 2048 : (i) < 19 ? p0 + ((i) - 13) * 2048 : q0 + ((i) - 19) * 2048, a1_ = (i) < 13 ? o1 + ((i) - 6) * 2048 : (i) < 19 ? p1 + ((i) - 13) * 2048 : q1 + ((i) - 19) * 2048; \
            acc0 = MFMA32((i) < 7 ? D0[(i) < 7 ? (i) : 0] : A0[c], Gb[i], acc0); if ((i) >= 6 && (i) < 21) A0[n] = ldtr(a0_, a0_ + 1024); SBAR(); \
            acc1 = MFMA32((i) < 7 ? D1[(i) < 7 ? (i) : 0] : A1[c], Gb[i], acc1); if ((i) >= 6 && (i) < 21) A1[n] = ldtr(a1_, a1_ + 1024); SBAR(); } while (0)
        P5_STEP(0); P5_STEP(1); P5_STEP(2); P5_STEP(3); P5_STEP(4); P5_STEP(5); P5_STEP(6); P5_STEP(7); P5_STEP(8); P5_STEP(9); P5_STEP(10);
        P5_STEP(11); P5_STEP(12); P5_STEP(13); P5_STEP(14); P5_STEP(15); P5_STEP(16); P5_STEP(17); P5_STEP(18); P5_STEP(19); P5_STEP(20); P5_STEP(21);
    }
    STAMP(11);
    BAR_L();
    {
        f4 lo, hi; reduce(lo, hi);
        const u4 Ores = {pk2(lo2(lo) + (f2){blo(Hres[0]), bhi(Hres[0])}), pk2(hi2(lo) + (f2){blo(Hres[1]), bhi(Hres[1])}), pk2(lo2(hi) + (f2){blo(Hres[2]), bhi(Hres[2])}), pk2(hi2(hi) + (f2){blo(Hres[3]), bhi(Hres[3])})};
        st16_plain(Ores, vres, mk_rsrc(out + U64 * 65536u, 65536u), 0);
    }
    asm volatile(".Lfb_code1:");
    STAMP(12);
    if (l == 0) ((__attribute__((address_space(1))) u2*)(ws + (20u << 20)))[b * 8 + w] = (u2){(unsigned)pt0, (unsigned)pt1};
#endif
}

__global__ void kb(const unsigned* p) {
#if __HIP_DEVICE_COMPILE__
    const unsigned ref = p[0]; int lo = -2000000, hi = -2000000;
    for (int i = 0; i < 32; ++i) { const int x = (int)(p[2 * (threadIdx.x + 64 * i)] - ref), y = (int)(p[2 * (threadIdx.x + 64 * i) + 1] - ref); lo = max(lo, x); hi = max(hi, y); }
    for (int o = 32; o; o >>= 1) { lo = max(lo, __shfl_xor(lo, o)); hi = max(hi, __shfl_xor(hi, o)); }
    const unsigned dd = __builtin_amdgcn_readfirstlane(hi - lo); const u64 d = dd > 6000u ? 0u : dd;
    { u64 now; asm volatile("s_memrealtime %0\n\ts_waitcnt lgkmcnt(0)" : "=s"(now)::"memory"); const u64 until = now + 10 * (d);
#pragma unroll 1
        for (int i = 0; i < 20000 && now < until; ++i) asm volatile("s_memrealtime %0\n\ts_waitcnt lgkmcnt(0)" : "=s"(now)::"memory"); }
#endif
}
#define WSOFF 0
extern "C" void kernel_launch(void* const* d_in, const int* in_sizes, int n_in, void* d_out, int out_size, void* d_ws, size_t ws_size, hipStream_t stream) {
    (void)in_sizes; (void)n_in; (void)out_size; (void)ws_size;
    (void)hipFuncSetAttribute((const void*)fused_block, hipFuncAttributeMaxDynamicSharedMemorySize, LDS_BYTES);
    fused_block<<<256, 512, LDS_BYTES, stream>>>((const char*)d_in[0], (const char*)d_in[1], (const char*)d_in[2], (const char*)d_in[4], (const char*)d_in[5], (const char*)d_in[6],
                                                 (char*)d_out, (char*)d_ws + WSOFF);
    kb<<<1, 64, 0, stream>>>((const unsigned*)((char*)d_ws + (20u << 20)));
}
#define LASTSTAMP 12
#define STAMP_NAMES "start", "P1 loop end", "KV drained", 0, "O first data", "O drained", "h drained", "h first data", "P4 pass1 end", "P4 end bar", "G first data", "P5 loop end", "end", 0, 0, 0, \
  "st0", "st1", "st2", "st3", "st4", "st5", "st6", "st7", "KV first data", "att done", "O stored", "P3 loop end", "P3 reduced", 0, "G stored", 0, "KV stored", "h stored", \
  "LN sums", "pre KV bar", "colsum done", "A read", 0, "setup done", "GU bar", "P4 pass2 end", "att blk3", "att blk4", "att blk0", "comb bar", 0, "G01 stored", "p2 pre B1", "p2 s7", "p2 pre B2", "p2 s15", "p2 s21", "p2 s27"
```

```cpp
#define KVS 132096u
#define OS 33792u
#define XB 6
#include <hip/hip_runtime.h>
#include <hip/hip_bf16.h>
#include <stdint.h>
#pragma clang diagnostic ignored "-Wint-to-pointer-cast"

typedef __bf16 bf8 __attribute__((ext_vector_type(8)));
typedef __bf16 bf2 __attribute__((ext_vector_type(2)));
typedef short s4 __attribute__((ext_vector_type(4)));
typedef short s8 __attribute__((ext_vector_type(8)));
typedef float f2 __attribute__((ext_vector_type(2)));
typedef float f4 __attribute__((ext_vector_type(4)));
typedef float f16 __attribute__((ext_vector_type(16)));
typedef unsigned u2 __attribute__((ext_vector_type(2)));
typedef unsigned u4 __attribute__((ext_vector_type(4)));
typedef unsigned long long u64;
#define LAS __attribute__((address_space(3)))
#define DEV __device__ __forceinline__

constexpr unsigned WS_F1 = 32768, WS_F2 = 65536, WS_F3 = 98304, WS_F4 = 131072;
constexpr unsigned WS_KV = 1u << 20;
constexpr unsigned WS_O = WS_KV + 32 * KVS;
constexpr unsigned WS_H = WS_O + 64 * OS;
constexpr unsigned WS_ST = WS_H + 64 * OS;
constexpr unsigned WS_G = WS_ST + 64 * 2048;
static_assert(WS_G + 64 * 90112u <= (16u << 20), "hand-off buffers");
constexpr unsigned WS_DIAG = 32u << 20;
constexpr unsigned LDS_BYTES = 163840;
constexpr unsigned L_HOLE = 73728;
constexpr unsigned L_OC = L_HOLE;
constexpr unsigned L_A = 98304;
constexpr unsigned L_CSP = 155648, L_CF = L_CSP + 3072;
constexpr int SPIN_CAP = 1 << 14;
constexpr unsigned OOB = 0x40000000u;

#define MFMA16(a, b, c) __builtin_amdgcn_mfma_f32_16x16x32_bf16(__builtin_bit_cast(bf8, a), __builtin_bit_cast(bf8, b), c, 0, 0, 0)
#define MFMA32(a, b, c) __builtin_amdgcn_mfma_f32_32x32x16_bf16(__builtin_bit_cast(bf8, a), __builtin_bit_cast(bf8, b), c, 0, 0, 0)
#define SBAR() __builtin_amdgcn_sched_barrier(0)
#define BAR_VM(N) asm volatile("s_waitcnt vmcnt(%0) lgkmcnt(0)\n\ts_barrier" ::"i"(N) : "memory")
#define BAR_L() asm volatile("s_waitcnt lgkmcnt(0)\n\ts_barrier" ::: "memory")

DEV u4 mk_rsrc(const void* p, unsigned bytes) { const u64 a = (u64)p; return (u4){(unsigned)a, (unsigned)(a >> 32) & 0xffffu, bytes, 0x00020000u}; }
DEV void dma_buf(unsigned lds, unsigned voff, u4 rs, unsigned soff) {
    asm volatile("s_mov_b32 m0, %0\n\ts_nop 0\n\tbuffer_load_dwordx4 %1, %2, %3 offen lds" ::"s"(lds), "v"(voff), "s"(rs), "s"(soff) : "memory"); }
template <unsigned LI, unsigned SI> DEV void dma_bufi(unsigned lds, unsigned voff, u4 rs, unsigned soff) {
    unsigned t; asm volatile("s_add_u32 m0, %1, %5\n\ts_add_u32 %0, %4, %6\n\tbuffer_load_dwordx4 %2, %3, %0 offen lds" : "=&s"(t) : "s"(lds), "v"(voff), "s"(rs), "s"(soff), "i"(LI), "i"(SI) : "memory", "scc"); }
template <unsigned LI> DEV void dma_glbi(unsigned lds, const char* p) {
    asm volatile("s_add_u32 m0, %0, %2\n\ts_nop 0\n\tglobal_load_lds_dwordx4 %1, off" ::"s"(lds), "v"(p), "i"(LI) : "memory", "scc"); }
DEV void dma_glb(unsigned lds, const char* p) {
    asm volatile("s_mov_b32 m0, %0\n\ts_nop 0\n\tglobal_load_lds_dwordx4 %1, off" ::"s"(lds), "v"(p) : "memory"); }
DEV void ld16(u4& d, unsigned voff, u4 rs, unsigned soff) {
    asm volatile("buffer_load_dwordx4 %0, %1, %2, %3 offen sc1" : "=v"(d) : "v"(voff), "s"(rs), "s"(soff) : "memory"); }
DEV void ld16_plain(u4& d, unsigned voff, u4 rs, unsigned soff) {
    asm volatile("buffer_load_dwordx4 %0, %1, %2, %3 offen" : "=v"(d) : "v"(voff), "s"(rs), "s"(soff) : "memory"); }
template <int OFF> DEV void ld16_imm(u4& d, unsigned voff, u4 rs) {
    asm volatile("buffer_load_dwordx4 %0, %1, %2, 0 offen offset:%3" : "=v"(d) : "v"(voff), "s"(rs), "i"(OFF) : "memory"); }
DEV void ld8(u2& d, unsigned voff, u4 rs, unsigned soff) {
    asm volatile("buffer_load_dwordx2 %0, %1, %2, %3 offen sc1" : "=v"(d) : "v"(voff), "s"(rs), "s"(soff) : "memory"); }
DEV void ld16_v(u4& d0, u4& d1, unsigned voff, u4 rs, unsigned soff) {
    asm volatile("s_mov_b64 exec, %5\n\tbuffer_load_dwordx4 %0, %2, %3, %4 offen offset:1024 sc1\n\tbuffer_load_dwordx4 %1, %2, %3, %4 offen offset:1536 sc1\n\ts_mov_b64 exec, -1"
                 : "+v"(d0), "+v"(d1) : "v"(voff), "s"(rs), "s"(soff), "s"(0x0000FFFF0000FFFFull) : "memory"); }
DEV void st16(u4 d, unsigned voff, u4 rs, unsigned soff) {
    asm volatile("buffer_store_dwordx4 %0, %1, %2, %3 offen sc1\n\ts_nop 1" ::"v"(d), "v"(voff), "s"(rs), "s"(soff) : "memory"); }
DEV void st16_plain(u4 d, unsigned voff, u4 rs, unsigned soff) {
    asm volatile("buffer_store_dwordx4 %0, %1, %2, %3 offen\n\ts_nop 1" ::"v"(d), "v"(voff), "s"(rs), "s"(soff) : "memory"); }
DEV void st8(u2 d, unsigned voff, u4 rs, unsigned soff) {
    asm volatile("buffer_store_dwordx2 %0, %1, %2, %3 offen sc1\n\ts_nop 1" ::"v"(d), "v"(voff), "s"(rs), "s"(soff) : "memory"); }
#define WAITV1(N, a) asm volatile("s_waitcnt vmcnt(%1)" : "+v"(a) : "i"(N) : "memory")
#define WAITV1D(ND, NS, a) asm volatile("s_waitcnt vmcnt(%1)\n\ts_cmp_lt_u32 %3, 4\n\ts_cbranch_scc1 .Lw%=\n\ts_waitcnt vmcnt(%2)\n.Lw%=:" : "+v"(a) : "i"(ND), "i"(NS), "s"(w) : "memory", "scc")
#define WAITV3(N, a, b, c) asm volatile("s_waitcnt vmcnt(%3)" : "+v"(a), "+v"(b), "+v"(c) : "i"(N) : "memory")
#define WAITV4(N, a, b, c, d) asm volatile("s_waitcnt vmcnt(%4)" : "+v"(a), "+v"(b), "+v"(c), "+v"(d) : "i"(N) : "memory")

DEV u4 lds16(unsigned a) { return *(LAS const u4*)a; }
DEV f4 lds16f(unsigned a) { return *(LAS const f4*)a; }
DEV float lds4f(unsigned a) { return *(LAS const float*)a; }
DEV void sts16(unsigned a, u4 v) { *(LAS u4*)a = v; }
DEV void sts16f(unsigned a, f4 v) { *(LAS f4*)a = v; }
DEV void sts8f(unsigned a, f2 v) { *(LAS f2*)a = v; }
DEV void sts4f(unsigned a, float v) { *(LAS float*)a = v; }
DEV u4 ldtr(unsigned a0, unsigned a1) {
    const s4 lo = __builtin_amdgcn_ds_read_tr16_b64_v4i16((LAS s4*)a0), hi = __builtin_amdgcn_ds_read_tr16_b64_v4i16((LAS s4*)a1);
    return __builtin_bit_cast(u4, __builtin_shufflevector(lo, hi, 0, 1, 2, 3, 4, 5, 6, 7)); }
DEV unsigned pk2(f2 a) { return __builtin_bit_cast(unsigned, __builtin_convertvector(a, bf2)); }
DEV f2 lo2(f4 a) { return (f2){a[0], a[1]}; }
DEV f2 hi2(f4 a) { return (f2){a[2], a[3]}; }
DEV unsigned pk(float a, float b) { return __builtin_bit_cast(unsigned, __builtin_convertvector((f2){a, b}, bf2)); }
DEV float blo(unsigned v) { return __builtin_bit_cast(float, v << 16); }
DEV float bhi(unsigned v) { return __builtin_bit_cast(float, v & 0xffff0000u); }
DEV float fu(unsigned v) { return __builtin_bit_cast(float, v); }
DEV unsigned uf(float v) { return __builtin_bit_cast(unsigned, v); }
DEV float add_x32(float v) { auto r = __builtin_amdgcn_permlane32_swap(uf(v), uf(v), false, false); const unsigned a = r[0], b = r[1]; return fu(a) + fu(b); }
DEV float add_x16(float v) { auto r = __builtin_amdgcn_permlane16_swap(uf(v), uf(v), false, false); const unsigned a = r[0], b = r[1]; return fu(a) + fu(b); }
DEV float max_x32(float v) { auto r = __builtin_amdgcn_permlane32_swap(uf(v), uf(v), false, false); const unsigned a = r[0], b = r[1]; return __builtin_fmaxf(fu(a), fu(b)); }
template <int C> DEV float add_dpp(float v) { return v + fu(__builtin_amdgcn_update_dpp(0u, uf(v), C, 0xf, 0xf, true)); }
DEV float add_x8(float v) { return v + fu(__builtin_amdgcn_update_dpp(0u, uf(v), 0x128, 0xf, 0xf, true)); }
constexpr u64 diag_mask(int r) { u64 m = 0; for (int l = 0; l < 64; ++l) { const int key = 16 * ((r >> 2) & 1) + 8 * (l >> 5) + 4 * (r >> 3) + (r & 3); if (key <= (l & 31)) m |= 1ull << l; } return m; }
DEV float sel(u64 m, float t, float f) { return __builtin_amdgcn_inverse_ballot_w64(m) ? t : f; }

template <unsigned LDS> DEV void touch(unsigned voff, u4 rs, unsigned soff) {
    asm volatile("s_mov_b32 m0, %3\n\ts_nop 0\n\tbuffer_load_dword %0, %1, %2 offen lds" ::"v"(voff), "s"(rs), "s"(soff), "i"(LDS) : "memory"); }
typedef short sh2 __attribute__((ext_vector_type(2)));
constexpr int kgo(int i) { return i < 8 ? 4 * i : (i - 8) / 3 * 4 + (i - 8) % 3 + 1; }
#define RSLEEP 4
DEV bool any_lane(bool c) { return __builtin_amdgcn_ballot_w64(c) != 0; }
#define UNLIKELY(x) __builtin_expect(!!(x), 0)
#define NRETRY()
#define FRESH16(d, voff, rs, soff) do { if (UNLIKELY(any_lane((d)[3] == ~0u))) { \
        _Pragma("unroll 1") for (int s_ = 0; s_ < SPIN_CAP; ++s_) { NRETRY(); u4 t_; asm volatile("buffer_load_dwordx4 %0, %1, %2, %3 offen sc1\n\ts_waitcnt vmcnt(0)" : "=&v"(t_) : "v"(voff), "s"(rs), "s"(soff) : "memory"); \
            d = t_; if (!any_lane(t_[3] == ~0u)) break; __builtin_amdgcn_s_sleep(2); } } } while (0)
#define POLL(voff) do { \
        _Pragma("unroll 1") for (int s_ = 0; s_ < 4 * SPIN_CAP; ++s_) { u2 t_; asm volatile("buffer_load_dwordx2 %0, %1, %2, 0 offen sc1\n\ts_waitcnt vmcnt(0)" : "=&v"(t_) : "v"(voff), "s"(rsF) : "memory"); \
            if (!any_lane(t_[0] != tag[0] || t_[1] != tag[1])) break; NRETRY(); __builtin_amdgcn_s_sleep(RSLEEP); } } while (0)
#define FLAG(off) st8(tag, l == 0 ? (off) : OOB, rsF, 0)
#define STAMP(i)

__global__ __launch_bounds__(512, 2) void fused_block(const char* x_, const char* wqkv_, const char* wo_, const char* wg_, const char* wu_, const char* wd_, char* out_, char* ws_) {
#if defined(__HIP_DEVICE_COMPILE__)

    typedef u64 u64x8 __attribute__((ext_vector_type(8)));
    u64x8 ka; asm volatile("s_load_dwordx16 %0, %1, 0x0\n\ts_waitcnt lgkmcnt(0)" : "=s"(ka) : "s"(__builtin_amdgcn_kernarg_segment_ptr()) : "memory");

    const char *x = (const char*)ka[0], *wqkv = (const char*)ka[1], *wo = (const char*)ka[2], *wg = (const char*)ka[3], *wu = (const char*)ka[4], *wd = (const char*)ka[5]; char *out = (char*)ka[6], *ws = (char*)ka[7];
    const unsigned l = threadIdx.x & 63, w = __builtin_amdgcn_readfirstlane(threadIdx.x >> 6);
    const unsigned b = blockIdx.x, xc = b & 7, rk = b >> 3;
    const unsigned T64 = 8 * (xc >> 1) + (rk >> 2), hq = 4 * (xc & 1) + (rk & 3);
    STAMP(0);
    const unsigned u8 = l >> 3, l7 = l & 7;
    u4 xr[8];
    const u4 rsX = mk_rsrc(x + T64 * 65536u, 65536u);
    const unsigned vx = (8 * w + u8) * 1024 + l7 * 16;
#define HOT 7
    {
        const unsigned hs = (3u - (unsigned)((u64)wqkv >> 7)) & 7u;
        if ((hs >> 2) == (xc & 1) && w == HOT) { const unsigned z = rk * 48 + (l < 48 ? l : 0); const char* p = wqkv + (hs * 128 + z * 1024); asm volatile("global_load_dword v254, %0, off" ::"v"(p) : "memory", "v254"); } }
    ld16_imm<0>(xr[0], vx, rsX); ld16_imm<128>(xr[1], vx, rsX); ld16_imm<256>(xr[2], vx, rsX); ld16_imm<384>(xr[3], vx, rsX);
    if (XB > 4) { ld16_imm<512>(xr[4], vx, rsX); ld16_imm<640>(xr[5], vx, rsX); }
    if (XB > 6) { ld16_imm<768>(xr[6], vx, rsX); ld16_imm<896>(xr[7], vx, rsX); }
    const u4 rsW = mk_rsrc(wqkv + hq * 128u, 512u * 3072u - hq * 128u);
    SBAR();
    const unsigned w3 = w & 3, wk2 = w3 * 2048;
    unsigned vw0 = (16 * w3 + u8) * 3072 + 32 * ((l7 >> 1) ^ ((l >> 4) & 1)) + 16 * (l7 & 1), vw1 = (vw0 + 24576) ^ 64;
#define SLOT(i) ((((i) + 1) & 3) * 24576u)
#define SP(i, p) dma_bufi<SLOT(i) + ((p) >> 1) * 8192 + ((p) & 1) * 1024, (i) * 196608u + ((p) >> 1) * 1024u>(wk2, ((p) & 1) ? vw1 : vw0, rsW, 0)
    const unsigned U64 = 4 * xc + (rk >> 3), fb = rk & 7;
    const unsigned g = 2 * (xc >> 1) + (rk >> 4), r = 16 * (xc & 1) + (rk & 15);
    const unsigned j = w & 1, hh = w >> 1;
    const unsigned t16 = l & 15, g4 = l >> 4, h = l >> 5, t32 = l & 31, q4 = t16 >> 2, pp = l & 3, l16 = l * 16, l128 = l * 128;
    const u4 rsF = mk_rsrc(ws, 16u << 20);
    const u4 ones = {0x3f803f80u, 0x3f803f80u, 0x3f803f80u, 0x3f803f80u};
    const unsigned kvb = WS_KV + (4 * hq + hh) * KVS;
    const unsigned skv = kvb + (2 * T64 + j) * 2048;
    const unsigned sOw = WS_O + (2 * T64 + j) * OS + (4 * hq + hh) * 1024;
    u2 tag; u4 xres;
    unsigned vres = (32 * j + 8 * hh + l7) * 1024 + fb * 128 + u8 * 16;
    unsigned xw0 = L_A + (8 * w + u8) * 1024 + 16 * (l7 ^ (8 * (w & 1) + u8)), xw1 = xw0 ^ 128;
    unsigned xa[4];
#pragma unroll
    for (int q = 0; q < 4; ++q) { xa[q] = L_A + (32 * j + t16) * 1024 + 16 * ((4 * q + g4) ^ t16); asm volatile("" : "+v"(xa[q])); }
    unsigned wa = (8 * g4 + q4) * 128 + 32 * (hh ^ ((q4 >> 1) | ((g4 & 1) << 1))) + 8 * pp;
    f4 aq[2] = {}, ak[2] = {}, av[2] = {};
    unsigned vk = (h * 32 + (16 * ((t32 >> 3) & 1) + 8 * ((t32 >> 2) & 1) + 4 * (t32 >> 4) + (t32 & 3))) * 16, vv_ = (h * 16 + t16) * 16;
    unsigned vst = 1024 + ((g4 & 1) * 32 + (g4 >> 1) * 16 + t16) * 16;
    unsigned vrow = u8 * 1024 + 16 * (l7 ^ (((l >> 4) & 1) << 2));
    const char* gp[3];
#pragma unroll
    for (int i = 0; i < 3; ++i) { const unsigned z = 64 * i + l, k = (z * 43691u) >> 19, c = z - 12 * k, m = c >= 6, cc = c - 6 * m; unsigned un = 3 * r + (cc >> 1); un = un > 85 ? 85 : un;
        gp[i] = (m ? wu : wg) + ((16 * w + k) * 2752 + un * 32 + (cc & 1) * 16); asm volatile("" : "+v"(gp[i])); }
    const unsigned wk3 = w * 3072;
#define GU(R, c) dma_glbi<(R) * 24576 + (c) * 1024>(wk3, gp[c] + (R) * 128 * 2752)
#define GUD(R, q) dma_glbi<(R) * 24576 + ((q) / 3) * 12288 + ((q) % 3) * 1024>(wk3, gp[(q) % 3] + ((R) * 128 + ((q) / 3) * 64) * 2752)
    asm volatile("" : "+v"(vw0), "+v"(vw1), "+v"(xw0), "+v"(xw1), "+v"(wa), "+v"(vres), "+v"(vk), "+v"(vv_), "+v"(vrow), "+v"(vst));
    STAMP(39);
    if (XB <= 4) { ld16_imm<512>(xr[4], vx, rsX); ld16_imm<640>(xr[5], vx, rsX); }
    if (XB <= 6) { ld16_imm<768>(xr[6], vx, rsX); ld16_imm<896>(xr[7], vx, rsX); }
#define TAMODE 0
    {
        const char* pc; unsigned nb; asm volatile("s_getpc_b64 %0\n.Lfb_code0:\n\ts_mov_b32 %1, .Lfb_code1-.Lfb_code0" : "=s"(pc), "=s"(nb));
        const unsigned co = ((rk * 8 + w) * 2 + (l & 1)) * 128;
        const char* p = TAMODE == 1 ? (l < 24 ? wqkv + (hq * 128 + (w * 64 + (T64 & 7) * 8 + l7) * 3072 + u8 * 1024) : pc + (co < nb ? co : 0))
                                    : (l < 6 ? wqkv + ((b * 8 + w) * 6 + l) * 128 : pc + (co < nb ? co : 0));
        asm volatile("global_load_dword v254, %0, off" ::"v"(p) : "memory", "v254"); }
#define NO ((void)0)
#define HOTB(wv, base, n) do { if (w == (wv)) { const char* p_ = base + ((((3u - (unsigned)((u64)base >> 7)) & 7u) * 128) + (rk * (n) + (l < (n) ? l : 0)) * 1024); asm volatile("global_load_dword v254, %0, off" ::"v"(p_) : "memory", "v254"); } } while (0)
#define TBPOS 0
#define TOUCH_B() do { const char* tp = (w == 4 ? wo : w == 5 ? wg : w == 6 ? wu : wd) + (w == 4 ? b * 16 + (l & 15) : b * 43 + (l < 43 ? l : 0)) * 128; \
        asm volatile("global_load_dword v255, %0, off" ::"v"(tp) : "memory", "v255"); } while (0)
#if !defined(NOTB) && TBPOS == 0
    if (w >= 4) TOUCH_B();
#define XW 1
#else
#define XW 0
#endif
#define SSTAMP(i)
    {
        f2 xf[32], s1 = {0.f, 0.f}, s2 = {0.f, 0.f};
#define LN_SUM(i, N, D0, D1) do { if (XW && (N) < 8 - (i)) WAITV1((N) + 1, xr[i]); else WAITV1D((N) + 1, 8 - (i) + XW, xr[i]); _Pragma("unroll") for (int e = 0; e < 4; ++e) { const unsigned d_ = xr[i][e]; f2 v_ = {blo(d_), bhi(d_)}; asm volatile("" : "+v"(v_)); xf[4 * (i) + e] = v_; \
        s1 += v_; s2 += v_ * v_; } asm volatile("" : "+v"(s1), "+v"(s2)); if (w < 4) { D0; D1; } SBAR(); } while (0)
        LN_SUM(0, 7, NO, NO); LN_SUM(1, 6, NO, NO); LN_SUM(2, 5, NO, NO); LN_SUM(3, 4, NO, NO);
        LN_SUM(4, 3, SP(0, 0), SP(0, 1)); LN_SUM(5, 4, SP(0, 2), SP(0, 3)); LN_SUM(6, 5, SP(0, 4), NO); LN_SUM(7, 5, SP(0, 5), NO);
        SSTAMP(34);
        if (w < 4) { SP(1, 0); SP(1, 1); SP(1, 2); SP(1, 3); SP(1, 4); SP(1, 5); }
#if !defined(NOTB) && TBPOS == 1
        else TOUCH_B();
#endif
        const float t1 = add_dpp<0x141>(add_dpp<0x4e>(add_dpp<0xb1>(s1[0] + s1[1]))), t2 = add_dpp<0x141>(add_dpp<0x4e>(add_dpp<0xb1>(s2[0] + s2[1])));
        const float mu = t1 * (1.f / 512), rs = __builtin_amdgcn_rsqf(__builtin_fmaxf(t2 * (1.f / 512) - mu * mu, 0.f) + 1e-5f), mr = -mu * rs;
#pragma unroll
        for (int i = 0; i < 8; ++i) sts16(((i & 1) ? xw1 : xw0) + (i >> 1) * 256, (u4){pk2(xf[4 * i] * rs + mr), pk2(xf[4 * i + 1] * rs + mr), pk2(xf[4 * i + 2] * rs + mr), pk2(xf[4 * i + 3] * rs + mr)});
    }
#define P1_STEP(n, D0, D1, D2) do { constexpr int c_ = (n) & 1, x_ = c_ ^ 1; constexpr bool m_ = (n) < 15; \
        const unsigned xb = xa[((n) + 1) & 3] + (((n) + 1) >> 2) * 256, wb = wa + SLOT(((n) + 1) >> 1) + x_ * 4096; \
        if (m_) X0[x_] = lds16(xb); SBAR(); \
        aq[0] = MFMA16(Wq[c_], X0[c_], aq[0]); if (m_) Wq[x_] = ldtr(wb, wb + 512); SBAR(); \
        aq[1] = MFMA16(Wq[c_], X1[c_], aq[1]); if (m_) X1[x_] = lds16(xb + 16384); D0; SBAR(); \
        ak[0] = MFMA16(Wk[c_], X0[c_], ak[0]); if (m_) Wk[x_] = ldtr(wb + 8192, wb + 8704); SBAR(); \
        ak[1] = MFMA16(Wk[c_], X1[c_], ak[1]); D1; SBAR(); \
        av[0] = MFMA16(X0[c_], Wv[c_], av[0]); if (m_) Wv[x_] = ldtr(wb + 16384, wb + 16896); SBAR(); \
        av[1] = MFMA16(X1[c_], Wv[c_], av[1]); D2; SBAR(); } while (0)
#define P1_FIRST() do { const unsigned wb = wa + SLOT(0); X0[0] = lds16(xa[0]); Wq[0] = ldtr(wb, wb + 512); X1[0] = lds16(xa[0] + 16384); Wk[0] = ldtr(wb + 8192, wb + 8704); Wv[0] = ldtr(wb + 16384, wb + 16896); } while (0)
#define XRES() ld16_plain(xres, vres, mk_rsrc(x + U64 * 65536u, 65536u), 0)
    if (w < 4) {
        u4 X0[2], X1[2], Wq[2], Wk[2], Wv[2];
        SP(2, 0); SP(2, 1); SP(2, 2); SP(2, 3); SP(2, 4); SP(2, 5);
        BAR_VM(12); STAMP(16);  P1_FIRST(); P1_STEP(0, (SP(3, 0), SP(3, 1)), (SP(3, 2), SP(3, 3)), (SP(3, 4), SP(3, 5)));
        BAR_VM(12); SSTAMP(17); P1_STEP(1, SP(4, 0), SP(4, 1), SP(4, 2)); P1_STEP(2, SP(4, 3), SP(4, 4), SP(4, 5));
        BAR_VM(12); SSTAMP(18); P1_STEP(3, SP(5, 0), SP(5, 1), SP(5, 2)); P1_STEP(4, SP(5, 3), SP(5, 4), SP(5, 5));
        BAR_VM(12); SSTAMP(19); P1_STEP(5, SP(6, 0), SP(6, 1), SP(6, 2)); P1_STEP(6, SP(6, 3), SP(6, 4), SP(6, 5));
        BAR_VM(12); SSTAMP(20); P1_STEP(7, SP(7, 0), SP(7, 1), SP(7, 2)); P1_STEP(8, SP(7, 3), SP(7, 4), SP(7, 5));
        BAR_VM(12); SSTAMP(21); P1_STEP(9, XRES(), GUD(1, 0), NO); P1_STEP(10, GUD(1, 1), GUD(1, 2), NO);
        BAR_VM(10); SSTAMP(22); P1_STEP(11, GUD(1, 3), GUD(1, 4), NO); P1_STEP(12, GUD(1, 5), NO, NO);
        BAR_VM(7);  SSTAMP(23); P1_STEP(13, NO, NO, NO); P1_STEP(14, NO, NO, NO); P1_STEP(15, NO, NO, NO);
        WAITV1(6, xres); tag = (u2){0u, 0u};
    } else {
        u4 X0[2], X1[2], Wq[2], Wk[2], Wv[2];
        const u4 bad = {~0u, ~0u, ~0u, ~0u};
#define MK(v, s_) st16(bad, v, rsF, s_)
#if !defined(NOTB) && TBPOS == 2
#define TCH(c) if (w == (c)) TOUCH_B()
#else
#define TCH(c) NO
#endif
        BAR_L(); STAMP(16);  P1_FIRST(); P1_STEP(0, ld8(tag, 0u * l, rsF, 0), MK(l16, skv), TCH(5));
        BAR_L(); SSTAMP(17); P1_STEP(1, NO, MK(l16 + 1024, skv), TCH(6)); P1_STEP(2, NO, MK(l16, skv - 2 * KVS), NO);
        BAR_L(); SSTAMP(18); P1_STEP(3, NO, MK(l16 + 1024, skv - 2 * KVS), TCH(4)); P1_STEP(4, NO, MK(l16, sOw), NO);
        BAR_L(); SSTAMP(19); P1_STEP(5, NO, MK(l16, sOw - 2048), TCH(7)); P1_STEP(6, XRES(), NO, NO);
        BAR_L(); SSTAMP(20); P1_STEP(7, NO, NO, NO); P1_STEP(8, NO, NO, NO);
        BAR_L(); SSTAMP(21); P1_STEP(9, NO, NO, NO); P1_STEP(10, NO, NO, NO);
        asm volatile("s_waitcnt vmcnt(0)" : "+v"(tag), "+v"(xres) :: "memory");
        BAR_L(); SSTAMP(22);
        { const unsigned lo = tag.x + 1u; tag.y += (lo == 0u); tag.x = lo; }
                             P1_STEP(11, FLAG(w == 4 ? WS_F1 + (T64 * 8 + hq) * 128 : OOB), NO, NO); P1_STEP(12, HOTB(6, wo, 16), NO, NO);
        BAR_L(); SSTAMP(23); P1_STEP(13, NO, NO, NO); P1_STEP(14, NO, NO, NO); P1_STEP(15, NO, NO, NO);
    }
    STAMP(1);
    u4 Qf;
    {
        { auto a = __builtin_amdgcn_permlane16_swap(pk2(lo2(ak[0])), pk2(lo2(ak[1])), false, false); auto c = __builtin_amdgcn_permlane16_swap(pk2(hi2(ak[0])), pk2(hi2(ak[1])), false, false);
          st16((u4){a[0], c[0], a[1], c[1]}, l16, rsF, skv); }
        st16((u4){pk2(lo2(av[0])), pk2(hi2(av[0])), pk2(lo2(av[1])), pk2(hi2(av[1]))}, vst, rsF, skv);
        STAMP(32);
        SBAR();
        constexpr float QS = 0.25f * 1.4426950408889634f;
        { auto a = __builtin_amdgcn_permlane16_swap(pk2(lo2(aq[0]) * QS), pk2(lo2(aq[1]) * QS), false, false); auto c = __builtin_amdgcn_permlane16_swap(pk2(hi2(aq[0]) * QS), pk2(hi2(aq[1]) * QS), false, false);
          Qf = (u4){a[0], c[0], a[1], c[1]}; }
        sts16(L_OC + (w ^ 1) * 2304 + l16, Qf);
    }
    const int Tb = 2 * (int)T64 - 8;
    u4 K[5], V0[5], V1[5];
    const int blk[5] = {Tb + 8 + (int)j, Tb + (int)j, Tb + 2 + 3 * (int)j, Tb + 3 + 3 * (int)j, Tb + 4 + 3 * (int)j};
#pragma unroll
    for (int i = 0; i < 5; ++i) { const unsigned o = blk[i] < 0 ? 0u : 0x3f803f80u; V0[i] = (u4){o, o, o, o}; V1[i] = V0[i]; asm volatile("" : "+v"(V0[i]), "+v"(V1[i])); }
    unsigned pm[8];
    { const short uq = (short)(t32 - 8 * h);
#pragma unroll
      for (int i = 0; i < 8; ++i) { const short c = (short)(16 * ((i >> 1) & 1) + 4 * (i >> 2) + 2 * (i & 1)); pm[i] = __builtin_bit_cast(unsigned, ((sh2){uq, uq} - (sh2){c, (short)(c + 1)}) >> (sh2){15, 15}); asm volatile("" : "+v"(pm[i])); } }
    unsigned so[5];
#pragma unroll
    for (int i = 0; i < 5; ++i) so[i] = blk[i] < 0 ? OOB : kvb + (unsigned)blk[i] * 2048;
    if (w == 7) {
        const int Tp = (int)T64 - 1 - (int)(l & 3); const unsigned vc = WS_F1 + ((Tp < 0 ? T64 : (unsigned)Tp) * 8 + hq) * 128; POLL(vc); }
    STAMP(35);
    BAR_VM(0);
    STAMP(2);
#define KVLD(i) do { ld16(K[i], vk, rsF, so[i]); ld16_v(V0[i], V1[i], vv_, rsF, so[i]); } while (0)
#define KVOK(N, i) do { WAITV3(N, K[i], V0[i], V1[i]); \
        if (UNLIKELY(any_lane(max(max(K[i][3], V0[i][3]), V1[i][3]) == ~0u))) { \
            _Pragma("unroll 1") for (int s_ = 0; s_ < SPIN_CAP; ++s_) { NRETRY(); KVLD(i); WAITV3(0, K[i], V0[i], V1[i]); if (!any_lane(max(max(K[i][3], V0[i][3]), V1[i][3]) == ~0u)) break; __builtin_amdgcn_s_sleep(2); } } } while (0)
    KVLD(2); KVLD(3);
    const u4 Qm = Qf, Qo = lds16(L_OC + w * 2304 + l16);
    f16 Om = {}, Oo = {};
    const f16 z16 = {};
    auto pv_tile = [&](const f16& S, const u4& va, const u4& vb, f16& O) {
        float p[16];
#pragma unroll
        for (int e = 0; e < 16; ++e) p[e] = __builtin_amdgcn_exp2f(S[e]);
        const u4 P0 = {pk(p[0], p[1]), pk(p[2], p[3]), pk(p[4], p[5]), pk(p[6], p[7])}, P1 = {pk(p[8], p[9]), pk(p[10], p[11]), pk(p[12], p[13]), pk(p[14], p[15])};
        O = MFMA32(va, P0, O); O = MFMA32(vb, P1, O);
    };
    const u4 rsWo = mk_rsrc(wo + fb * 128u, 512u * 1024u - fb * 128u);
    const unsigned wol = L_A + w * 8192, wos = w * 65536;
    const unsigned vh = (u8 >> 1) * 1024 + ((u8 & 1) * 32 + 8 * hh + l7) * 16, vs = l < 8 ? (8 * hh + l) * 8 : OOB;
    const unsigned sHw = WS_H + (2 * U64 + j) * OS + fb * 4096, sSw = WS_ST + (2 * U64 + j) * 2048u + fb * 256;
    const unsigned sGa = r < 29 ? WS_G + (8 * g + w) * 90112u + r * 2048 : OOB, sGb = r < 28 ? WS_G + (8 * g + w) * 90112u + (60 + r) * 1024 : OOB;
#define WO_(i) dma_bufi<(i) * 1024, (i) * 8192>(wol, vrow, rsWo, wos)
#define PIN16(a) asm volatile("" : "+v"(a))
#define MEM(...) do { SBAR(); __VA_ARGS__; SBAR(); } while (0)
#define HALF(S, o, va, O) do { float p_[8]; _Pragma("unroll") for (int e = 0; e < 8; ++e) p_[e] = __builtin_amdgcn_exp2f(S[(o) + e]); \
        u4 q_ = {pk(p_[0], p_[1]), pk(p_[2], p_[3]), pk(p_[4], p_[5]), pk(p_[6], p_[7])}; asm volatile("" : "+v"(q_)); O = MFMA32(va, q_, O); PIN16(O); SBAR(); } while (0)
#define SCORE(S, k, q) do { S = MFMA32(k, q, z16); PIN16(S); SBAR(); } while (0)
    f16 Sa, Sb, Sc;
    u2 ck; const unsigned vc1 = WS_F1 + (U64 * 8 + l7) * 128;
    const u4 bad = {~0u, ~0u, ~0u, ~0u};
    KVOK(3, 2);
    STAMP(24);
    SCORE(Sa, K[2], Qm); SCORE(Sb, K[2], Qo); MEM(KVLD(4));
    HALF(Sa, 0, V0[2], Om); MEM(KVLD(0)); HALF(Sa, 8, V1[2], Om); MEM(KVLD(1)); KVOK(9, 3);
    SCORE(Sa, K[3], Qm); HALF(Sb, 0, V0[2], Oo); MEM(WO_(0)); HALF(Sb, 8, V1[2], Oo); MEM(WO_(1));
    SSTAMP(42);
    SCORE(Sb, K[3], Qo); HALF(Sa, 0, V0[3], Om); MEM(WO_(2)); HALF(Sa, 8, V1[3], Om); MEM(WO_(3)); KVOK(10, 4);
    SCORE(Sa, K[4], Qm); HALF(Sb, 0, V0[3], Oo); MEM(WO_(4)); HALF(Sb, 8, V1[3], Oo); MEM(WO_(5));
    SSTAMP(43);
    SCORE(Sb, K[4], Qo); HALF(Sa, 0, V0[4], Om); MEM(WO_(6)); HALF(Sa, 8, V1[4], Om); MEM(WO_(7)); KVOK(11, 0); KVOK(8, 1);
    if (j == 0) { SCORE(Sa, K[0], Qo); HALF(Sb, 0, V0[4], Oo); MEM(st16(bad, vh, rsF, sHw)); HALF(Sb, 8, V1[4], Oo); MEM(st8((u2){~0u, ~0u}, vs, rsF, sSw), st16(bad, l16, rsF, sGb));
                  SCORE(Sb, K[0], Qm); SCORE(Sc, K[1], Qm); HALF(Sa, 0, V0[0], Oo); HALF(Sa, 8, V1[0], Oo); }
    else        { SCORE(Sa, K[1], Qo); HALF(Sb, 0, V0[4], Oo); MEM(st16(bad, vh, rsF, sHw)); HALF(Sb, 8, V1[4], Oo); MEM(st8((u2){~0u, ~0u}, vs, rsF, sSw), st16(bad, l16, rsF, sGb));
                  SCORE(Sb, K[0], Qm); SCORE(Sc, K[1], Qm); HALF(Sa, 0, V0[1], Oo); HALF(Sa, 8, V1[1], Oo); }
    SSTAMP(44);
    MEM(ld8(ck, w == 7 ? vc1 : OOB, rsF, 0), GU(2, 0), GU(2, 1), GU(2, 2));
    const unsigned oc = L_OC + w * 2304;
    {
        const f16 Sx = Sb, Sy = Sc;
        float p[16];
#pragma unroll
        for (int e = 0; e < 8; ++e) p[e] = __builtin_amdgcn_exp2f(sel(diag_mask(e), Sx[e], Sy[e]));
        SBAR();
        sts16f(oc + l16, (f4){Oo[0], Oo[1], Oo[2], Oo[3]}); sts16f(oc + 1024 + l16, (f4){Oo[4], Oo[5], Oo[6], Oo[7]}); sts4f(oc + 2048 + l * 4, Oo[8]);
#pragma unroll
        for (int e = 8; e < 16; ++e) p[e] = __builtin_amdgcn_exp2f(sel(diag_mask(e), Sx[e], Sy[e]));
        unsigned P[8];
#pragma unroll
        for (int e = 0; e < 8; ++e) P[e] = pk(p[2 * e], p[2 * e + 1]);
        const u4 Y0 = {P[0] & pm[0], P[1] & pm[1], P[2] & pm[2], P[3] & pm[3]}, Y1 = {P[4] & pm[4], P[5] & pm[5], P[6] & pm[6], P[7] & pm[7]};
        const u4 X0_ = {P[0] ^ Y0[0], P[1] ^ Y0[1], P[2] ^ Y0[2], P[3] ^ Y0[3]}, X1_ = {P[4] ^ Y1[0], P[5] ^ Y1[1], P[6] ^ Y1[2], P[7] ^ Y1[3]};
        Om = MFMA32(V0[0], X0_, Om); Om = MFMA32(V1[0], X1_, Om); Om = MFMA32(V0[1], Y0, Om); Om = MFMA32(V1[1], Y1, Om);
    }
    SBAR();
    STAMP(25);
    asm volatile("s_waitcnt vmcnt(3) lgkmcnt(0)\n\ts_barrier" : "+v"(ck) :: "memory");
    SSTAMP(45);
    const unsigned op = L_OC + (w ^ 1) * 2304;
    f4 oa = lds16f(op + l16) + (f4){Om[0], Om[1], Om[2], Om[3]}, ob = lds16f(op + 1024 + l16) + (f4){Om[4], Om[5], Om[6], Om[7]};
    float den = Om[8] + lds4f(op + 2048 + l * 4);
    if (UNLIKELY(any_lane(!(den >= 0x1p-60f && den <= 0x1p60f)))) {
        f16 O = {}; float m = -__builtin_inff();
#pragma unroll 1
        for (int kb = 8; kb >= 0; --kb) {
            const int T = 2 * (int)T64 + (int)j - 8 + kb;
            if (T < 0) break;
            u4 Ks, Va = ones, Vb = ones;
            ld16(Ks, vk, rsF, kvb + (unsigned)T * 2048); ld16_v(Va, Vb, vv_, rsF, kvb + (unsigned)T * 2048);
            WAITV3(0, Ks, Va, Vb);
            f16 S = MFMA32(Ks, Qm, z16);
            const u64 all = (kb != 0 && kb != 8) ? ~0ull : 0ull, inv = kb == 0 ? ~0ull : 0ull;
            float mx = m;
#pragma unroll
            for (int e = 0; e < 16; ++e) { S[e] = sel((diag_mask(e) ^ inv) | all, S[e], -__builtin_inff()); mx = __builtin_fmaxf(mx, S[e]); }
            mx = max_x32(mx);
            const float al = __builtin_amdgcn_exp2f(m - mx); m = mx;
#pragma unroll
            for (int e = 0; e < 16; ++e) { O[e] *= al; S[e] -= mx; }
            pv_tile(S, Va, Vb, O);
        }
        oa = (f4){O[0], O[1], O[2], O[3]}; ob = (f4){O[4], O[5], O[6], O[7]}; den = O[8];
    }
    const float rd = __builtin_amdgcn_rcpf(den);
    st16((u4){pk2(lo2(oa) * rd), pk2(hi2(oa) * rd), pk2(lo2(ob) * rd), pk2(hi2(ob) * rd)}, l16, rsF, sOw);
    FLAG(w == 4 ? WS_F2 + (U64 * 8 + fb) * 128 : w == 5 ? WS_F3 + (g * 32 + r) * 128 : OOB);
    STAMP(26);
    const unsigned ta0 = (4 * h + q4) * 128 + 64 * (q4 >> 1) + 32 * (g4 & 1) + 8 * pp, ta1 = ta0 ^ 64;
    f16 acc0 = {}, acc1 = {};
    {
        const unsigned sO = WS_O + (2 * U64 + j) * OS + hh * 8192;
        const unsigned oa0 = L_A + hh * 16384 + ta0, oa1 = L_A + hh * 16384 + ta1;
        u4 A0[8], A1[8], B[8];
#pragma unroll
        for (int i = 0; i < 8; ++i) { A0[i] = ldtr(oa0 + i * 2048, oa0 + i * 2048 + 1024); A1[i] = ldtr(oa1 + i * 2048, oa1 + i * 2048 + 1024); }
        if (w == 7 && UNLIKELY(any_lane(ck[0] != tag[0] || ck[1] != tag[1]))) POLL(vc1);
        STAMP(37);
        BAR_VM(1);
        STAMP(5);
#pragma unroll
        for (int i = 0; i < 8; ++i) ld16(B[i], l16, rsF, sO + i * 1024);
#define P3_PAIR(i, NW, M0, M1) do { asm volatile("s_waitcnt vmcnt(%2)" : "+v"(B[i]), "+v"(B[(i) + 1]) : "i"(NW) : "memory"); \
            if (UNLIKELY(any_lane(max(B[i][3], B[(i) + 1][3]) == ~0u))) { \
                _Pragma("unroll 1") for (int s_ = 0; s_ < SPIN_CAP; ++s_) { NRETRY(); __builtin_amdgcn_s_sleep(3); asm volatile("s_waitcnt vmcnt(0)" ::: "memory"); unsigned m_ = 0; \
                    _Pragma("unroll") for (int q = (i); q < 8; ++q) ld16(B[q], l16, rsF, sO + q * 1024); \
                    _Pragma("unroll") for (int q = (i); q < 8; ++q) { WAITV1(7 - q, B[q]); m_ = max(m_, B[q][3]); } \
                    if (!any_lane(m_ == ~0u)) break; } } if ((i) == 0) STAMP(4); \
            acc0 = MFMA32(A0[i], B[i], acc0); acc1 = MFMA32(A1[i], B[i], acc1); M0; SBAR(); acc0 = MFMA32(A0[(i) + 1], B[(i) + 1], acc0); acc1 = MFMA32(A1[(i) + 1], B[(i) + 1], acc1); M1; SBAR(); } while (0)
        P3_PAIR(0, 6, NO, NO); P3_PAIR(2, 4, GU(0, 0), GU(0, 1)); P3_PAIR(4, 4, GU(0, 2), GU(3, 0)); P3_PAIR(6, 4, GU(3, 1), GU(3, 2));
    }
    const unsigned vc2 = WS_F2 + (g * 32 + t32) * 128;
    ld8(ck, w == 7 ? vc2 : OOB, rsF, 0);
    STAMP(27);
    const unsigned rw0 = L_A + (hh * 2 + j) * 8192 + h * 512 + t32 * 16, rw1 = rw0 ^ 128, rr = L_A + j * 8192 + u8 * 1024 + (((8 * hh + l7) ^ ((u8 & 1) << 3)) * 16);
    auto reduce = [&](f4& lo, f4& hi) {
#pragma unroll
        for (int u = 0; u < 4; ++u) {
            sts16f(((u & 1) ? rw1 : rw0) + u * 1024, (f4){acc0[4 * u], acc0[4 * u + 1], acc0[4 * u + 2], acc0[4 * u + 3]});
            sts16f(((u & 1) ? rw1 : rw0) + (4 + u) * 1024, (f4){acc1[4 * u], acc1[4 * u + 1], acc1[4 * u + 2], acc1[4 * u + 3]});
        }
        BAR_L();
        lo = (lds16f(rr) + lds16f(rr + 16384)) + (lds16f(rr + 32768) + lds16f(rr + 49152));
        hi = (lds16f(rr + 512) + lds16f(rr + 16896)) + (lds16f(rr + 33280) + lds16f(rr + 49664));
    };
    u4 Hres;
    const unsigned ga = (8 * h + q4) * 192 + (g4 & 1) * 96 + 8 * pp;
    f16 cs[3] = {};
    {
        f4 lo, hi; reduce(lo, hi);
        STAMP(28);
        Hres = (u4){pk2(lo2(lo) + (f2){blo(xres[0]), bhi(xres[0])}), pk2(hi2(lo) + (f2){blo(xres[1]), bhi(xres[1])}), pk2(lo2(hi) + (f2){blo(xres[2]), bhi(xres[2])}), pk2(hi2(hi) + (f2){blo(xres[3]), bhi(xres[3])})};
        st16(Hres, vh, rsF, sHw);
        f2 s1 = {0.f, 0.f}, s2 = {0.f, 0.f};
#pragma unroll
        for (int e = 0; e < 4; ++e) { const f2 a = {blo(Hres[e]), bhi(Hres[e])}; s1 += a; s2 += a * a; }
        const float t1 = add_x32(add_x16(add_x8(s1[0] + s1[1]))), t2 = add_x32(add_x16(add_x8(s2[0] + s2[1])));
        st8((u2){uf(t1), uf(t2)}, vs, rsF, sSw);
        STAMP(33);
        asm volatile("s_waitcnt vmcnt(2) lgkmcnt(0)\n\ts_barrier" : "+v"(ck) :: "memory");
        STAMP(40);
        const unsigned o = ga + w * 12288;
        u4 a[12];
#pragma unroll
        for (int i = 0; i < 12; ++i) a[i] = ldtr(o + (i / 3) * 3072 + (i % 3) * 32, o + (i / 3) * 3072 + (i % 3) * 32 + 768);
        SBAR();
#pragma unroll
        for (int i = 0; i < 12; ++i) cs[i % 3] = MFMA32(a[i], ones, cs[i % 3]);
        asm volatile("" : "+v"(cs[0]), "+v"(cs[1]), "+v"(cs[2]));
    }
    STAMP(36);
    const u4 rsWd = mk_rsrc(wd + fb * 128u, 1376u * 1024u - fb * 128u);
    if (t32 == 0) {
#pragma unroll
        for (int u = 0; u < 3; ++u)
#pragma unroll
            for (int qd = 0; qd < 4; ++qd) sts16f(L_CSP + w * 384 + u * 128 + qd * 32 + h * 16, (f4){cs[u][4 * qd], cs[u][4 * qd + 1], cs[u][4 * qd + 2], cs[u][4 * qd + 3]});
    }
    if (w == 7 && UNLIKELY(any_lane(ck[0] != tag[0] || ck[1] != tag[1]))) POLL(vc2);
    if (w == 5 && b == 255) {
#pragma unroll 1
        for (int s_ = 0; s_ < 4 * SPIN_CAP; ++s_) { u2 f0, f1, f2_, f3; const unsigned vc = WS_F1 + l * 128;
            ld8(f0, vc, rsF, 0); ld8(f1, vc, rsF, 8192); ld8(f2_, vc, rsF, 16384); ld8(f3, vc, rsF, 24576); WAITV4(0, f0, f1, f2_, f3);
            if (!any_lane(((f0[0] ^ tag[0]) | (f1[0] ^ tag[0]) | (f2_[0] ^ tag[0]) | (f3[0] ^ tag[0]) | (f0[1] ^ tag[1]) | (f1[1] ^ tag[1]) | (f2_[1] ^ tag[1]) | (f3[1] ^ tag[1])) != 0u)) break; }
        FLAG(0u);
    }
    BAR_VM(0);
    STAMP(6);
    const unsigned a0 = 15 * hh, a1 = a0 + 1, jo = j * 8192;
    const unsigned wde = (48 * (a0 >> 1) + 16 * (a0 & 1)) * 1024 + jo, wdo = (48 * (a1 >> 1) + 16 * (a1 & 1)) * 1024 + jo, wdt = (336 * hh + 32) * 1024 + jo;
    const unsigned wla = L_A + hh * 14336 + j * 1024, wlb = hh * 12288 + j * 1024, wlc = 49152 + hh * 4096 + j * 1024;
#define WD(i) dma_bufi<((i) < 14 ? (i) % 7 : (i) < 20 ? (i) - 14 : (i) - 20) * 2048u, ((i) < 15 ? (i) >> 1 : (i) - 15) * 49152u>((i) < 14 ? wla : (i) < 20 ? wlb : wlc, vrow, rsWd, (i) >= 15 ? wdt : ((i) & 1) ? wdo : wde)
    u4 D0[7], D1[7];
    {
        const unsigned sH = WS_H + (8 * g + w) * OS, sS = WS_ST + (8 * g + w) * 2048u;
#define PD4 6
        constexpr int PD = PD4;
        u4 Hb[32], A0[3], A1[3], A2[4];
        u2 st[4];
        f16 ag[3] = {};
        const unsigned cf = L_CF + w * 384, vstat = h * 1024 + t32 * 8;
#pragma unroll
        for (int i = 0; i < PD; ++i) ld16(Hb[kgo(i)], l16, rsF, sH + kgo(i) * 1024);
        if (l < 48) { f2 s = {0.f, 0.f};
#pragma unroll
            for (int i = 0; i < 8; ++i) s += *(LAS const f2*)(L_CSP + i * 384 + l * 8);
            sts8f(cf + l * 8, s); }
        A0[0] = ldtr(ga, ga + 768); A1[0] = ldtr(ga + 32, ga + 800); A0[1] = ldtr(ga + kgo(1) * 3072, ga + kgo(1) * 3072 + 768); A1[1] = ldtr(ga + kgo(1) * 3072 + 32, ga + kgo(1) * 3072 + 800);
        unsigned mx = 0;
#define HDS 10
#define P4_STEP(i) do { constexpr int c = (i) % 3, n = ((i) + 2) % 3, k = kgo(i); const unsigned o = ga + ((i) < 30 ? kgo((i) + 2) * 3072 : 0); \
            if ((i) + PD < 32) ld16(Hb[kgo((i) + PD < 32 ? (i) + PD : 0)], l16, rsF, sH + kgo((i) + PD) * 1024); \
            else if ((i) + PD < 36) ld8(st[((i) + PD - 32) & 3], vstat, rsF, sS + ((i) + PD - 32) * 256); \
            WAITV1((i) + PD < 36 ? PD : 35 - (i), Hb[k]); if ((i) == HDS) HOTB(3, wd, 43); \
            if ((i) < 8) FRESH16(Hb[k], l16, rsF, sH + k * 1024); else { mx = max(mx, Hb[k][3]); asm volatile("" : "+v"(mx)); } if ((i) == 0) STAMP(7); SBAR(); \
            ag[0] = MFMA32(A0[c], Hb[k], ag[0]); if ((i) < 30) A0[n] = ldtr(o, o + 768); else A2[((i) - 30) & 1] = ldtr(ga + 64 + (((i) - 30) & 1) * 3072, ga + 832 + (((i) - 30) & 1) * 3072); SBAR(); \
            ag[1] = MFMA32(A1[c], Hb[k], ag[1]); if ((i) < 30) A1[n] = ldtr(o + 32, o + 800); else if ((i) == 31) A2[2] = ldtr(ga + 64 + 6144, ga + 832 + 6144); SBAR(); } while (0)
#define P4_4(i) P4_STEP(i); P4_STEP((i) + 1); P4_STEP((i) + 2); P4_STEP((i) + 3)
        P4_4(0); P4_4(4); P4_4(8); P4_4(12); P4_4(16); P4_4(20); P4_4(24); P4_4(28);
        WAITV4(0, st[0], st[1], st[2], st[3]);
        if (UNLIKELY(any_lane(max(max(mx, max(st[0][1], st[1][1])), max(st[2][1], st[3][1])) == ~0u))) {
#pragma unroll
            for (int i = 0; i < 32; ++i) FRESH16(Hb[i], l16, rsF, sH + i * 1024);
#pragma unroll 1
            for (int s_ = 0; s_ < SPIN_CAP; ++s_) { NRETRY();
#pragma unroll
                for (int i = 0; i < 4; ++i) ld8(st[i], vstat, rsF, sS + i * 256);
                WAITV4(0, st[0], st[1], st[2], st[3]);
                if (!any_lane(max(max(st[0][1], st[1][1]), max(st[2][1], st[3][1])) == ~0u)) break;
            }
            ag[0] = z16; ag[1] = z16;
#pragma unroll
            for (int i = 0; i < 32; ++i) { const unsigned o = ga + kgo(i) * 3072; ag[0] = MFMA32(ldtr(o, o + 768), Hb[kgo(i)], ag[0]); ag[1] = MFMA32(ldtr(o + 32, o + 800), Hb[kgo(i)], ag[1]); }
        }
        STAMP(8);
        const float t1 = add_x32(fu(st[0].x) + fu(st[1].x) + fu(st[2].x) + fu(st[3].x)), t2 = add_x32(fu(st[0].y) + fu(st[1].y) + fu(st[2].y) + fu(st[3].y));
        const float nm = t1 * (-1.f / 512), ve = __builtin_fmaxf(t2 * (1.f / 512) - nm * nm, 0.f) + 1e-5f, rs = __builtin_amdgcn_rsqf(ve);
        const float ka_ = rs * -2.3022081986f, kb_ = rs * rs * rs * -0.1029432397f;
#define G_UNIT(u, so_, M0, M1) do { u4 y; _Pragma("unroll") for (int qd = 0; qd < 2; ++qd) { \
                const f4 cg = lds16f(cf + (u) * 128 + qd * 32 + h * 16), cu = lds16f(cf + (u) * 128 + (2 + qd) * 32 + h * 16); \
                _Pragma("unroll") for (int e = 0; e < 2; ++e) { \
                    const f2 gt = (f2){ag[u][4 * qd + 2 * e], ag[u][4 * qd + 2 * e + 1]} + (e ? hi2(cg) : lo2(cg)) * nm, up = (f2){ag[u][8 + 4 * qd + 2 * e], ag[u][9 + 4 * qd + 2 * e]} + (e ? hi2(cu) : lo2(cu)) * nm; \
                    const f2 z = gt * (gt * gt * kb_ + ka_), d = (f2){__builtin_amdgcn_exp2f(z[0]), __builtin_amdgcn_exp2f(z[1])} * ve + ve; \
                    unsigned y_ = pk2(gt * up * (f2){__builtin_amdgcn_rcpf(d[0]), __builtin_amdgcn_rcpf(d[1])}); asm volatile("" : "+v"(y_)); y[2 * qd + e] = y_; } \
                if (qd == 0) MEM(M0); } \
            MEM(st16(y, l16, rsF, so_); M1); } while (0)
        MEM(WD(0), WD(1), WD(2));
        G_UNIT(0, sGa, (WD(3), WD(4), WD(5), WD(6)), NO); G_UNIT(1, sGa + 1024, NO, NO);
        STAMP(47);
        const unsigned da0 = L_A + hh * 14336 + ta0, da1 = L_A + hh * 14336 + ta1;
        const unsigned vc3 = WS_F3 + ((U64 >> 2) * 32 + min(t32, 28u)) * 128, vc4 = vc3 + (WS_F4 - WS_F3);
#define B1 6
#define WDS(i, m) if ((i) == B1 + 8 + (B1 + 21 <= 31 ? (m) : (m) * (24 - B1) / 13)) WD(7 + (m))
#define P4B_STEP(i) do { constexpr int c = (i) & 3, n = ((i) + 3) & 3; const unsigned o = ga + ((i) + 3) * 3072 + 64; \
            if ((i) == B1) SSTAMP(48); if ((i) == B1 + 1) SSTAMP(49); if ((i) == B1 + 8) SSTAMP(50); if ((i) == B1 + 9) SSTAMP(51); if ((i) == B1 + 15) SSTAMP(52); if ((i) == B1 + 21) SSTAMP(53); \
            if ((i) == B1) BAR_VM(2);         \
            if ((i) == B1 + 8) { BAR_VM(0); FLAG(w == 4 ? WS_F4 + (g * 32 + r) * 128 : OOB); }        SBAR(); \
            ag[2] = MFMA32(A2[c], Hb[i], ag[2]); if ((i) < 29) A2[n] = ldtr(o, o + 768); \
            if ((i) >= B1 && (i) < B1 + 7) { constexpr int q = ((i) + 14 - B1) % 7; D0[q] = ldtr(da0 + q * 2048, da0 + q * 2048 + 1024); D1[q] = ldtr(da1 + q * 2048, da1 + q * 2048 + 1024); } SBAR(); \
            WDS(i, 0); WDS(i, 1); WDS(i, 2); WDS(i, 3); WDS(i, 4); WDS(i, 5); WDS(i, 6); WDS(i, 7); WDS(i, 8); WDS(i, 9); WDS(i, 10); WDS(i, 11); WDS(i, 12); \
            if ((i) == (B1 + 21 < 31 ? B1 + 21 : 31)) ld8(ck, w == 7 ? vc3 : w == 6 ? vc4 : OOB, rsF, 0); SBAR(); } while (0)
#define P4B_4(i) P4B_STEP(i); P4B_STEP((i) + 1); P4B_STEP((i) + 2); P4B_STEP((i) + 3)
        P4B_4(0); P4B_4(4); P4B_4(8); P4B_4(12); P4B_4(16); P4B_4(20); P4B_4(24); P4B_4(28);
        STAMP(41);
        G_UNIT(2, sGb, NO, NO);
        STAMP(30);
        WAITV1(1, ck);
        if (w >= 6 && UNLIKELY(any_lane(ck[0] != tag[0] || ck[1] != tag[1]))) { const unsigned vc = w == 7 ? vc3 : vc4; POLL(vc); }
    }
    BAR_L();
    STAMP(9);
    {
        constexpr int PD = 6;
        u4 Gb[22], A0[2], A1[2];
        const unsigned gs = WS_G + (2 * U64 + j) * 90112u + 15 * hh * 1024, gz = hh == 3 ? OOB : gs, gt = WS_G + (2 * U64 + j) * 90112u + (60 + 7 * hh - 15) * 1024;
        const unsigned o0 = L_A + hh * 14336 + ta0, o1 = L_A + hh * 14336 + ta1, p0 = hh * 12288 + ta0, p1 = hh * 12288 + ta1, q0 = 49152 + hh * 4096 + ta0, q1 = 49152 + hh * 4096 + ta1;
#define GOFF(i) (((i) >= 15 ? gt : (i) >= 13 ? gz : gs) + (i) * 1024)
#pragma unroll
        for (int i = 0; i < PD; ++i) ld16(Gb[i], l16, rsF, GOFF(i));
        WD(20); WD(21);
        acc0 = z16; acc1 = z16;
#define P5_STEP(i) do { constexpr int c = (i) & 1, n = c ^ 1; \
            if ((i) + PD < 22) ld16(Gb[(i) + PD < 22 ? (i) + PD : 0], l16, rsF, GOFF((i) + PD)); \
            WAITV1(((i) + PD < 22 ? PD : 21 - (i)) + ((i) < PD ? 2 : 0), Gb[i]); if ((i) >= 15) FRESH16(Gb[i], l16, rsF, GOFF(i)); if ((i) == 0) STAMP(10); \
            if ((i) == 12) BAR_L(); SBAR(); \
            const unsigned a0_ = (i) < 13 ? o0 + ((i) - 6) * 2048 : (i) < 19 ? p0 + ((i) - 13) * 2048 : q0 + ((i) - 19) * 2048, a1_ = (i) < 13 ? o1 + ((i) - 6) * 2048 : (i) < 19 ? p1 + ((i) - 13) * 2048 : q1 + ((i) - 19) * 2048; \
            acc0 = MFMA32((i) < 7 ? D0[(i) < 7 ? (i) : 0] : A0[c], Gb[i], acc0); if ((i) >= 6 && (i) < 21) A0[n] = ldtr(a0_, a0_ + 1024); SBAR(); \
            acc1 = MFMA32((i) < 7 ? D1[(i) < 7 ? (i) : 0] : A1[c], Gb[i], acc1); if ((i) >= 6 && (i) < 21) A1[n] = ldtr(a1_, a1_ + 1024); SBAR(); } while (0)
        P5_STEP(0); P5_STEP(1); P5_STEP(2); P5_STEP(3); P5_STEP(4); P5_STEP(5); P5_STEP(6); P5_STEP(7); P5_STEP(8); P5_STEP(9); P5_STEP(10);
        P5_STEP(11); P5_STEP(12); P5_STEP(13); P5_STEP(14); P5_STEP(15); P5_STEP(16); P5_STEP(17); P5_STEP(18); P5_STEP(19); P5_STEP(20); P5_STEP(21);
    }
    STAMP(11);
    BAR_L();
    {
        f4 lo, hi; reduce(lo, hi);
        const u4 Ores = {pk2(lo2(lo) + (f2){blo(Hres[0]), bhi(Hres[0])}), pk2(hi2(lo) + (f2){blo(Hres[1]), bhi(Hres[1])}), pk2(lo2(hi) + (f2){blo(Hres[2]), bhi(Hres[2])}), pk2(hi2(hi) + (f2){blo(Hres[3]), bhi(Hres[3])})};
        st16_plain(Ores, vres, mk_rsrc(out + U64 * 65536u, 65536u), 0);
    }
    asm volatile(".Lfb_code1:");
    STAMP(12);
#endif
}

#define WSOFF 0
extern "C" void kernel_launch(void* const* d_in, const int* in_sizes, int n_in, void* d_out, int out_size, void* d_ws, size_t ws_size, hipStream_t stream) {
    (void)in_sizes; (void)n_in; (void)out_size; (void)ws_size;
    (void)hipFuncSetAttribute((const void*)fused_block, hipFuncAttributeMaxDynamicSharedMemorySize, LDS_BYTES);
    fused_block<<<256, 512, LDS_BYTES, stream>>>((const char*)d_in[0], (const char*)d_in[1], (const char*)d_in[2], (const char*)d_in[4], (const char*)d_in[5], (const char*)d_in[6],
                                                 (char*)d_out, (char*)d_ws + WSOFF);
}
#define LASTSTAMP 12
#define STAMP_NAMES "start", "P1 loop end", "KV drained", 0, "O first data", "O drained", "h drained", "h first data", "P4 pass1 end", "P4 end bar", "G first data", "P5 loop end", "end", 0, 0, 0, \
  "st0", "st1", "st2", "st3", "st4", "st5", "st6", "st7", "KV first data", "att done", "O stored", "P3 loop end", "P3 reduced", 0, "G stored", 0, "KV stored", "h stored", \
  "LN sums", "pre KV bar", "colsum done", "A read", 0, "setup done", "GU bar", "P4 pass2 end", "att blk3", "att blk4", "att blk0", "comb bar", 0, "G01 stored", "p2 pre B1", "p2 s7", "p2 pre B2", "p2 s15", "p2 s21", "p2 s27"
```

```cpp
#define SL2 8
#include <hip/hip_runtime.h>
#include <hip/hip_bf16.h>
#include <stdint.h>
#pragma clang diagnostic ignored "-Wint-to-pointer-cast"

typedef __bf16 bf8 __attribute__((ext_vector_type(8)));
typedef __bf16 bf2 __attribute__((ext_vector_type(2)));
typedef short s4 __attribute__((ext_vector_type(4)));
typedef short s8 __attribute__((ext_vector_type(8)));
typedef float f2 __attribute__((ext_vector_type(2)));
typedef float f4 __attribute__((ext_vector_type(4)));
typedef float f16 __attribute__((ext_vector_type(16)));
typedef unsigned u2 __attribute__((ext_vector_type(2)));
typedef unsigned u4 __attribute__((ext_vector_type(4)));
typedef unsigned long long u64;
#define LAS __attribute__((address_space(3)))
#define DEV __device__ __forceinline__

constexpr unsigned WS_F1 = 32768, WS_F2 = 65536, WS_F3 = 98304, WS_F4 = 131072;
#define KVS 132096u
#define OS 33792u
constexpr unsigned WS_KV = 1u << 20;
constexpr unsigned WS_O = WS_KV + 32 * KVS;
constexpr unsigned WS_H = WS_O + 64 * OS;
constexpr unsigned WS_ST = WS_H + 64 * OS;
constexpr unsigned WS_G = WS_ST + 64 * 4096;
static_assert(WS_G + 64 * 90112u <= (16u << 20), "hand-off buffers");
constexpr unsigned WS_DIAG = 32u << 20;
constexpr unsigned LDS_BYTES = 163840;
constexpr unsigned L_HOLE = 73728;
constexpr unsigned L_OC = L_HOLE;
constexpr unsigned L_A = 98304;
constexpr unsigned L_CSP = 155648, L_CF = L_CSP + 4096;
constexpr int SPIN_CAP = 1 << 14;
constexpr unsigned OOB = 0x40000000u;

#define MFMA16(a, b, c) __builtin_amdgcn_mfma_f32_16x16x32_bf16(__builtin_bit_cast(bf8, a), __builtin_bit_cast(bf8, b), c, 0, 0, 0)
#define MFMA32(a, b, c) __builtin_amdgcn_mfma_f32_32x32x16_bf16(__builtin_bit_cast(bf8, a), __builtin_bit_cast(bf8, b), c, 0, 0, 0)
#define SBAR() __builtin_amdgcn_sched_barrier(0)
#define BAR_VM(N) asm volatile("s_waitcnt vmcnt(%0) lgkmcnt(0)\n\ts_barrier" ::"i"(N) : "memory")
#define BAR_L() asm volatile("s_waitcnt lgkmcnt(0)\n\ts_barrier" ::: "memory")

DEV u4 mk_rsrc(const void* p, unsigned bytes) { const u64 a = (u64)p; return (u4){(unsigned)a, (unsigned)(a >> 32) & 0xffffu, bytes, 0x00020000u}; }
DEV void dma_buf(unsigned lds, unsigned voff, u4 rs, unsigned soff) {
    asm volatile("s_mov_b32 m0, %0\n\ts_nop 0\n\tbuffer_load_dwordx4 %1, %2, %3 offen lds" ::"s"(lds), "v"(voff), "s"(rs), "s"(soff) : "memory"); }
template <unsigned LI, unsigned SI> DEV void dma_bufi(unsigned lds, unsigned voff, u4 rs, unsigned soff) {
    unsigned t; asm volatile("s_add_u32 m0, %1, %5\n\ts_add_u32 %0, %4, %6\n\tbuffer_load_dwordx4 %2, %3, %0 offen lds" : "=&s"(t) : "s"(lds), "v"(voff), "s"(rs), "s"(soff), "i"(LI), "i"(SI) : "memory", "scc"); }
template <unsigned LI> DEV void dma_glbi(unsigned lds, const char* p) {
    asm volatile("s_add_u32 m0, %0, %2\n\ts_nop 0\n\tglobal_load_lds_dwordx4 %1, off" ::"s"(lds), "v"(p), "i"(LI) : "memory", "scc"); }
DEV void dma_glb(unsigned lds, const char* p) {
    asm volatile("s_mov_b32 m0, %0\n\ts_nop 0\n\tglobal_load_lds_dwordx4 %1, off" ::"s"(lds), "v"(p) : "memory"); }
DEV void ld16(u4& d, unsigned voff, u4 rs, unsigned soff) {
    asm volatile("buffer_load_dwordx4 %0, %1, %2, %3 offen sc1" : "=v"(d) : "v"(voff), "s"(rs), "s"(soff) : "memory"); }
DEV void ld16_plain(u4& d, unsigned voff, u4 rs, unsigned soff) {
    asm volatile("buffer_load_dwordx4 %0, %1, %2, %3 offen" : "=v"(d) : "v"(voff), "s"(rs), "s"(soff) : "memory"); }
template <int OFF> DEV void ld16_imm(u4& d, unsigned voff, u4 rs) {
    asm volatile("buffer_load_dwordx4 %0, %1, %2, 0 offen offset:%3" : "=v"(d) : "v"(voff), "s"(rs), "i"(OFF) : "memory"); }
DEV void ld8(u2& d, unsigned voff, u4 rs, unsigned soff) {
    asm volatile("buffer_load_dwordx2 %0, %1, %2, %3 offen sc1" : "=v"(d) : "v"(voff), "s"(rs), "s"(soff) : "memory"); }
DEV void ld16_v(u4& d0, u4& d1, unsigned voff, u4 rs, unsigned soff) {
    asm volatile("s_mov_b64 exec, %5\n\tbuffer_load_dwordx4 %0, %2, %3, %4 offen offset:1024 sc1\n\tbuffer_load_dwordx4 %1, %2, %3, %4 offen offset:1536 sc1\n\ts_mov_b64 exec, -1"
                 : "+v"(d0), "+v"(d1) : "v"(voff), "s"(rs), "s"(soff), "s"(0x0000FFFF0000FFFFull) : "memory"); }
DEV void st16(u4 d, unsigned voff, u4 rs, unsigned soff) {
    asm volatile("buffer_store_dwordx4 %0, %1, %2, %3 offen sc1\n\ts_nop 1" ::"v"(d), "v"(voff), "s"(rs), "s"(soff) : "memory"); }
DEV void st16_plain(u4 d, unsigned voff, u4 rs, unsigned soff) {
    asm volatile("buffer_store_dwordx4 %0, %1, %2, %3 offen\n\ts_nop 1" ::"v"(d), "v"(voff), "s"(rs), "s"(soff) : "memory"); }
DEV void st8(u2 d, unsigned voff, u4 rs, unsigned soff) {
    asm volatile("buffer_store_dwordx2 %0, %1, %2, %3 offen sc1\n\ts_nop 1" ::"v"(d), "v"(voff), "s"(rs), "s"(soff) : "memory"); }
#define WAITV1(N, a) asm volatile("s_waitcnt vmcnt(%1)" : "+v"(a) : "i"(N) : "memory")
#define WAITV1D(ND, NS, a) asm volatile("s_waitcnt vmcnt(%1)\n\ts_cmp_lt_u32 %3, 4\n\ts_cbranch_scc1 .Lw%=\n\ts_waitcnt vmcnt(%2)\n.Lw%=:" : "+v"(a) : "i"(ND), "i"(NS), "s"(w) : "memory", "scc")
#define WAITV3(N, a, b, c) asm volatile("s_waitcnt vmcnt(%3)" : "+v"(a), "+v"(b), "+v"(c) : "i"(N) : "memory")
#define WAITV4(N, a, b, c, d) asm volatile("s_waitcnt vmcnt(%4)" : "+v"(a), "+v"(b), "+v"(c), "+v"(d) : "i"(N) : "memory")

DEV u4 lds16(unsigned a) { return *(LAS const u4*)a; }
DEV f4 lds16f(unsigned a) { return *(LAS const f4*)a; }
DEV float lds4f(unsigned a) { return *(LAS const float*)a; }
DEV void sts16(unsigned a, u4 v) { *(LAS u4*)a = v; }
DEV void sts16f(unsigned a, f4 v) { *(LAS f4*)a = v; }
DEV void sts8f(unsigned a, f2 v) { *(LAS f2*)a = v; }
DEV void sts4f(unsigned a, float v) { *(LAS float*)a = v; }
DEV u4 ldtr(unsigned a0, unsigned a1) {
    const s4 lo = __builtin_amdgcn_ds_read_tr16_b64_v4i16((LAS s4*)a0), hi = __builtin_amdgcn_ds_read_tr16_b64_v4i16((LAS s4*)a1);
    return __builtin_bit_cast(u4, __builtin_shufflevector(lo, hi, 0, 1, 2, 3, 4, 5, 6, 7)); }
DEV unsigned pk2(f2 a) { return __builtin_bit_cast(unsigned, __builtin_convertvector(a, bf2)); }
DEV f2 lo2(f4 a) { return (f2){a[0], a[1]}; }
DEV f2 hi2(f4 a) { return (f2){a[2], a[3]}; }
DEV unsigned pk(float a, float b) { return __builtin_bit_cast(unsigned, __builtin_convertvector((f2){a, b}, bf2)); }
DEV float blo(unsigned v) { return __builtin_bit_cast(float, v << 16); }
DEV float bhi(unsigned v) { return __builtin_bit_cast(float, v & 0xffff0000u); }
DEV float fu(unsigned v) { return __builtin_bit_cast(float, v); }
DEV unsigned uf(float v) { return __builtin_bit_cast(unsigned, v); }
DEV float add_x32(float v) { auto r = __builtin_amdgcn_permlane32_swap(uf(v), uf(v), false, false); const unsigned a = r[0], b = r[1]; return fu(a) + fu(b); }
DEV float add_x16(float v) { auto r = __builtin_amdgcn_permlane16_swap(uf(v), uf(v), false, false); const unsigned a = r[0], b = r[1]; return fu(a) + fu(b); }
DEV float max_x32(float v) { auto r = __builtin_amdgcn_permlane32_swap(uf(v), uf(v), false, false); const unsigned a = r[0], b = r[1]; return __builtin_fmaxf(fu(a), fu(b)); }
template <int C> DEV float add_dpp(float v) { return v + fu(__builtin_amdgcn_update_dpp(0u, uf(v), C, 0xf, 0xf, true)); }
DEV float add_x8(float v) { return v + fu(__builtin_amdgcn_update_dpp(0u, uf(v), 0x128, 0xf, 0xf, true)); }
constexpr u64 diag_mask(int r) { u64 m = 0; for (int l = 0; l < 64; ++l) { const int key = 16 * ((r >> 2) & 1) + 8 * (l >> 5) + 4 * (r >> 3) + (r & 3); if (key <= (l & 31)) m |= 1ull << l; } return m; }
DEV float sel(u64 m, float t, float f) { return __builtin_amdgcn_inverse_ballot_w64(m) ? t : f; }

template <unsigned LDS> DEV void touch(unsigned voff, u4 rs, unsigned soff) {
    asm volatile("s_mov_b32 m0, %3\n\ts_nop 0\n\tbuffer_load_dword %0, %1, %2 offen lds" ::"v"(voff), "s"(rs), "s"(soff), "i"(LDS) : "memory"); }
typedef short sh2 __attribute__((ext_vector_type(2)));
constexpr int kgo(int i) { return i < 8 ? 4 * i : (i - 8) / 3 * 4 + (i - 8) % 3 + 1; }
#define RSLEEP 4
#define SL1 0
#define SL3 0
DEV bool any_lane(bool c) { return __builtin_amdgcn_ballot_w64(c) != 0; }
#define UNLIKELY(x) __builtin_expect(!!(x), 0)
#define NRETRY()
#define FRESH16(d, voff, rs, soff) do { if (UNLIKELY(any_lane((d)[3] == ~0u))) { \
        _Pragma("unroll 1") for (int s_ = 0; s_ < SPIN_CAP; ++s_) { NRETRY(); u4 t_; asm volatile("buffer_load_dwordx4 %0, %1, %2, %3 offen sc1\n\ts_waitcnt vmcnt(0)" : "=&v"(t_) : "v"(voff), "s"(rs), "s"(soff) : "memory"); \
            d = t_; if (!any_lane(t_[3] == ~0u)) break; __builtin_amdgcn_s_sleep(2); } } } while (0)
#define POLL(voff) do { \
        _Pragma("unroll 1") for (int s_ = 0; s_ < 4 * SPIN_CAP; ++s_) { u2 t_; asm volatile("buffer_load_dwordx2 %0, %1, %2, 0 offen sc1\n\ts_waitcnt vmcnt(0)" : "=&v"(t_) : "v"(voff), "s"(rsF) : "memory"); \
            if (!any_lane(t_[0] != tag[0] || t_[1] != tag[1])) break; NRETRY(); __builtin_amdgcn_s_sleep(RSLEEP); } } while (0)
#define FLAG(off) st8(tag, l == 0 ? (off) : OOB, rsF, 0)
#define STAMP(i)

__global__ __launch_bounds__(512, 2) void fused_block(const char* x_, const char* wqkv_, const char* wo_, const char* wg_, const char* wu_, const char* wd_, char* out_, char* ws_) {
#if defined(__HIP_DEVICE_COMPILE__)

    typedef u64 u64x8 __attribute__((ext_vector_type(8)));
    u64x8 ka; asm volatile("s_load_dwordx16 %0, %1, 0x0\n\ts_waitcnt lgkmcnt(0)" : "=s"(ka) : "s"(__builtin_amdgcn_kernarg_segment_ptr()) : "memory");

    const char *x = (const char*)ka[0], *wqkv = (const char*)ka[1], *wo = (const char*)ka[2], *wg = (const char*)ka[3], *wu = (const char*)ka[4], *wd = (const char*)ka[5]; char *out = (char*)ka[6], *ws = (char*)ka[7];
    const unsigned l = threadIdx.x & 63, w = __builtin_amdgcn_readfirstlane(threadIdx.x >> 6);
    const unsigned b = blockIdx.x, xc = b & 7, rk = b >> 3;
    const unsigned T64 = 8 * (xc >> 1) + (rk >> 2), hq = 4 * (xc & 1) + (rk & 3);
    STAMP(0);
    const unsigned u8 = l >> 3, l7 = l & 7;
    u4 xr[8];
    const u4 rsX = mk_rsrc(x + T64 * 65536u, 65536u);
    const unsigned vx = (8 * w + u8) * 1024 + l7 * 16;
#define HOT 7
    {
        const unsigned hs = (3u - (unsigned)((u64)wqkv >> 7)) & 7u;
        if ((hs >> 2) == (xc & 1) && w == HOT) { const unsigned z = rk * 48 + (l < 48 ? l : 0); const char* p = wqkv + (hs * 128 + z * 1024); asm volatile("global_load_dword v254, %0, off" ::"v"(p) : "memory", "v254"); } }
    ld16_imm<0>(xr[0], vx, rsX); ld16_imm<128>(xr[1], vx, rsX); ld16_imm<256>(xr[2], vx, rsX); ld16_imm<384>(xr[3], vx, rsX);
#define XB 6
    if (XB > 4) { ld16_imm<512>(xr[4], vx, rsX); ld16_imm<640>(xr[5], vx, rsX); }
    if (XB > 6) { ld16_imm<768>(xr[6], vx, rsX); ld16_imm<896>(xr[7], vx, rsX); }
    const u4 rsW = mk_rsrc(wqkv + hq * 128u, 512u * 3072u - hq * 128u);
    SBAR();
    const unsigned w3 = w & 3, wk2 = w3 * 2048;
    unsigned vw0 = (16 * w3 + u8) * 3072 + 32 * ((l7 >> 1) ^ ((l >> 4) & 1)) + 16 * (l7 & 1), vw1 = (vw0 + 24576) ^ 64;
#define SLOT(i) ((((i) + 1) & 3) * 24576u)
#define SP(i, p) dma_bufi<SLOT(i) + ((p) >> 1) * 8192 + ((p) & 1) * 1024, (i) * 196608u + ((p) >> 1) * 1024u>(wk2, ((p) & 1) ? vw1 : vw0, rsW, 0)
    const unsigned U64 = 4 * xc + (rk >> 3), fb = rk & 7;
    const unsigned g = 2 * (xc >> 1) + (rk >> 4), r = 16 * (xc & 1) + (rk & 15);
    const unsigned j = w & 1, hh = w >> 1;
    const unsigned t16 = l & 15, g4 = l >> 4, h = l >> 5, t32 = l & 31, q4 = t16 >> 2, pp = l & 3, l16 = l * 16, l128 = l * 128;
    const u4 rsF = mk_rsrc(ws, 16u << 20);
    const u4 ones = {0x3f803f80u, 0x3f803f80u, 0x3f803f80u, 0x3f803f80u};
    const unsigned kvb = WS_KV + (4 * hq + hh) * KVS;
    const unsigned skv = kvb + (2 * T64 + j) * 2048;
    const unsigned sOw = WS_O + (2 * T64 + j) * OS + (4 * hq + hh) * 1024;
    u2 tag; u4 xres;
    unsigned vres = (32 * j + 8 * hh + l7) * 1024 + fb * 128 + u8 * 16;
    unsigned xw0 = L_A + (8 * w + u8) * 1024 + 16 * (l7 ^ (8 * (w & 1) + u8)), xw1 = xw0 ^ 128;
    unsigned xa[4];
#pragma unroll
    for (int q = 0; q < 4; ++q) { xa[q] = L_A + (32 * j + t16) * 1024 + 16 * ((4 * q + g4) ^ t16); asm volatile("" : "+v"(xa[q])); }
    unsigned wa = (8 * g4 + q4) * 128 + 32 * (hh ^ ((q4 >> 1) | ((g4 & 1) << 1))) + 8 * pp;
    f4 aq[2] = {}, ak[2] = {}, av[2] = {};
    unsigned vk = (h * 32 + (16 * ((t32 >> 3) & 1) + 8 * ((t32 >> 2) & 1) + 4 * (t32 >> 4) + (t32 & 3))) * 16, vv_ = (h * 16 + t16) * 16;
    unsigned vst = 1024 + ((g4 & 1) * 32 + (g4 >> 1) * 16 + t16) * 16;
    unsigned vrow = u8 * 1024 + 16 * (l7 ^ (((l >> 4) & 1) << 2));
    const char* gp[3];
#pragma unroll
    for (int i = 0; i < 3; ++i) { const unsigned z = 64 * i + l, k = (z * 43691u) >> 19, c = z - 12 * k, m = c >= 6, cc = c - 6 * m; unsigned un = 3 * r + (cc >> 1); un = un > 85 ? 85 : un;
        gp[i] = (m ? wu : wg) + ((16 * w + k) * 2752 + un * 32 + (cc & 1) * 16); asm volatile("" : "+v"(gp[i])); }
    const unsigned wk3 = w * 3072;
#define GU(R, c) dma_glbi<(R) * 24576 + (c) * 1024>(wk3, gp[c] + (R) * 128 * 2752)
#define GUD(R, q) dma_glbi<(R) * 24576 + ((q) / 3) * 12288 + ((q) % 3) * 1024>(wk3, gp[(q) % 3] + ((R) * 128 + ((q) / 3) * 64) * 2752)
    asm volatile("" : "+v"(vw0), "+v"(vw1), "+v"(xw0), "+v"(xw1), "+v"(wa), "+v"(vres), "+v"(vk), "+v"(vv_), "+v"(vrow), "+v"(vst));
    STAMP(39);
    if (XB <= 4) { ld16_imm<512>(xr[4], vx, rsX); ld16_imm<640>(xr[5], vx, rsX); }
    if (XB <= 6) { ld16_imm<768>(xr[6], vx, rsX); ld16_imm<896>(xr[7], vx, rsX); }
#define TAMODE 0
    {
        const char* pc; unsigned nb; asm volatile("s_getpc_b64 %0\n.Lfb_code0:\n\ts_mov_b32 %1, .Lfb_code1-.Lfb_code0" : "=s"(pc), "=s"(nb));
        const unsigned co = ((rk * 8 + w) * 2 + (l & 1)) * 128;
        const char* p = TAMODE == 1 ? (l < 24 ? wqkv + (hq * 128 + (w * 64 + (T64 & 7) * 8 + l7) * 3072 + u8 * 1024) : pc + (co < nb ? co : 0))
                                    : (l < 6 ? wqkv + ((b * 8 + w) * 6 + l) * 128 : pc + (co < nb ? co : 0));
        asm volatile("global_load_dword v254, %0, off" ::"v"(p) : "memory", "v254"); }
#define NO ((void)0)
#define HOTB(wv, base, n) do { if (w == (wv)) { const char* p_ = base + ((((3u - (unsigned)((u64)base >> 7)) & 7u) * 128) + (rk * (n) + (l < (n) ? l : 0)) * 1024); asm volatile("global_load_dword v254, %0, off" ::"v"(p_) : "memory", "v254"); } } while (0)
#define TBPOS 0
#define TOUCH_B() do { const char* tp = (w == 4 ? wo : w == 5 ? wg : w == 6 ? wu : wd) + (w == 4 ? b * 16 + (l & 15) : b * 43 + (l < 43 ? l : 0)) * 128; \
        asm volatile("global_load_dword v255, %0, off" ::"v"(tp) : "memory", "v255"); } while (0)
#if !defined(NOTB) && TBPOS == 0
    if (w >= 4) TOUCH_B();
#define XW 1
#else
#define XW 0
#endif
#define SSTAMP(i)
    {
        f2 xf[32], s1 = {0.f, 0.f}, s2 = {0.f, 0.f};
#define LW 0
#define TWD() NO
#define LN_SUM(i, N, D0, D1) do { if (XW && (N) < 8 - (i)) WAITV1((N) + 1, xr[i]); else WAITV1D((N) + 1, 8 - (i) + XW - LW, xr[i]); _Pragma("unroll") for (int e = 0; e < 4; ++e) { const unsigned d_ = xr[i][e]; f2 v_ = {blo(d_), bhi(d_)}; asm volatile("" : "+v"(v_)); xf[4 * (i) + e] = v_; \
        s1 += v_; s2 += v_ * v_; } asm volatile("" : "+v"(s1), "+v"(s2)); if (w < 4) { D0; D1; } SBAR(); } while (0)
        LN_SUM(0, 7, NO, NO); LN_SUM(1, 6, NO, NO); LN_SUM(2, 5, NO, NO); LN_SUM(3, 4, NO, NO);
        LN_SUM(4, 3, SP(0, 0), SP(0, 1)); LN_SUM(5, 4, SP(0, 2), SP(0, 3)); LN_SUM(6, 5, SP(0, 4), NO); LN_SUM(7, 5, SP(0, 5), NO);
        SSTAMP(34);
        if (w < 4) { SP(1, 0); SP(1, 1); SP(1, 2); SP(1, 3); SP(1, 4); SP(1, 5); }
#if !defined(NOTB) && TBPOS == 1
        else TOUCH_B();
#endif
        const float t1 = add_dpp<0x141>(add_dpp<0x4e>(add_dpp<0xb1>(s1[0] + s1[1]))), t2 = add_dpp<0x141>(add_dpp<0x4e>(add_dpp<0xb1>(s2[0] + s2[1])));
        const float mu = t1 * (1.f / 512), rs = __builtin_amdgcn_rsqf(__builtin_fmaxf(t2 * (1.f / 512) - mu * mu, 0.f) + 1e-5f), mr = -mu * rs;
#pragma unroll
        for (int i = 0; i < 8; ++i) sts16(((i & 1) ? xw1 : xw0) + (i >> 1) * 256, (u4){pk2(xf[4 * i] * rs + mr), pk2(xf[4 * i + 1] * rs + mr), pk2(xf[4 * i + 2] * rs + mr), pk2(xf[4 * i + 3] * rs + mr)});
    }
#define P1_STEP(n, D0, D1, D2) do { constexpr int c_ = (n) & 1, x_ = c_ ^ 1; constexpr bool m_ = (n) < 15; \
        const unsigned xb = xa[((n) + 1) & 3] + (((n) + 1) >> 2) * 256, wb = wa + SLOT(((n) + 1) >> 1) + x_ * 4096; \
        if (m_) X0[x_] = lds16(xb); SBAR(); \
        aq[0] = MFMA16(Wq[c_], X0[c_], aq[0]); if (m_) Wq[x_] = ldtr(wb, wb + 512); SBAR(); \
        aq[1] = MFMA16(Wq[c_], X1[c_], aq[1]); if (m_) X1[x_] = lds16(xb + 16384); D0; SBAR(); \
        ak[0] = MFMA16(Wk[c_], X0[c_], ak[0]); if (m_) Wk[x_] = ldtr(wb + 8192, wb + 8704); SBAR(); \
        ak[1] = MFMA16(Wk[c_], X1[c_], ak[1]); D1; SBAR(); \
        av[0] = MFMA16(X0[c_], Wv[c_], av[0]); if (m_) Wv[x_] = ldtr(wb + 16384, wb + 16896); SBAR(); \
        av[1] = MFMA16(X1[c_], Wv[c_], av[1]); D2; SBAR(); } while (0)
#define P1_FIRST() do { const unsigned wb = wa + SLOT(0); X0[0] = lds16(xa[0]); Wq[0] = ldtr(wb, wb + 512); X1[0] = lds16(xa[0] + 16384); Wk[0] = ldtr(wb + 8192, wb + 8704); Wv[0] = ldtr(wb + 16384, wb + 16896); } while (0)
#define XRES() ld16_plain(xres, vres, mk_rsrc(x + U64 * 65536u, 65536u), 0)
    if (w < 4) {
        u4 X0[2], X1[2], Wq[2], Wk[2], Wv[2];
        SP(2, 0); SP(2, 1); SP(2, 2); SP(2, 3); SP(2, 4); SP(2, 5);
        BAR_VM(12); STAMP(16);  P1_FIRST(); P1_STEP(0, (SP(3, 0), SP(3, 1)), (SP(3, 2), SP(3, 3)), (SP(3, 4), SP(3, 5)));
        BAR_VM(12); SSTAMP(17); P1_STEP(1, SP(4, 0), SP(4, 1), SP(4, 2)); P1_STEP(2, SP(4, 3), SP(4, 4), SP(4, 5));
        BAR_VM(12); SSTAMP(18); P1_STEP(3, SP(5, 0), SP(5, 1), SP(5, 2)); P1_STEP(4, SP(5, 3), SP(5, 4), SP(5, 5));
        BAR_VM(12); SSTAMP(19); P1_STEP(5, SP(6, 0), SP(6, 1), SP(6, 2)); P1_STEP(6, SP(6, 3), SP(6, 4), SP(6, 5));
        BAR_VM(12); SSTAMP(20); P1_STEP(7, SP(7, 0), SP(7, 1), SP(7, 2)); P1_STEP(8, SP(7, 3), SP(7, 4), SP(7, 5));
        BAR_VM(12); SSTAMP(21); P1_STEP(9, XRES(), GUD(1, 0), NO); P1_STEP(10, GUD(1, 1), GUD(1, 2), NO);
        BAR_VM(10); SSTAMP(22); P1_STEP(11, GUD(1, 3), GUD(1, 4), NO); P1_STEP(12, GUD(1, 5), NO, NO);
        BAR_VM(7);  SSTAMP(23); P1_STEP(13, NO, NO, NO); P1_STEP(14, NO, NO, NO); P1_STEP(15, NO, NO, NO);
        WAITV1(6, xres); tag = (u2){0u, 0u};
    } else {
        u4 X0[2], X1[2], Wq[2], Wk[2], Wv[2];
        const u4 bad = {~0u, ~0u, ~0u, ~0u};
#define MK(v, s_) st16(bad, v, rsF, s_)
#if !defined(NOTB) && TBPOS == 2
#define TCH(c) if (w == (c)) TOUCH_B()
#else
#define TCH(c) NO
#endif
        BAR_L(); STAMP(16);  P1_FIRST(); P1_STEP(0, ld8(tag, 0u * l, rsF, 0), MK(l16, skv), TCH(5));
        BAR_L(); SSTAMP(17); P1_STEP(1, NO, MK(l16 + 1024, skv), TCH(6)); P1_STEP(2, NO, MK(l16, skv - 2 * KVS), NO);
        BAR_L(); SSTAMP(18); P1_STEP(3, NO, MK(l16 + 1024, skv - 2 * KVS), TCH(4)); P1_STEP(4, NO, MK(l16, sOw), NO);
        BAR_L(); SSTAMP(19); P1_STEP(5, NO, MK(l16, sOw - 2048), TCH(7)); P1_STEP(6, XRES(), NO, NO);
        BAR_L(); SSTAMP(20); P1_STEP(7, NO, NO, NO); P1_STEP(8, NO, NO, NO);
        BAR_L(); SSTAMP(21); P1_STEP(9, NO, NO, NO); P1_STEP(10, NO, NO, NO);
        asm volatile("s_waitcnt vmcnt(0)" : "+v"(tag), "+v"(xres) :: "memory");
        BAR_L(); SSTAMP(22);
        { const unsigned lo = tag.x + 1u; tag.y += (lo == 0u); tag.x = lo; }
                             P1_STEP(11, FLAG(w == 4 ? WS_F1 + (T64 * 8 + hq) * 128 : OOB), NO, NO); P1_STEP(12, HOTB(6, wo, 16), NO, NO);
        BAR_L(); SSTAMP(23); P1_STEP(13, NO, NO, NO); P1_STEP(14, NO, NO, NO); P1_STEP(15, NO, NO, NO);
    }
    STAMP(1);
    u4 Qf;
    {
        { auto a = __builtin_amdgcn_permlane16_swap(pk2(lo2(ak[0])), pk2(lo2(ak[1])), false, false); auto c = __builtin_amdgcn_permlane16_swap(pk2(hi2(ak[0])), pk2(hi2(ak[1])), false, false);
          st16((u4){a[0], c[0], a[1], c[1]}, l16, rsF, skv); }
        st16((u4){pk2(lo2(av[0])), pk2(hi2(av[0])), pk2(lo2(av[1])), pk2(hi2(av[1]))}, vst, rsF, skv);
        STAMP(32);
        SBAR();
        constexpr float QS = 0.25f * 1.4426950408889634f;
        { auto a = __builtin_amdgcn_permlane16_swap(pk2(lo2(aq[0]) * QS), pk2(lo2(aq[1]) * QS), false, false); auto c = __builtin_amdgcn_permlane16_swap(pk2(hi2(aq[0]) * QS), pk2(hi2(aq[1]) * QS), false, false);
          Qf = (u4){a[0], c[0], a[1], c[1]}; }
        sts16(L_OC + (w ^ 1) * 2304 + l16, Qf);
    }
    const int Tb = 2 * (int)T64 - 8;
    u4 K[5], V0[5], V1[5];
    const int blk[5] = {Tb + 8 + (int)j, Tb + (int)j, Tb + 2 + 3 * (int)j, Tb + 3 + 3 * (int)j, Tb + 4 + 3 * (int)j};
#pragma unroll
    for (int i = 0; i < 5; ++i) { const unsigned o = blk[i] < 0 ? 0u : 0x3f803f80u; V0[i] = (u4){o, o, o, o}; V1[i] = V0[i]; asm volatile("" : "+v"(V0[i]), "+v"(V1[i])); }
    unsigned pm[8];
    { const short uq = (short)(t32 - 8 * h);
#pragma unroll
      for (int i = 0; i < 8; ++i) { const short c = (short)(16 * ((i >> 1) & 1) + 4 * (i >> 2) + 2 * (i & 1)); pm[i] = __builtin_bit_cast(unsigned, ((sh2){uq, uq} - (sh2){c, (short)(c + 1)}) >> (sh2){15, 15}); asm volatile("" : "+v"(pm[i])); } }
    unsigned so[5];
#pragma unroll
    for (int i = 0; i < 5; ++i) so[i] = blk[i] < 0 ? OOB : kvb + (unsigned)blk[i] * 2048;
    if (w == 7) {
        const int Tp = (int)T64 - 1 - (int)(l & 3); const unsigned vc = WS_F1 + ((Tp < 0 ? T64 : (unsigned)Tp) * 8 + hq) * 128; POLL(vc); }
    STAMP(35);
    BAR_VM(0);
    STAMP(2);
#define KVLD(i) do { ld16(K[i], vk, rsF, so[i]); ld16_v(V0[i], V1[i], vv_, rsF, so[i]); } while (0)
#define KVOK(N, i) do { WAITV3(N, K[i], V0[i], V1[i]); \
        if (UNLIKELY(any_lane(max(max(K[i][3], V0[i][3]), V1[i][3]) == ~0u))) { \
            _Pragma("unroll 1") for (int s_ = 0; s_ < SPIN_CAP; ++s_) { NRETRY(); KVLD(i); WAITV3(0, K[i], V0[i], V1[i]); if (!any_lane(max(max(K[i][3], V0[i][3]), V1[i][3]) == ~0u)) break; __builtin_amdgcn_s_sleep(2); } } } while (0)
    if (SL1) __builtin_amdgcn_s_sleep(SL1);
    KVLD(2); KVLD(3);
    const u4 Qm = Qf, Qo = lds16(L_OC + w * 2304 + l16);
    f16 Om = {}, Oo = {};
    const f16 z16 = {};
    auto pv_tile = [&](const f16& S, const u4& va, const u4& vb, f16& O) {
        float p[16];
#pragma unroll
        for (int e = 0; e < 16; ++e) p[e] = __builtin_amdgcn_exp2f(S[e]);
        const u4 P0 = {pk(p[0], p[1]), pk(p[2], p[3]), pk(p[4], p[5]), pk(p[6], p[7])}, P1 = {pk(p[8], p[9]), pk(p[10], p[11]), pk(p[12], p[13]), pk(p[14], p[15])};
        O = MFMA32(va, P0, O); O = MFMA32(vb, P1, O);
    };
    const u4 rsWo = mk_rsrc(wo + fb * 128u, 512u * 1024u - fb * 128u);
    const unsigned wol = L_A + w * 8192, wos = w * 65536;
    const unsigned vh = (u8 >> 1) * 1024 + ((u8 & 1) * 32 + 8 * hh + l7) * 16, vs = l < 8 ? (8 * hh + l) * 16 : OOB;
    const unsigned sHw = WS_H + (2 * U64 + j) * OS + fb * 4096, sSw = WS_ST + (2 * U64 + j) * 4096u + fb * 512;
    const unsigned sGa = r < 29 ? WS_G + (8 * g + w) * 90112u + r * 2048 : OOB, sGb = r < 28 ? WS_G + (8 * g + w) * 90112u + (60 + r) * 1024 : OOB;
#define WO_(i) dma_bufi<(i) * 1024, (i) * 8192>(wol, vrow, rsWo, wos)
#define PIN16(a) asm volatile("" : "+v"(a))
#define MEM(...) do { SBAR(); __VA_ARGS__; SBAR(); } while (0)
#define HALF(S, o, va, O) do { float p_[8]; _Pragma("unroll") for (int e = 0; e < 8; ++e) p_[e] = __builtin_amdgcn_exp2f(S[(o) + e]); \
        u4 q_ = {pk(p_[0], p_[1]), pk(p_[2], p_[3]), pk(p_[4], p_[5]), pk(p_[6], p_[7])}; asm volatile("" : "+v"(q_)); O = MFMA32(va, q_, O); PIN16(O); SBAR(); } while (0)
#define SCORE(S, k, q) do { S = MFMA32(k, q, z16); PIN16(S); SBAR(); } while (0)
    f16 Sa, Sb, Sc;
    u2 ck; const unsigned vc1 = WS_F1 + (U64 * 8 + l7) * 128;
    const u4 bad = {~0u, ~0u, ~0u, ~0u};
    KVOK(3, 2);
    STAMP(24);
    SCORE(Sa, K[2], Qm); SCORE(Sb, K[2], Qo); MEM(KVLD(4));
    HALF(Sa, 0, V0[2], Om); MEM(KVLD(0)); HALF(Sa, 8, V1[2], Om); MEM(KVLD(1)); KVOK(9, 3); TWD();
    SCORE(Sa, K[3], Qm); HALF(Sb, 0, V0[2], Oo); MEM(WO_(0)); HALF(Sb, 8, V1[2], Oo); MEM(WO_(1));
    SSTAMP(42);
    SCORE(Sb, K[3], Qo); HALF(Sa, 0, V0[3], Om); MEM(WO_(2)); HALF(Sa, 8, V1[3], Om); MEM(WO_(3)); KVOK(10, 4);
    SCORE(Sa, K[4], Qm); HALF(Sb, 0, V0[3], Oo); MEM(WO_(4)); HALF(Sb, 8, V1[3], Oo); MEM(WO_(5));
    SSTAMP(43);
    SCORE(Sb, K[4], Qo); HALF(Sa, 0, V0[4], Om); MEM(WO_(6)); HALF(Sa, 8, V1[4], Om); MEM(WO_(7)); KVOK(11, 0); KVOK(8, 1);
    if (j == 0) { SCORE(Sa, K[0], Qo); HALF(Sb, 0, V0[4], Oo); MEM(st16(bad, vh, rsF, sHw)); HALF(Sb, 8, V1[4], Oo); MEM(st16(bad, vs, rsF, sSw), st16(bad, l16, rsF, sGb));
                  SCORE(Sb, K[0], Qm); SCORE(Sc, K[1], Qm); HALF(Sa, 0, V0[0], Oo); HALF(Sa, 8, V1[0], Oo); }
    else        { SCORE(Sa, K[1], Qo); HALF(Sb, 0, V0[4], Oo); MEM(st16(bad, vh, rsF, sHw)); HALF(Sb, 8, V1[4], Oo); MEM(st16(bad, vs, rsF, sSw), st16(bad, l16, rsF, sGb));
                  SCORE(Sb, K[0], Qm); SCORE(Sc, K[1], Qm); HALF(Sa, 0, V0[1], Oo); HALF(Sa, 8, V1[1], Oo); }
    SSTAMP(44);
    MEM(ld8(ck, w == 7 ? vc1 : OOB, rsF, 0), GU(2, 0), GU(2, 1), GU(2, 2));
    const unsigned oc = L_OC + w * 2304;
    {
        const f16 Sx = Sb, Sy = Sc;
        float p[16];
#pragma unroll
        for (int e = 0; e < 8; ++e) p[e] = __builtin_amdgcn_exp2f(sel(diag_mask(e), Sx[e], Sy[e]));
        SBAR();
        sts16f(oc + l16, (f4){Oo[0], Oo[1], Oo[2], Oo[3]}); sts16f(oc + 1024 + l16, (f4){Oo[4], Oo[5], Oo[6], Oo[7]}); sts4f(oc + 2048 + l * 4, Oo[8]);
#pragma unroll
        for (int e = 8; e < 16; ++e) p[e] = __builtin_amdgcn_exp2f(sel(diag_mask(e), Sx[e], Sy[e]));
        unsigned P[8];
#pragma unroll
        for (int e = 0; e < 8; ++e) P[e] = pk(p[2 * e], p[2 * e + 1]);
        const u4 Y0 = {P[0] & pm[0], P[1] & pm[1], P[2] & pm[2], P[3] & pm[3]}, Y1 = {P[4] & pm[4], P[5] & pm[5], P[6] & pm[6], P[7] & pm[7]};
        const u4 X0_ = {P[0] ^ Y0[0], P[1] ^ Y0[1], P[2] ^ Y0[2], P[3] ^ Y0[3]}, X1_ = {P[4] ^ Y1[0], P[5] ^ Y1[1], P[6] ^ Y1[2], P[7] ^ Y1[3]};
        Om = MFMA32(V0[0], X0_, Om); Om = MFMA32(V1[0], X1_, Om); Om = MFMA32(V0[1], Y0, Om); Om = MFMA32(V1[1], Y1, Om);
    }
    SBAR();
    STAMP(25);
    asm volatile("s_waitcnt vmcnt(3) lgkmcnt(0)\n\ts_barrier" : "+v"(ck) :: "memory");
    SSTAMP(45);
    const unsigned op = L_OC + (w ^ 1) * 2304;
    f4 oa = lds16f(op + l16) + (f4){Om[0], Om[1], Om[2], Om[3]}, ob = lds16f(op + 1024 + l16) + (f4){Om[4], Om[5], Om[6], Om[7]};
    float den = Om[8] + lds4f(op + 2048 + l * 4);
    if (UNLIKELY(any_lane(!(den >= 0x1p-60f && den <= 0x1p60f)))) {
        f16 O = {}; float m = -__builtin_inff();
#pragma unroll 1
        for (int kb = 8; kb >= 0; --kb) {
            const int T = 2 * (int)T64 + (int)j - 8 + kb;
            if (T < 0) break;
            u4 Ks, Va = ones, Vb = ones;
            ld16(Ks, vk, rsF, kvb + (unsigned)T * 2048); ld16_v(Va, Vb, vv_, rsF, kvb + (unsigned)T * 2048);
            WAITV3(0, Ks, Va, Vb);
            f16 S = MFMA32(Ks, Qm, z16);
            const u64 all = (kb != 0 && kb != 8) ? ~0ull : 0ull, inv = kb == 0 ? ~0ull : 0ull;
            float mx = m;
#pragma unroll
            for (int e = 0; e < 16; ++e) { S[e] = sel((diag_mask(e) ^ inv) | all, S[e], -__builtin_inff()); mx = __builtin_fmaxf(mx, S[e]); }
            mx = max_x32(mx);
            const float al = __builtin_amdgcn_exp2f(m - mx); m = mx;
#pragma unroll
            for (int e = 0; e < 16; ++e) { O[e] *= al; S[e] -= mx; }
            pv_tile(S, Va, Vb, O);
        }
        oa = (f4){O[0], O[1], O[2], O[3]}; ob = (f4){O[4], O[5], O[6], O[7]}; den = O[8];
    }
    const float rd = __builtin_amdgcn_rcpf(den);
    st16((u4){pk2(lo2(oa) * rd), pk2(hi2(oa) * rd), pk2(lo2(ob) * rd), pk2(hi2(ob) * rd)}, l16, rsF, sOw);
#define F23() FLAG(w == 4 ? WS_F2 + (U64 * 8 + fb) * 128 : w == 5 ? WS_F3 + (g * 32 + r) * 128 : OOB)
    F23();
    STAMP(26);
    const unsigned ta0 = (4 * h + q4) * 128 + 64 * (q4 >> 1) + 32 * (g4 & 1) + 8 * pp, ta1 = ta0 ^ 64;
    f16 acc0 = {}, acc1 = {};
    {
        const unsigned sO = WS_O + (2 * U64 + j) * OS + hh * 8192;
        const unsigned oa0 = L_A + hh * 16384 + ta0, oa1 = L_A + hh * 16384 + ta1;
        u4 A0[8], A1[8], B[8];
#pragma unroll
        for (int i = 0; i < 8; ++i) { A0[i] = ldtr(oa0 + i * 2048, oa0 + i * 2048 + 1024); A1[i] = ldtr(oa1 + i * 2048, oa1 + i * 2048 + 1024); }
        if (w == 7 && UNLIKELY(any_lane(ck[0] != tag[0] || ck[1] != tag[1]))) POLL(vc1);
        STAMP(37);
        BAR_VM(1);
        STAMP(5);
        if (SL2) __builtin_amdgcn_s_sleep(SL2);
#pragma unroll
        for (int i = 0; i < 8; ++i) ld16(B[i], l16, rsF, sO + i * 1024);
#define P3_PAIR(i, NW, M0, M1) do { asm volatile("s_waitcnt vmcnt(%2)" : "+v"(B[i]), "+v"(B[(i) + 1]) : "i"(NW) : "memory"); \
            if (UNLIKELY(any_lane(max(B[i][3], B[(i) + 1][3]) == ~0u))) { \
                _Pragma("unroll 1") for (int s_ = 0; s_ < SPIN_CAP; ++s_) { NRETRY(); __builtin_amdgcn_s_sleep(3); asm volatile("s_waitcnt vmcnt(0)" ::: "memory"); unsigned m_ = 0; \
                    _Pragma("unroll") for (int q = (i); q < 8; ++q) ld16(B[q], l16, rsF, sO + q * 1024); \
                    _Pragma("unroll") for (int q = (i); q < 8; ++q) { WAITV1(7 - q, B[q]); m_ = max(m_, B[q][3]); } \
                    if (!any_lane(m_ == ~0u)) break; } } if ((i) == 0) STAMP(4); \
            acc0 = MFMA32(A0[i], B[i], acc0); acc1 = MFMA32(A1[i], B[i], acc1); M0; SBAR(); acc0 = MFMA32(A0[(i) + 1], B[(i) + 1], acc0); acc1 = MFMA32(A1[(i) + 1], B[(i) + 1], acc1); M1; SBAR(); } while (0)
        P3_PAIR(0, 6, NO, NO); P3_PAIR(2, 4, GU(0, 0), GU(0, 1)); P3_PAIR(4, 4, GU(0, 2), GU(3, 0)); P3_PAIR(6, 4, GU(3, 1), GU(3, 2));
    }
    const unsigned vc2 = WS_F2 + (g * 32 + t32) * 128;
    ld8(ck, w == 7 ? vc2 : OOB, rsF, 0);
    STAMP(27);
    const unsigned rw0 = L_A + (hh * 2 + j) * 8192 + h * 512 + t32 * 16, rw1 = rw0 ^ 128, rr = L_A + j * 8192 + u8 * 1024 + (((8 * hh + l7) ^ ((u8 & 1) << 3)) * 16);
    auto reduce = [&](f4& lo, f4& hi) {
#pragma unroll
        for (int u = 0; u < 4; ++u) {
            sts16f(((u & 1) ? rw1 : rw0) + u * 1024, (f4){acc0[4 * u], acc0[4 * u + 1], acc0[4 * u + 2], acc0[4 * u + 3]});
            sts16f(((u & 1) ? rw1 : rw0) + (4 + u) * 1024, (f4){acc1[4 * u], acc1[4 * u + 1], acc1[4 * u + 2], acc1[4 * u + 3]});
        }
        BAR_L();
        lo = (lds16f(rr) + lds16f(rr + 16384)) + (lds16f(rr + 32768) + lds16f(rr + 49152));
        hi = (lds16f(rr + 512) + lds16f(rr + 16896)) + (lds16f(rr + 33280) + lds16f(rr + 49664));
    };
    u4 Hres;
    const unsigned ga = (8 * h + q4) * 192 + (g4 & 1) * 96 + 8 * pp;
    f16 cs[3] = {};
    {
        f4 lo, hi; reduce(lo, hi);
        STAMP(28);
        Hres = (u4){pk2(lo2(lo) + (f2){blo(xres[0]), bhi(xres[0])}), pk2(hi2(lo) + (f2){blo(xres[1]), bhi(xres[1])}), pk2(lo2(hi) + (f2){blo(xres[2]), bhi(xres[2])}), pk2(hi2(hi) + (f2){blo(xres[3]), bhi(xres[3])})};
        st16(Hres, vh, rsF, sHw);
        f2 s1 = {0.f, 0.f}, s2 = {0.f, 0.f};
#pragma unroll
        for (int e = 0; e < 4; ++e) { const f2 a = {blo(Hres[e]), bhi(Hres[e])}; s1 += a; s2 += a * a; }
        const float t1 = add_x32(add_x16(add_x8(s1[0] + s1[1]))), t2 = add_x32(add_x16(add_x8(s2[0] + s2[1])));
        st16((u4){uf(t1), uf(t2), uf(t1), uf(t2)}, vs, rsF, sSw);
        STAMP(33);
        asm volatile("s_waitcnt vmcnt(2) lgkmcnt(0)\n\ts_barrier" : "+v"(ck) :: "memory");
        STAMP(40);
        const unsigned o = ga + w * 12288;
        u4 a[12];
#pragma unroll
        for (int i = 0; i < 12; ++i) a[i] = ldtr(o + (i / 3) * 3072 + (i % 3) * 32, o + (i / 3) * 3072 + (i % 3) * 32 + 768);
        SBAR();
#pragma unroll
        for (int i = 0; i < 12; ++i) cs[i % 3] = MFMA32(ones, a[i], cs[i % 3]);
        sts16f(L_CSP + w * 512 + t32 * 16, (f4){cs[0][0], cs[1][0], cs[2][0], 0.f});
    }
    STAMP(36);
    const u4 rsWd = mk_rsrc(wd + fb * 128u, 1376u * 1024u - fb * 128u);
    if (w == 7 && UNLIKELY(any_lane(ck[0] != tag[0] || ck[1] != tag[1]))) POLL(vc2);
    if (w == 5 && b == 255) {
#pragma unroll 1
        for (int s_ = 0; s_ < 4 * SPIN_CAP; ++s_) { u2 f0, f1, f2_, f3; const unsigned vc = WS_F1 + l * 128;
            ld8(f0, vc, rsF, 0); ld8(f1, vc, rsF, 8192); ld8(f2_, vc, rsF, 16384); ld8(f3, vc, rsF, 24576); WAITV4(0, f0, f1, f2_, f3);
            if (!any_lane(((f0[0] ^ tag[0]) | (f1[0] ^ tag[0]) | (f2_[0] ^ tag[0]) | (f3[0] ^ tag[0]) | (f0[1] ^ tag[1]) | (f1[1] ^ tag[1]) | (f2_[1] ^ tag[1]) | (f3[1] ^ tag[1])) != 0u)) break; }
        FLAG(0u);
    }
    BAR_VM(0);
    STAMP(6);
    if (SL3) __builtin_amdgcn_s_sleep(SL3);
    const unsigned a0 = 15 * hh, a1 = a0 + 1, jo = j * 8192;
    const unsigned wde = (48 * (a0 >> 1) + 16 * (a0 & 1)) * 1024 + jo, wdo = (48 * (a1 >> 1) + 16 * (a1 & 1)) * 1024 + jo, wdt = (336 * hh + 32) * 1024 + jo;
    const unsigned wla = L_A + hh * 14336 + j * 1024, wlb = hh * 12288 + j * 1024, wlc = 49152 + hh * 4096 + j * 1024;
#define WD(i) dma_bufi<((i) < 14 ? (i) % 7 : (i) < 20 ? (i) - 14 : (i) - 20) * 2048u, ((i) < 15 ? (i) >> 1 : (i) - 15) * 49152u>((i) < 14 ? wla : (i) < 20 ? wlb : wlc, vrow, rsWd, (i) >= 15 ? wdt : ((i) & 1) ? wdo : wde)
    u4 D0[7], D1[7];
    {
        const unsigned sH = WS_H + (8 * g + w) * OS, sS = WS_ST + (8 * g + w) * 4096u;
#define PD4 6
        constexpr int PD = PD4;
        u4 Hb[32], A0[3], A1[3], A2[4];
        u2 st[4];
        f16 ag[3] = {};
        const unsigned cf = L_CF + w * 384, vstat = h * 2048 + t32 * 16;
#pragma unroll
        for (int i = 0; i < PD; ++i) ld16(Hb[kgo(i)], l16, rsF, sH + kgo(i) * 1024);
        {
            const unsigned o = L_CSP + h * 2048 + t32 * 16; const f4 s = (lds16f(o) + lds16f(o + 512)) + (lds16f(o + 1024) + lds16f(o + 1536));
            const float c0 = add_x32(s[0]), c1 = add_x32(s[1]), c2 = add_x32(s[2]);
            sts4f(cf + l * 4, h ? c1 : c0); sts4f(cf + 256 + t32 * 4, c2); }
        A0[0] = ldtr(ga, ga + 768); A1[0] = ldtr(ga + 32, ga + 800); A0[1] = ldtr(ga + kgo(1) * 3072, ga + kgo(1) * 3072 + 768); A1[1] = ldtr(ga + kgo(1) * 3072 + 32, ga + kgo(1) * 3072 + 800);
        unsigned mx = 0;
#define HDS 10
#define P4_STEP(i) do { constexpr int c = (i) % 3, n = ((i) + 2) % 3, k = kgo(i); const unsigned o = ga + ((i) < 30 ? kgo((i) + 2) * 3072 : 0); \
            if ((i) + PD < 32) ld16(Hb[kgo((i) + PD < 32 ? (i) + PD : 0)], l16, rsF, sH + kgo((i) + PD) * 1024); \
            else if ((i) + PD < 36) ld8(st[((i) + PD - 32) & 3], vstat, rsF, sS + ((i) + PD - 32) * 512); \
            WAITV1((i) + PD < 36 ? PD : 35 - (i), Hb[k]); if ((i) == HDS) HOTB(3, wd, 43); \
            if ((i) < 8) FRESH16(Hb[k], l16, rsF, sH + k * 1024); else { mx = max(mx, Hb[k][3]); asm volatile("" : "+v"(mx)); } if ((i) == 0) STAMP(7); SBAR(); \
            ag[0] = MFMA32(A0[c], Hb[k], ag[0]); if ((i) < 30) A0[n] = ldtr(o, o + 768); else A2[((i) - 30) & 1] = ldtr(ga + 64 + (((i) - 30) & 1) * 3072, ga + 832 + (((i) - 30) & 1) * 3072); SBAR(); \
            ag[1] = MFMA32(A1[c], Hb[k], ag[1]); if ((i) < 30) A1[n] = ldtr(o + 32, o + 800); else if ((i) == 31) A2[2] = ldtr(ga + 64 + 6144, ga + 832 + 6144); SBAR(); } while (0)
#define P4_4(i) P4_STEP(i); P4_STEP((i) + 1); P4_STEP((i) + 2); P4_STEP((i) + 3)
        P4_4(0); P4_4(4); P4_4(8); P4_4(12); P4_4(16); P4_4(20); P4_4(24); P4_4(28);
        WAITV4(0, st[0], st[1], st[2], st[3]);
        if (UNLIKELY(any_lane(max(max(mx, max(st[0][1], st[1][1])), max(st[2][1], st[3][1])) == ~0u))) {
#pragma unroll
            for (int i = 0; i < 32; ++i) FRESH16(Hb[i], l16, rsF, sH + i * 1024);
#pragma unroll 1
            for (int s_ = 0; s_ < SPIN_CAP; ++s_) { NRETRY();
#pragma unroll
                for (int i = 0; i < 4; ++i) ld8(st[i], vstat, rsF, sS + i * 512);
                WAITV4(0, st[0], st[1], st[2], st[3]);
                if (!any_lane(max(max(st[0][1], st[1][1]), max(st[2][1], st[3][1])) == ~0u)) break;
            }
            ag[0] = z16; ag[1] = z16;
#pragma unroll
            for (int i = 0; i < 32; ++i) { const unsigned o = ga + kgo(i) * 3072; ag[0] = MFMA32(ldtr(o, o + 768), Hb[kgo(i)], ag[0]); ag[1] = MFMA32(ldtr(o + 32, o + 800), Hb[kgo(i)], ag[1]); }
        }
        STAMP(8);
        const float t1 = add_x32(fu(st[0].x) + fu(st[1].x) + fu(st[2].x) + fu(st[3].x)), t2 = add_x32(fu(st[0].y) + fu(st[1].y) + fu(st[2].y) + fu(st[3].y));
        const float nm = t1 * (-1.f / 512), ve = __builtin_fmaxf(t2 * (1.f / 512) - nm * nm, 0.f) + 1e-5f, rs = __builtin_amdgcn_rsqf(ve);
        const float ka_ = rs * -2.3022081986f, kb_ = rs * rs * rs * -0.1029432397f;
#define G_UNIT(u, so_, M0, M1) do { u4 y; _Pragma("unroll") for (int qd = 0; qd < 2; ++qd) { \
                const f4 cg = lds16f(cf + (u) * 128 + qd * 32 + h * 16), cu = lds16f(cf + (u) * 128 + (2 + qd) * 32 + h * 16); \
                _Pragma("unroll") for (int e = 0; e < 2; ++e) { \
                    const f2 gt = (f2){ag[u][4 * qd + 2 * e], ag[u][4 * qd + 2 * e + 1]} + (e ? hi2(cg) : lo2(cg)) * nm, up = (f2){ag[u][8 + 4 * qd + 2 * e], ag[u][9 + 4 * qd + 2 * e]} + (e ? hi2(cu) : lo2(cu)) * nm; \
                    const f2 z = gt * (gt * gt * kb_ + ka_), d = (f2){__builtin_amdgcn_exp2f(z[0]), __builtin_amdgcn_exp2f(z[1])} * ve + ve; \
                    unsigned y_ = pk2(gt * up * (f2){__builtin_amdgcn_rcpf(d[0]), __builtin_amdgcn_rcpf(d[1])}); asm volatile("" : "+v"(y_)); y[2 * qd + e] = y_; } \
                if (qd == 0) MEM(M0); } \
            MEM(st16(y, l16, rsF, so_); M1); } while (0)
        MEM(WD(0), WD(1), WD(2));
        G_UNIT(0, sGa, (WD(3), WD(4), WD(5), WD(6)), NO); G_UNIT(1, sGa + 1024, NO, NO);
        STAMP(47);
        const unsigned da0 = L_A + hh * 14336 + ta0, da1 = L_A + hh * 14336 + ta1;
        const unsigned vc3 = WS_F3 + ((U64 >> 2) * 32 + min(t32, 28u)) * 128, vc4 = vc3 + (WS_F4 - WS_F3);
#define B1 6
#define F4D 0
#define CKS (B1 + 21)
#define WDS(i, m) if ((i) == B1 + 8 + (B1 + 21 <= 31 ? (m) : (m) * (24 - B1) / 13)) WD(7 + (m))
#define P4B_STEP(i) do { constexpr int c = (i) & 3, n = ((i) + 3) & 3; const unsigned o = ga + ((i) + 3) * 3072 + 64; \
            if ((i) == B1) SSTAMP(48); if ((i) == B1 + 1) SSTAMP(49); if ((i) == B1 + 8) SSTAMP(50); if ((i) == B1 + 9) SSTAMP(51); if ((i) == B1 + 15) SSTAMP(52); if ((i) == B1 + 21) SSTAMP(53); \
            if ((i) == B1) BAR_VM(2);         \
            if ((i) == B1 + 8) BAR_VM(0);        \
            if ((i) == B1 + 8 + F4D) FLAG(w == 4 ? WS_F4 + (g * 32 + r) * 128 : OOB); SBAR(); \
            ag[2] = MFMA32(A2[c], Hb[i], ag[2]); if ((i) < 29) A2[n] = ldtr(o, o + 768); \
            if ((i) >= B1 && (i) < B1 + 7) { constexpr int q = ((i) + 14 - B1) % 7; D0[q] = ldtr(da0 + q * 2048, da0 + q * 2048 + 1024); D1[q] = ldtr(da1 + q * 2048, da1 + q * 2048 + 1024); } SBAR(); \
            WDS(i, 0); WDS(i, 1); WDS(i, 2); WDS(i, 3); WDS(i, 4); WDS(i, 5); WDS(i, 6); WDS(i, 7); WDS(i, 8); WDS(i, 9); WDS(i, 10); WDS(i, 11); WDS(i, 12); \
            if ((i) == CKS) ld8(ck, w == 7 ? vc3 : w == 6 ? vc4 : OOB, rsF, 0); SBAR(); } while (0)
#define P4B_4(i) P4B_STEP(i); P4B_STEP((i) + 1); P4B_STEP((i) + 2); P4B_STEP((i) + 3)
        P4B_4(0); P4B_4(4); P4B_4(8); P4B_4(12); P4B_4(16); P4B_4(20); P4B_4(24); P4B_4(28);
        STAMP(41);
        G_UNIT(2, sGb, NO, NO);
        STAMP(30);
        WAITV1(1, ck);
        if (w >= 6 && UNLIKELY(any_lane(ck[0] != tag[0] || ck[1] != tag[1]))) { const unsigned vc = w == 7 ? vc3 : vc4; POLL(vc); }
    }
    BAR_L();
    STAMP(9);
    {
        constexpr int PD = 6;
        u4 Gb[22], A0[2], A1[2];
        const unsigned gs = WS_G + (2 * U64 + j) * 90112u + 15 * hh * 1024, gz = hh == 3 ? OOB : gs, gt = WS_G + (2 * U64 + j) * 90112u + (60 + 7 * hh - 15) * 1024;
        const unsigned o0 = L_A + hh * 14336 + ta0, o1 = L_A + hh * 14336 + ta1, p0 = hh * 12288 + ta0, p1 = hh * 12288 + ta1, q0 = 49152 + hh * 4096 + ta0, q1 = 49152 + hh * 4096 + ta1;
#define GOFF(i) (((i) >= 15 ? gt : (i) >= 13 ? gz : gs) + (i) * 1024)
#pragma unroll
        for (int i = 0; i < PD; ++i) ld16(Gb[i], l16, rsF, GOFF(i));
        WD(20); WD(21);
        acc0 = z16; acc1 = z16;
#define P5_STEP(i) do { constexpr int c = (i) & 1, n = c ^ 1; \
            if ((i) + PD < 22) ld16(Gb[(i) + PD < 22 ? (i) + PD : 0], l16, rsF, GOFF((i) + PD)); \
            WAITV1(((i) + PD < 22 ? PD : 21 - (i)) + ((i) < PD ? 2 : 0), Gb[i]); if ((i) >= 15) FRESH16(Gb[i], l16, rsF, GOFF(i)); if ((i) == 0) STAMP(10); \
            if ((i) == 14) BAR_L(); SBAR();        \
            const unsigned a0_ = (i) < 13 ? o0 + ((i) - 6) * 2048 : (i) < 19 ? p0 + ((i) - 13) * 2048 : q0 + ((i) - 19) * 2048, a1_ = (i) < 13 ? o1 + ((i) - 6) * 2048 : (i) < 19 ? p1 + ((i) - 13) * 2048 : q1 + ((i) - 19) * 2048; \
            acc0 = MFMA32((i) < 7 ? D0[(i) < 7 ? (i) : 0] : A0[c], Gb[i], acc0); if ((i) >= 6 && (i) < 21) A0[n] = ldtr(a0_, a0_ + 1024); SBAR(); \
            acc1 = MFMA32((i) < 7 ? D1[(i) < 7 ? (i) : 0] : A1[c], Gb[i], acc1); if ((i) >= 6 && (i) < 21) A1[n] = ldtr(a1_, a1_ + 1024); SBAR(); } while (0)
        P5_STEP(0); P5_STEP(1); P5_STEP(2); P5_STEP(3); P5_STEP(4); P5_STEP(5); P5_STEP(6); P5_STEP(7); P5_STEP(8); P5_STEP(9); P5_STEP(10);
        P5_STEP(11); P5_STEP(12); P5_STEP(13); P5_STEP(14); P5_STEP(15); P5_STEP(16); P5_STEP(17); P5_STEP(18); P5_STEP(19); P5_STEP(20); P5_STEP(21);
    }
    STAMP(11);
    {
        f4 lo, hi; reduce(lo, hi);
        const u4 Ores = {pk2(lo2(lo) + (f2){blo(Hres[0]), bhi(Hres[0])}), pk2(hi2(lo) + (f2){blo(Hres[1]), bhi(Hres[1])}), pk2(lo2(hi) + (f2){blo(Hres[2]), bhi(Hres[2])}), pk2(hi2(hi) + (f2){blo(Hres[3]), bhi(Hres[3])})};
        st16_plain(Ores, vres, mk_rsrc(out + U64 * 65536u, 65536u), 0);
    }
    asm volatile(".Lfb_code1:");
    STAMP(12);
#endif
}

#define WSOFF 0
extern "C" void kernel_launch(void* const* d_in, const int* in_sizes, int n_in, void* d_out, int out_size, void* d_ws, size_t ws_size, hipStream_t stream) {
    (void)in_sizes; (void)n_in; (void)out_size; (void)ws_size;
    (void)hipFuncSetAttribute((const void*)fused_block, hipFuncAttributeMaxDynamicSharedMemorySize, LDS_BYTES);
    fused_block<<<256, 512, LDS_BYTES, stream>>>((const char*)d_in[0], (const char*)d_in[1], (const char*)d_in[2], (const char*)d_in[4], (const char*)d_in[5], (const char*)d_in[6],
                                                 (char*)d_out, (char*)d_ws + WSOFF);
}
#define LASTSTAMP 12
#define STAMP_NAMES "start", "P1 loop end", "KV drained", 0, "O first data", "O drained", "h drained", "h first data", "P4 pass1 end", "P4 end bar", "G first data", "P5 loop end", "end", 0, 0, 0, \
  "st0", "st1", "st2", "st3", "st4", "st5", "st6", "st7", "KV first data", "att done", "O stored", "P3 loop end", "P3 reduced", 0, "G stored", 0, "KV stored", "h stored", \
  "LN sums", "pre KV bar", "colsum done", "A read", 0, "setup done", "GU bar", "P4 pass2 end", "att blk3", "att blk4", "att blk0", "comb bar", 0, "G01 stored", "p2 pre B1", "p2 s7", "p2 pre B2", "p2 s15", "p2 s21", "p2 s27", "h landed", "st landed"
```

```cpp
#define CF 0
#include <hip/hip_runtime.h>
#include <hip/hip_bf16.h>
#include <stdint.h>
#pragma clang diagnostic ignored "-Wint-to-pointer-cast"

typedef __bf16 bf8 __attribute__((ext_vector_type(8)));
typedef __bf16 bf2 __attribute__((ext_vector_type(2)));
typedef short s4 __attribute__((ext_vector_type(4)));
typedef short s8 __attribute__((ext_vector_type(8)));
typedef float f2 __attribute__((ext_vector_type(2)));
typedef float f4 __attribute__((ext_vector_type(4)));
typedef float f16 __attribute__((ext_vector_type(16)));
typedef unsigned u2 __attribute__((ext_vector_type(2)));
typedef unsigned u4 __attribute__((ext_vector_type(4)));
typedef unsigned long long u64;
#define LAS __attribute__((address_space(3)))
#define DEV __device__ __forceinline__

constexpr unsigned WS_F1 = 32768, WS_F2 = 65536, WS_F3 = 98304, WS_F4 = 131072;
#define KVS 132096u
#define OS 33792u
constexpr unsigned WS_KV = 1u << 20;
constexpr unsigned WS_O = WS_KV + 32 * KVS;
constexpr unsigned WS_H = WS_O + 64 * OS;
constexpr unsigned WS_ST = WS_H + 64 * OS;
constexpr unsigned WS_G = WS_ST + 64 * 4096;
static_assert(WS_G + 64 * 90112u <= (16u << 20), "hand-off buffers");
constexpr unsigned WS_DIAG = 32u << 20;
constexpr unsigned LDS_BYTES = 163840;
constexpr unsigned L_HOLE = 73728;
constexpr unsigned L_OC = L_HOLE;
constexpr unsigned L_A = 98304;
constexpr unsigned L_CSP = 155648, L_CF = L_CSP + 4096;
constexpr int SPIN_CAP = 1 << 14;
constexpr unsigned OOB = 0x40000000u;

#define MFMA16(a, b, c) __builtin_amdgcn_mfma_f32_16x16x32_bf16(__builtin_bit_cast(bf8, a), __builtin_bit_cast(bf8, b), c, 0, 0, 0)
#define MFMA32(a, b, c) __builtin_amdgcn_mfma_f32_32x32x16_bf16(__builtin_bit_cast(bf8, a), __builtin_bit_cast(bf8, b), c, 0, 0, 0)
#define SBAR() __builtin_amdgcn_sched_barrier(0)
#define BAR_VM(N) asm volatile("s_waitcnt vmcnt(%0) lgkmcnt(0)\n\ts_barrier" ::"i"(N) : "memory")
#define BAR_L() asm volatile("s_waitcnt lgkmcnt(0)\n\ts_barrier" ::: "memory")

DEV u4 mk_rsrc(const void* p, unsigned bytes) { const u64 a = (u64)p; return (u4){(unsigned)a, (unsigned)(a >> 32) & 0xffffu, bytes, 0x00020000u}; }
DEV void dma_buf(unsigned lds, unsigned voff, u4 rs, unsigned soff) {
    asm volatile("s_mov_b32 m0, %0\n\ts_nop 0\n\tbuffer_load_dwordx4 %1, %2, %3 offen lds" ::"s"(lds), "v"(voff), "s"(rs), "s"(soff) : "memory"); }
template <unsigned LI, unsigned SI> DEV void dma_bufi(unsigned lds, unsigned voff, u4 rs, unsigned soff) {
    unsigned t; asm volatile("s_add_u32 m0, %1, %5\n\ts_add_u32 %0, %4, %6\n\tbuffer_load_dwordx4 %2, %3, %0 offen lds" : "=&s"(t) : "s"(lds), "v"(voff), "s"(rs), "s"(soff), "i"(LI), "i"(SI) : "memory", "scc"); }
template <unsigned LI> DEV void dma_glbi(unsigned lds, const char* p) {
    asm volatile("s_add_u32 m0, %0, %2\n\ts_nop 0\n\tglobal_load_lds_dwordx4 %1, off" ::"s"(lds), "v"(p), "i"(LI) : "memory", "scc"); }
DEV void dma_glb(unsigned lds, const char* p) {
    asm volatile("s_mov_b32 m0, %0\n\ts_nop 0\n\tglobal_load_lds_dwordx4 %1, off" ::"s"(lds), "v"(p) : "memory"); }
DEV void ld16(u4& d, unsigned voff, u4 rs, unsigned soff) {
    asm volatile("buffer_load_dwordx4 %0, %1, %2, %3 offen sc1" : "=v"(d) : "v"(voff), "s"(rs), "s"(soff) : "memory"); }
DEV void ld16_plain(u4& d, unsigned voff, u4 rs, unsigned soff) {
    asm volatile("buffer_load_dwordx4 %0, %1, %2, %3 offen" : "=v"(d) : "v"(voff), "s"(rs), "s"(soff) : "memory"); }
template <int OFF> DEV void ld16_imm(u4& d, unsigned voff, u4 rs) {
    asm volatile("buffer_load_dwordx4 %0, %1, %2, 0 offen offset:%3" : "=v"(d) : "v"(voff), "s"(rs), "i"(OFF) : "memory"); }
DEV void ld8(u2& d, unsigned voff, u4 rs, unsigned soff) {
    asm volatile("buffer_load_dwordx2 %0, %1, %2, %3 offen sc1" : "=v"(d) : "v"(voff), "s"(rs), "s"(soff) : "memory"); }
DEV void ld16_v(u4& d0, u4& d1, unsigned voff, u4 rs, unsigned soff) {
    asm volatile("s_mov_b64 exec, %5\n\tbuffer_load_dwordx4 %0, %2, %3, %4 offen offset:1024 sc1\n\tbuffer_load_dwordx4 %1, %2, %3, %4 offen offset:1536 sc1\n\ts_mov_b64 exec, -1"
                 : "+v"(d0), "+v"(d1) : "v"(voff), "s"(rs), "s"(soff), "s"(0x0000FFFF0000FFFFull) : "memory"); }
DEV void st16(u4 d, unsigned voff, u4 rs, unsigned soff) {
    asm volatile("buffer_store_dwordx4 %0, %1, %2, %3 offen sc1\n\ts_nop 1" ::"v"(d), "v"(voff), "s"(rs), "s"(soff) : "memory"); }
DEV void st16_plain(u4 d, unsigned voff, u4 rs, unsigned soff) {
    asm volatile("buffer_store_dwordx4 %0, %1, %2, %3 offen\n\ts_nop 1" ::"v"(d), "v"(voff), "s"(rs), "s"(soff) : "memory"); }
DEV void st8(u2 d, unsigned voff, u4 rs, unsigned soff) {
    asm volatile("buffer_store_dwordx2 %0, %1, %2, %3 offen sc1\n\ts_nop 1" ::"v"(d), "v"(voff), "s"(rs), "s"(soff) : "memory"); }
#define WAITV1(N, a) asm volatile("s_waitcnt vmcnt(%1)" : "+v"(a) : "i"(N) : "memory")
#define WAITV1D(ND, NS, a) asm volatile("s_waitcnt vmcnt(%1)\n\ts_cmp_lt_u32 %3, 4\n\ts_cbranch_scc1 .Lw%=\n\ts_waitcnt vmcnt(%2)\n.Lw%=:" : "+v"(a) : "i"(ND), "i"(NS), "s"(w) : "memory", "scc")
#define WAITV3(N, a, b, c) asm volatile("s_waitcnt vmcnt(%3)" : "+v"(a), "+v"(b), "+v"(c) : "i"(N) : "memory")
#define WAITV4(N, a, b, c, d) asm volatile("s_waitcnt vmcnt(%4)" : "+v"(a), "+v"(b), "+v"(c), "+v"(d) : "i"(N) : "memory")

DEV u4 lds16(unsigned a) { return *(LAS const u4*)a; }
DEV f4 lds16f(unsigned a) { return *(LAS const f4*)a; }
DEV float lds4f(unsigned a) { return *(LAS const float*)a; }
DEV void sts16(unsigned a, u4 v) { *(LAS u4*)a = v; }
DEV void sts16f(unsigned a, f4 v) { *(LAS f4*)a = v; }
DEV void sts8f(unsigned a, f2 v) { *(LAS f2*)a = v; }
DEV void sts4f(unsigned a, float v) { *(LAS float*)a = v; }
DEV u4 ldtr(unsigned a0, unsigned a1) {
    const s4 lo = __builtin_amdgcn_ds_read_tr16_b64_v4i16((LAS s4*)a0), hi = __builtin_amdgcn_ds_read_tr16_b64_v4i16((LAS s4*)a1);
    return __builtin_bit_cast(u4, __builtin_shufflevector(lo, hi, 0, 1, 2, 3, 4, 5, 6, 7)); }
DEV unsigned pk2(f2 a) { return __builtin_bit_cast(unsigned, __builtin_convertvector(a, bf2)); }
DEV f2 lo2(f4 a) { return (f2){a[0], a[1]}; }
DEV f2 hi2(f4 a) { return (f2){a[2], a[3]}; }
DEV unsigned pk(float a, float b) { return __builtin_bit_cast(unsigned, __builtin_convertvector((f2){a, b}, bf2)); }
DEV float blo(unsigned v) { return __builtin_bit_cast(float, v << 16); }
DEV float bhi(unsigned v) { return __builtin_bit_cast(float, v & 0xffff0000u); }
DEV float fu(unsigned v) { return __builtin_bit_cast(float, v); }
DEV unsigned uf(float v) { return __builtin_bit_cast(unsigned, v); }
DEV float add_x32(float v) { auto r = __builtin_amdgcn_permlane32_swap(uf(v), uf(v), false, false); const unsigned a = r[0], b = r[1]; return fu(a) + fu(b); }
DEV float add_x16(float v) { auto r = __builtin_amdgcn_permlane16_swap(uf(v), uf(v), false, false); const unsigned a = r[0], b = r[1]; return fu(a) + fu(b); }
DEV float max_x32(float v) { auto r = __builtin_amdgcn_permlane32_swap(uf(v), uf(v), false, false); const unsigned a = r[0], b = r[1]; return __builtin_fmaxf(fu(a), fu(b)); }
template <int C> DEV float add_dpp(float v) { return v + fu(__builtin_amdgcn_update_dpp(0u, uf(v), C, 0xf, 0xf, true)); }
DEV float add_x8(float v) { return v + fu(__builtin_amdgcn_update_dpp(0u, uf(v), 0x128, 0xf, 0xf, true)); }
constexpr u64 diag_mask(int r) { u64 m = 0; for (int l = 0; l < 64; ++l) { const int key = 16 * ((r >> 2) & 1) + 8 * (l >> 5) + 4 * (r >> 3) + (r & 3); if (key <= (l & 31)) m |= 1ull << l; } return m; }
DEV float sel(u64 m, float t, float f) { return __builtin_amdgcn_inverse_ballot_w64(m) ? t : f; }

template <unsigned LDS> DEV void touch(unsigned voff, u4 rs, unsigned soff) {
    asm volatile("s_mov_b32 m0, %3\n\ts_nop 0\n\tbuffer_load_dword %0, %1, %2 offen lds" ::"v"(voff), "s"(rs), "s"(soff), "i"(LDS) : "memory"); }
typedef short sh2 __attribute__((ext_vector_type(2)));
constexpr int kgo(int i) { return i < 8 ? 4 * i : (i - 8) / 3 * 4 + (i - 8) % 3 + 1; }
#define RSLEEP 4
#define SL1 0
#define SL2 0
#define SL3 0
DEV bool any_lane(bool c) { return __builtin_amdgcn_ballot_w64(c) != 0; }
#define UNLIKELY(x) __builtin_expect(!!(x), 0)
#define NRETRY()
#define FRESH16(d, voff, rs, soff) do { if (UNLIKELY(any_lane((d)[3] == ~0u))) { \
        _Pragma("unroll 1") for (int s_ = 0; s_ < SPIN_CAP; ++s_) { NRETRY(); u4 t_; asm volatile("buffer_load_dwordx4 %0, %1, %2, %3 offen sc1\n\ts_waitcnt vmcnt(0)" : "=&v"(t_) : "v"(voff), "s"(rs), "s"(soff) : "memory"); \
            d = t_; if (!any_lane(t_[3] == ~0u)) break; __builtin_amdgcn_s_sleep(2); } } } while (0)
#define POLL(voff) do { \
        _Pragma("unroll 1") for (int s_ = 0; s_ < 4 * SPIN_CAP; ++s_) { u2 t_; asm volatile("buffer_load_dwordx2 %0, %1, %2, 0 offen sc1\n\ts_waitcnt vmcnt(0)" : "=&v"(t_) : "v"(voff), "s"(rsF) : "memory"); \
            if (!any_lane(t_[0] != tag[0] || t_[1] != tag[1])) break; NRETRY(); __builtin_amdgcn_s_sleep(RSLEEP); } } while (0)
#define FLAG(off) st8(tag, l == 0 ? (off) : OOB, rsF, 0)
#define STAMP(i)

__global__ __launch_bounds__(512, 2) void fused_block(const char* x_, const char* wqkv_, const char* wo_, const char* wg_, const char* wu_, const char* wd_, char* out_, char* ws_) {
#if defined(__HIP_DEVICE_COMPILE__)

    typedef u64 u64x8 __attribute__((ext_vector_type(8)));
    u64x8 ka; asm volatile("s_load_dwordx16 %0, %1, 0x0\n\ts_waitcnt lgkmcnt(0)" : "=s"(ka) : "s"(__builtin_amdgcn_kernarg_segment_ptr()) : "memory");

    const char *x = (const char*)ka[0], *wqkv = (const char*)ka[1], *wo = (const char*)ka[2], *wg = (const char*)ka[3], *wu = (const char*)ka[4], *wd = (const char*)ka[5]; char *out = (char*)ka[6], *ws = (char*)ka[7];
    const unsigned l = threadIdx.x & 63, w = __builtin_amdgcn_readfirstlane(threadIdx.x >> 6);
    const unsigned b = blockIdx.x, xc = b & 7, rk = b >> 3;
    const unsigned T64 = 8 * (xc >> 1) + (rk >> 2), hq = 4 * (xc & 1) + (rk & 3);
    STAMP(0);
    const unsigned u8 = l >> 3, l7 = l & 7;
    u4 xr[8];
    const u4 rsX = mk_rsrc(x + T64 * 65536u, 65536u);
    const unsigned vx = (8 * w + u8) * 1024 + l7 * 16;
    const char* pc; unsigned nb; asm volatile("s_getpc_b64 %0\n.Lfb_code0:\n\ts_mov_b32 %1, .Lfb_code1-.Lfb_code0" : "=s"(pc), "=s"(nb));
#define TOUCH_A(CODE, WQ) do { const unsigned co = ((rk * 8 + w) * 2 + (l & 1)) * 128; \
        const char* p = (WQ && (!CODE || l < 32)) ? wqkv + ((b * 8 + w) * 6 + (l < 6 ? l : 0)) * 128 : pc + (co < nb ? co : 0); \
        asm volatile("global_load_dword v254, %0, off" ::"v"(p) : "memory", "v254"); } while (0)
    if (CF == 1) TOUCH_A(1, 0);
    if (CF == 2) TOUCH_A(1, 1);
#define HOT 7
    {
        const unsigned hs = (3u - (unsigned)((u64)wqkv >> 7)) & 7u;
        if ((hs >> 2) == (xc & 1) && w == HOT) { const unsigned z = rk * 48 + (l < 48 ? l : 0); const char* p = wqkv + (hs * 128 + z * 1024); asm volatile("global_load_dword v254, %0, off" ::"v"(p) : "memory", "v254"); } }
    ld16_imm<0>(xr[0], vx, rsX); ld16_imm<128>(xr[1], vx, rsX); ld16_imm<256>(xr[2], vx, rsX); ld16_imm<384>(xr[3], vx, rsX);
#define XB 6
    if (XB > 4) { ld16_imm<512>(xr[4], vx, rsX); ld16_imm<640>(xr[5], vx, rsX); }
    if (XB > 6) { ld16_imm<768>(xr[6], vx, rsX); ld16_imm<896>(xr[7], vx, rsX); }
    const u4 rsW = mk_rsrc(wqkv + hq * 128u, 512u * 3072u - hq * 128u);
    SBAR();
    const unsigned w3 = w & 3, wk2 = w3 * 2048;
    unsigned vw0 = (16 * w3 + u8) * 3072 + 32 * ((l7 >> 1) ^ ((l >> 4) & 1)) + 16 * (l7 & 1), vw1 = (vw0 + 24576) ^ 64;
#define SLOT(i) ((((i) + 1) & 3) * 24576u)
#define SP(i, p) dma_bufi<SLOT(i) + ((p) >> 1) * 8192 + ((p) & 1) * 1024, (i) * 196608u + ((p) >> 1) * 1024u>(wk2, ((p) & 1) ? vw1 : vw0, rsW, 0)
    const unsigned U64 = 4 * xc + (rk >> 3), fb = rk & 7;
    const unsigned g = 2 * (xc >> 1) + (rk >> 4), r = 16 * (xc & 1) + (rk & 15);
    const unsigned j = w & 1, hh = w >> 1;
    const unsigned t16 = l & 15, g4 = l >> 4, h = l >> 5, t32 = l & 31, q4 = t16 >> 2, pp = l & 3, l16 = l * 16, l128 = l * 128;
    const u4 rsF = mk_rsrc(ws, 16u << 20);
    const u4 ones = {0x3f803f80u, 0x3f803f80u, 0x3f803f80u, 0x3f803f80u};
    const unsigned kvb = WS_KV + (4 * hq + hh) * KVS;
    const unsigned skv = kvb + (2 * T64 + j) * 2048;
    const unsigned sOw = WS_O + (2 * T64 + j) * OS + (4 * hq + hh) * 1024;
    u2 tag; u4 xres;
    unsigned vres = (32 * j + 8 * hh + l7) * 1024 + fb * 128 + u8 * 16;
    unsigned xw0 = L_A + (8 * w + u8) * 1024 + 16 * (l7 ^ (8 * (w & 1) + u8)), xw1 = xw0 ^ 128;
    unsigned xa[4];
#pragma unroll
    for (int q = 0; q < 4; ++q) { xa[q] = L_A + (32 * j + t16) * 1024 + 16 * ((4 * q + g4) ^ t16); asm volatile("" : "+v"(xa[q])); }
    unsigned wa = (8 * g4 + q4) * 128 + 32 * (hh ^ ((q4 >> 1) | ((g4 & 1) << 1))) + 8 * pp;
    f4 aq[2] = {}, ak[2] = {}, av[2] = {};
    unsigned vk = (h * 32 + (16 * ((t32 >> 3) & 1) + 8 * ((t32 >> 2) & 1) + 4 * (t32 >> 4) + (t32 & 3))) * 16, vv_ = (h * 16 + t16) * 16;
    unsigned vst = 1024 + ((g4 & 1) * 32 + (g4 >> 1) * 16 + t16) * 16;
    unsigned vrow = u8 * 1024 + 16 * (l7 ^ (((l >> 4) & 1) << 2));
    const char* gp[3];
#pragma unroll
    for (int i = 0; i < 3; ++i) { const unsigned z = 64 * i + l, k = (z * 43691u) >> 19, c = z - 12 * k, m = c >= 6, cc = c - 6 * m; unsigned un = 3 * r + (cc >> 1); un = un > 85 ? 85 : un;
        gp[i] = (m ? wu : wg) + ((16 * w + k) * 2752 + un * 32 + (cc & 1) * 16); asm volatile("" : "+v"(gp[i])); }
    const unsigned wk3 = w * 3072;
#define GU(R, c) dma_glbi<(R) * 24576 + (c) * 1024>(wk3, gp[c] + (R) * 128 * 2752)
#define GUD(R, q) dma_glbi<(R) * 24576 + ((q) / 3) * 12288 + ((q) % 3) * 1024>(wk3, gp[(q) % 3] + ((R) * 128 + ((q) / 3) * 64) * 2752)
    asm volatile("" : "+v"(vw0), "+v"(vw1), "+v"(xw0), "+v"(xw1), "+v"(wa), "+v"(vres), "+v"(vk), "+v"(vv_), "+v"(vrow), "+v"(vst));
    STAMP(39);
    if (XB <= 4) { ld16_imm<512>(xr[4], vx, rsX); ld16_imm<640>(xr[5], vx, rsX); }
    if (XB <= 6) { ld16_imm<768>(xr[6], vx, rsX); ld16_imm<896>(xr[7], vx, rsX); }
    if (CF == 0) TOUCH_A(1, 1);
    if (CF == 1) TOUCH_A(0, 1);
#define XA (CF != 2)
#define NO ((void)0)
#define HOTB(wv, base, n) do { if (w == (wv)) { const char* p_ = base + ((((3u - (unsigned)((u64)base >> 7)) & 7u) * 128) + (rk * (n) + (l < (n) ? l : 0)) * 1024); asm volatile("global_load_dword v254, %0, off" ::"v"(p_) : "memory", "v254"); } } while (0)
#define TBPOS 0
#define TOUCH_B() do { const char* tp = (w == 4 ? wo : w == 5 ? wg : w == 6 ? wu : wd) + (w == 4 ? b * 16 + (l & 15) : b * 43 + (l < 43 ? l : 0)) * 128; \
        asm volatile("global_load_dword v255, %0, off" ::"v"(tp) : "memory", "v255"); } while (0)
#if !defined(NOTB) && TBPOS == 0
    if (w >= 4) TOUCH_B();
#define XW 1
#else
#define XW 0
#endif
#define SSTAMP(i)
    {
        f2 xf[32], s1 = {0.f, 0.f}, s2 = {0.f, 0.f};
#define LW 0
#define TWD() NO
#define LN_SUM(i, N, D0, D1) do { if (XW && (N) < 8 - (i)) WAITV1((N) + XA, xr[i]); else WAITV1D((N) + XA, 7 + XA - (i) + XW - LW, xr[i]); _Pragma("unroll") for (int e = 0; e < 4; ++e) { const unsigned d_ = xr[i][e]; f2 v_ = {blo(d_), bhi(d_)}; asm volatile("" : "+v"(v_)); xf[4 * (i) + e] = v_; \
        s1 += v_; s2 += v_ * v_; } asm volatile("" : "+v"(s1), "+v"(s2)); if (w < 4) { D0; D1; } SBAR(); } while (0)
        LN_SUM(0, 7, NO, NO); LN_SUM(1, 6, NO, NO); LN_SUM(2, 5, NO, NO); LN_SUM(3, 4, NO, NO);
        LN_SUM(4, 3, SP(0, 0), SP(0, 1)); LN_SUM(5, 4, SP(0, 2), SP(0, 3)); LN_SUM(6, 5, SP(0, 4), NO); LN_SUM(7, 5, SP(0, 5), NO);
        SSTAMP(34);
        if (w < 4) { SP(1, 0); SP(1, 1); SP(1, 2); SP(1, 3); SP(1, 4); SP(1, 5); }
#if !defined(NOTB) && TBPOS == 1
        else TOUCH_B();
#endif
        const float t1 = add_dpp<0x141>(add_dpp<0x4e>(add_dpp<0xb1>(s1[0] + s1[1]))), t2 = add_dpp<0x141>(add_dpp<0x4e>(add_dpp<0xb1>(s2[0] + s2[1])));
        const float mu = t1 * (1.f / 512), rs = __builtin_amdgcn_rsqf(__builtin_fmaxf(t2 * (1.f / 512) - mu * mu, 0.f) + 1e-5f), mr = -mu * rs;
#pragma unroll
        for (int i = 0; i < 8; ++i) sts16(((i & 1) ? xw1 : xw0) + (i >> 1) * 256, (u4){pk2(xf[4 * i] * rs + mr), pk2(xf[4 * i + 1] * rs + mr), pk2(xf[4 * i + 2] * rs + mr), pk2(xf[4 * i + 3] * rs + mr)});
    }
#define P1_STEP(n, D0, D1, D2) do { constexpr int c_ = (n) & 1, x_ = c_ ^ 1; constexpr bool m_ = (n) < 15; \
        const unsigned xb = xa[((n) + 1) & 3] + (((n) + 1) >> 2) * 256, wb = wa + SLOT(((n) + 1) >> 1) + x_ * 4096; \
        if (m_) X0[x_] = lds16(xb); SBAR(); \
        aq[0] = MFMA16(Wq[c_], X0[c_], aq[0]); if (m_) Wq[x_] = ldtr(wb, wb + 512); SBAR(); \
        aq[1] = MFMA16(Wq[c_], X1[c_], aq[1]); if (m_) X1[x_] = lds16(xb + 16384); D0; SBAR(); \
        ak[0] = MFMA16(Wk[c_], X0[c_], ak[0]); if (m_) Wk[x_] = ldtr(wb + 8192, wb + 8704); SBAR(); \
        ak[1] = MFMA16(Wk[c_], X1[c_], ak[1]); D1; SBAR(); \
        av[0] = MFMA16(X0[c_], Wv[c_], av[0]); if (m_) Wv[x_] = ldtr(wb + 16384, wb + 16896); SBAR(); \
        av[1] = MFMA16(X1[c_], Wv[c_], av[1]); D2; SBAR(); } while (0)
#define P1_FIRST() do { const unsigned wb = wa + SLOT(0); X0[0] = lds16(xa[0]); Wq[0] = ldtr(wb, wb + 512); X1[0] = lds16(xa[0] + 16384); Wk[0] = ldtr(wb + 8192, wb + 8704); Wv[0] = ldtr(wb + 16384, wb + 16896); } while (0)
#define XRES() ld16_plain(xres, vres, mk_rsrc(x + U64 * 65536u, 65536u), 0)
    if (w < 4) {
        u4 X0[2], X1[2], Wq[2], Wk[2], Wv[2];
        SP(2, 0); SP(2, 1); SP(2, 2); SP(2, 3); SP(2, 4); SP(2, 5);
        BAR_VM(12); STAMP(16);  P1_FIRST(); P1_STEP(0, (SP(3, 0), SP(3, 1)), (SP(3, 2), SP(3, 3)), (SP(3, 4), SP(3, 5)));
        BAR_VM(12); SSTAMP(17); P1_STEP(1, SP(4, 0), SP(4, 1), SP(4, 2)); P1_STEP(2, SP(4, 3), SP(4, 4), SP(4, 5));
        BAR_VM(12); SSTAMP(18); P1_STEP(3, SP(5, 0), SP(5, 1), SP(5, 2)); P1_STEP(4, SP(5, 3), SP(5, 4), SP(5, 5));
        BAR_VM(12); SSTAMP(19); P1_STEP(5, SP(6, 0), SP(6, 1), SP(6, 2)); P1_STEP(6, SP(6, 3), SP(6, 4), SP(6, 5));
        BAR_VM(12); SSTAMP(20); P1_STEP(7, SP(7, 0), SP(7, 1), SP(7, 2)); P1_STEP(8, SP(7, 3), SP(7, 4), SP(7, 5));
        BAR_VM(12); SSTAMP(21); P1_STEP(9, XRES(), GUD(1, 0), NO); P1_STEP(10, GUD(1, 1), GUD(1, 2), NO);
        BAR_VM(10); SSTAMP(22); P1_STEP(11, GUD(1, 3), GUD(1, 4), NO); P1_STEP(12, GUD(1, 5), NO, NO);
        BAR_VM(7);  SSTAMP(23); P1_STEP(13, NO, NO, NO); P1_STEP(14, NO, NO, NO); P1_STEP(15, NO, NO, NO);
        WAITV1(6, xres); tag = (u2){0u, 0u};
    } else {
        u4 X0[2], X1[2], Wq[2], Wk[2], Wv[2];
        const u4 bad = {~0u, ~0u, ~0u, ~0u};
#define MK(v, s_) st16(bad, v, rsF, s_)
#if !defined(NOTB) && TBPOS == 2
#define TCH(c) if (w == (c)) TOUCH_B()
#else
#define TCH(c) NO
#endif
        BAR_L(); STAMP(16);  P1_FIRST(); P1_STEP(0, ld8(tag, 0u * l, rsF, 0), MK(l16, skv), TCH(5));
        BAR_L(); SSTAMP(17); P1_STEP(1, NO, MK(l16 + 1024, skv), TCH(6)); P1_STEP(2, NO, MK(l16, skv - 2 * KVS), NO);
        BAR_L(); SSTAMP(18); P1_STEP(3, NO, MK(l16 + 1024, skv - 2 * KVS), TCH(4)); P1_STEP(4, NO, MK(l16, sOw), NO);
        BAR_L(); SSTAMP(19); P1_STEP(5, NO, MK(l16, sOw - 2048), TCH(7)); P1_STEP(6, XRES(), NO, NO);
        BAR_L(); SSTAMP(20); P1_STEP(7, NO, NO, NO); P1_STEP(8, NO, NO, NO);
        BAR_L(); SSTAMP(21); P1_STEP(9, NO, NO, NO); P1_STEP(10, NO, NO, NO);
        asm volatile("s_waitcnt vmcnt(0)" : "+v"(tag), "+v"(xres) :: "memory");
        BAR_L(); SSTAMP(22);
        { const unsigned lo = tag.x + 1u; tag.y += (lo == 0u); tag.x = lo; }
                             P1_STEP(11, FLAG(w == 4 ? WS_F1 + (T64 * 8 + hq) * 128 : OOB), NO, NO); P1_STEP(12, HOTB(6, wo, 16), NO, NO);
        BAR_L(); SSTAMP(23); P1_STEP(13, NO, NO, NO); P1_STEP(14, NO, NO, NO); P1_STEP(15, NO, NO, NO);
    }
    STAMP(1);
    u4 Qf;
    {
        { auto a = __builtin_amdgcn_permlane16_swap(pk2(lo2(ak[0])), pk2(lo2(ak[1])), false, false); auto c = __builtin_amdgcn_permlane16_swap(pk2(hi2(ak[0])), pk2(hi2(ak[1])), false, false);
          st16((u4){a[0], c[0], a[1], c[1]}, l16, rsF, skv); }
        st16((u4){pk2(lo2(av[0])), pk2(hi2(av[0])), pk2(lo2(av[1])), pk2(hi2(av[1]))}, vst, rsF, skv);
        STAMP(32);
        SBAR();
        constexpr float QS = 0.25f * 1.4426950408889634f;
        { auto a = __builtin_amdgcn_permlane16_swap(pk2(lo2(aq[0]) * QS), pk2(lo2(aq[1]) * QS), false, false); auto c = __builtin_amdgcn_permlane16_swap(pk2(hi2(aq[0]) * QS), pk2(hi2(aq[1]) * QS), false, false);
          Qf = (u4){a[0], c[0], a[1], c[1]}; }
        sts16(L_OC + (w ^ 1) * 2304 + l16, Qf);
    }
    const int Tb = 2 * (int)T64 - 8;
    u4 K[5], V0[5], V1[5];
    const int blk[5] = {Tb + 8 + (int)j, Tb + (int)j, Tb + 2 + 3 * (int)j, Tb + 3 + 3 * (int)j, Tb + 4 + 3 * (int)j};
#pragma unroll
    for (int i = 0; i < 5; ++i) { const unsigned o = blk[i] < 0 ? 0u : 0x3f803f80u; V0[i] = (u4){o, o, o, o}; V1[i] = V0[i]; asm volatile("" : "+v"(V0[i]), "+v"(V1[i])); }
    unsigned pm[8];
    { const short uq = (short)(t32 - 8 * h);
#pragma unroll
      for (int i = 0; i < 8; ++i) { const short c = (short)(16 * ((i >> 1) & 1) + 4 * (i >> 2) + 2 * (i & 1)); pm[i] = __builtin_bit_cast(unsigned, ((sh2){uq, uq} - (sh2){c, (short)(c + 1)}) >> (sh2){15, 15}); asm volatile("" : "+v"(pm[i])); } }
    unsigned so[5];
#pragma unroll
    for (int i = 0; i < 5; ++i) so[i] = blk[i] < 0 ? OOB : kvb + (unsigned)blk[i] * 2048;
    if (w == 7) {
        const int Tp = (int)T64 - 1 - (int)(l & 3); const unsigned vc = WS_F1 + ((Tp < 0 ? T64 : (unsigned)Tp) * 8 + hq) * 128; POLL(vc); }
    STAMP(35);
    BAR_VM(0);
    STAMP(2);
#define KVLD(i) do { ld16(K[i], vk, rsF, so[i]); ld16_v(V0[i], V1[i], vv_, rsF, so[i]); } while (0)
#define KVOK(N, i) do { WAITV3(N, K[i], V0[i], V1[i]); \
        if (UNLIKELY(any_lane(max(max(K[i][3], V0[i][3]), V1[i][3]) == ~0u))) { \
            _Pragma("unroll 1") for (int s_ = 0; s_ < SPIN_CAP; ++s_) { NRETRY(); KVLD(i); WAITV3(0, K[i], V0[i], V1[i]); if (!any_lane(max(max(K[i][3], V0[i][3]), V1[i][3]) == ~0u)) break; __builtin_amdgcn_s_sleep(2); } } } while (0)
    if (SL1) __builtin_amdgcn_s_sleep(SL1);
    KVLD(2); KVLD(3);
    const u4 Qm = Qf, Qo = lds16(L_OC + w * 2304 + l16);
    f16 Om = {}, Oo = {};
    const f16 z16 = {};
    auto pv_tile = [&](const f16& S, const u4& va, const u4& vb, f16& O) {
        float p[16];
#pragma unroll
        for (int e = 0; e < 16; ++e) p[e] = __builtin_amdgcn_exp2f(S[e]);
        const u4 P0 = {pk(p[0], p[1]), pk(p[2], p[3]), pk(p[4], p[5]), pk(p[6], p[7])}, P1 = {pk(p[8], p[9]), pk(p[10], p[11]), pk(p[12], p[13]), pk(p[14], p[15])};
        O = MFMA32(va, P0, O); O = MFMA32(vb, P1, O);
    };
    const u4 rsWo = mk_rsrc(wo + fb * 128u, 512u * 1024u - fb * 128u);
    const unsigned wol = L_A + w * 8192, wos = w * 65536;
    const unsigned vh = (u8 >> 1) * 1024 + ((u8 & 1) * 32 + 8 * hh + l7) * 16, vs = l < 8 ? (8 * hh + l) * 16 : OOB;
    const unsigned sHw = WS_H + (2 * U64 + j) * OS + fb * 4096, sSw = WS_ST + (2 * U64 + j) * 4096u + fb * 512;
    const unsigned sGa = r < 29 ? WS_G + (8 * g + w) * 90112u + r * 2048 : OOB, sGb = r < 28 ? WS_G + (8 * g + w) * 90112u + (60 + r) * 1024 : OOB;
#define WO_(i) dma_bufi<(i) * 1024, (i) * 8192>(wol, vrow, rsWo, wos)
#define PIN16(a) asm volatile("" : "+v"(a))
#define MEM(...) do { SBAR(); __VA_ARGS__; SBAR(); } while (0)
#define HALF(S, o, va, O) do { float p_[8]; _Pragma("unroll") for (int e = 0; e < 8; ++e) p_[e] = __builtin_amdgcn_exp2f(S[(o) + e]); \
        u4 q_ = {pk(p_[0], p_[1]), pk(p_[2], p_[3]), pk(p_[4], p_[5]), pk(p_[6], p_[7])}; asm volatile("" : "+v"(q_)); O = MFMA32(va, q_, O); PIN16(O); SBAR(); } while (0)
#define SCORE(S, k, q) do { S = MFMA32(k, q, z16); PIN16(S); SBAR(); } while (0)
    f16 Sa, Sb, Sc;
    u2 ck; const unsigned vc1 = WS_F1 + (U64 * 8 + l7) * 128;
    const u4 bad = {~0u, ~0u, ~0u, ~0u};
    KVOK(3, 2);
    STAMP(24);
    SCORE(Sa, K[2], Qm); SCORE(Sb, K[2], Qo); MEM(KVLD(4));
    HALF(Sa, 0, V0[2], Om); MEM(KVLD(0)); HALF(Sa, 8, V1[2], Om); MEM(KVLD(1)); KVOK(9, 3); TWD();
    SCORE(Sa, K[3], Qm); HALF(Sb, 0, V0[2], Oo); MEM(WO_(0)); HALF(Sb, 8, V1[2], Oo); MEM(WO_(1));
    SSTAMP(42);
    SCORE(Sb, K[3], Qo); HALF(Sa, 0, V0[3], Om); MEM(WO_(2)); HALF(Sa, 8, V1[3], Om); MEM(WO_(3)); KVOK(10, 4);
    SCORE(Sa, K[4], Qm); HALF(Sb, 0, V0[3], Oo); MEM(WO_(4)); HALF(Sb, 8, V1[3], Oo); MEM(WO_(5));
    SSTAMP(43);
    SCORE(Sb, K[4], Qo); HALF(Sa, 0, V0[4], Om); MEM(WO_(6)); HALF(Sa, 8, V1[4], Om); MEM(WO_(7)); KVOK(11, 0); KVOK(8, 1);
    if (j == 0) { SCORE(Sa, K[0], Qo); HALF(Sb, 0, V0[4], Oo); MEM(st16(bad, vh, rsF, sHw)); HALF(Sb, 8, V1[4], Oo); MEM(st16(bad, vs, rsF, sSw), st16(bad, l16, rsF, sGb));
                  SCORE(Sb, K[0], Qm); SCORE(Sc, K[1], Qm); HALF(Sa, 0, V0[0], Oo); HALF(Sa, 8, V1[0], Oo); }
    else        { SCORE(Sa, K[1], Qo); HALF(Sb, 0, V0[4], Oo); MEM(st16(bad, vh, rsF, sHw)); HALF(Sb, 8, V1[4], Oo); MEM(st16(bad, vs, rsF, sSw), st16(bad, l16, rsF, sGb));
                  SCORE(Sb, K[0], Qm); SCORE(Sc, K[1], Qm); HALF(Sa, 0, V0[1], Oo); HALF(Sa, 8, V1[1], Oo); }
    SSTAMP(44);
    MEM(ld8(ck, w == 7 ? vc1 : OOB, rsF, 0), GU(2, 0), GU(2, 1), GU(2, 2));
    const unsigned oc = L_OC + w * 2304;
    {
        const f16 Sx = Sb, Sy = Sc;
        float p[16];
#pragma unroll
        for (int e = 0; e < 8; ++e) p[e] = __builtin_amdgcn_exp2f(sel(diag_mask(e), Sx[e], Sy[e]));
        SBAR();
        sts16f(oc + l16, (f4){Oo[0], Oo[1], Oo[2], Oo[3]}); sts16f(oc + 1024 + l16, (f4){Oo[4], Oo[5], Oo[6], Oo[7]}); sts4f(oc + 2048 + l * 4, Oo[8]);
#pragma unroll
        for (int e = 8; e < 16; ++e) p[e] = __builtin_amdgcn_exp2f(sel(diag_mask(e), Sx[e], Sy[e]));
        unsigned P[8];
#pragma unroll
        for (int e = 0; e < 8; ++e) P[e] = pk(p[2 * e], p[2 * e + 1]);
        const u4 Y0 = {P[0] & pm[0], P[1] & pm[1], P[2] & pm[2], P[3] & pm[3]}, Y1 = {P[4] & pm[4], P[5] & pm[5], P[6] & pm[6], P[7] & pm[7]};
        const u4 X0_ = {P[0] ^ Y0[0], P[1] ^ Y0[1], P[2] ^ Y0[2], P[3] ^ Y0[3]}, X1_ = {P[4] ^ Y1[0], P[5] ^ Y1[1], P[6] ^ Y1[2], P[7] ^ Y1[3]};
        Om = MFMA32(V0[0], X0_, Om); Om = MFMA32(V1[0], X1_, Om); Om = MFMA32(V0[1], Y0, Om); Om = MFMA32(V1[1], Y1, Om);
    }
    SBAR();
    STAMP(25);
    asm volatile("s_waitcnt vmcnt(3) lgkmcnt(0)\n\ts_barrier" : "+v"(ck) :: "memory");
    SSTAMP(45);
    const unsigned op = L_OC + (w ^ 1) * 2304;
    f4 oa = lds16f(op + l16) + (f4){Om[0], Om[1], Om[2], Om[3]}, ob = lds16f(op + 1024 + l16) + (f4){Om[4], Om[5], Om[6], Om[7]};
    float den = Om[8] + lds4f(op + 2048 + l * 4);
    if (UNLIKELY(any_lane(!(den >= 0x1p-60f && den <= 0x1p60f)))) {
        f16 O = {}; float m = -__builtin_inff();
#pragma unroll 1
        for (int kb = 8; kb >= 0; --kb) {
            const int T = 2 * (int)T64 + (int)j - 8 + kb;
            if (T < 0) break;
            u4 Ks, Va = ones, Vb = ones;
            ld16(Ks, vk, rsF, kvb + (unsigned)T * 2048); ld16_v(Va, Vb, vv_, rsF, kvb + (unsigned)T * 2048);
            WAITV3(0, Ks, Va, Vb);
            f16 S = MFMA32(Ks, Qm, z16);
            const u64 all = (kb != 0 && kb != 8) ? ~0ull : 0ull, inv = kb == 0 ? ~0ull : 0ull;
            float mx = m;
#pragma unroll
            for (int e = 0; e < 16; ++e) { S[e] = sel((diag_mask(e) ^ inv) | all, S[e], -__builtin_inff()); mx = __builtin_fmaxf(mx, S[e]); }
            mx = max_x32(mx);
            const float al = __builtin_amdgcn_exp2f(m - mx); m = mx;
#pragma unroll
            for (int e = 0; e < 16; ++e) { O[e] *= al; S[e] -= mx; }
            pv_tile(S, Va, Vb, O);
        }
        oa = (f4){O[0], O[1], O[2], O[3]}; ob = (f4){O[4], O[5], O[6], O[7]}; den = O[8];
    }
    const float rd = __builtin_amdgcn_rcpf(den);
    st16((u4){pk2(lo2(oa) * rd), pk2(hi2(oa) * rd), pk2(lo2(ob) * rd), pk2(hi2(ob) * rd)}, l16, rsF, sOw);
#define F23() FLAG(w == 4 ? WS_F2 + (U64 * 8 + fb) * 128 : w == 5 ? WS_F3 + (g * 32 + r) * 128 : OOB)
    F23();
    STAMP(26);
    const unsigned ta0 = (4 * h + q4) * 128 + 64 * (q4 >> 1) + 32 * (g4 & 1) + 8 * pp, ta1 = ta0 ^ 64;
    f16 acc0 = {}, acc1 = {};
    {
        const unsigned sO = WS_O + (2 * U64 + j) * OS + hh * 8192;
        const unsigned oa0 = L_A + hh * 16384 + ta0, oa1 = L_A + hh * 16384 + ta1;
        u4 A0[8], A1[8], B[8];
#pragma unroll
        for (int i = 0; i < 8; ++i) { A0[i] = ldtr(oa0 + i * 2048, oa0 + i * 2048 + 1024); A1[i] = ldtr(oa1 + i * 2048, oa1 + i * 2048 + 1024); }
        if (w == 7 && UNLIKELY(any_lane(ck[0] != tag[0] || ck[1] != tag[1]))) POLL(vc1);
        STAMP(37);
        BAR_VM(1);
        STAMP(5);
        if (SL2) __builtin_amdgcn_s_sleep(SL2);
#pragma unroll
        for (int i = 0; i < 8; ++i) ld16(B[i], l16, rsF, sO + i * 1024);
#define P3_PAIR(i, NW, M0, M1) do { asm volatile("s_waitcnt vmcnt(%2)" : "+v"(B[i]), "+v"(B[(i) + 1]) : "i"(NW) : "memory"); \
            if (UNLIKELY(any_lane(max(B[i][3], B[(i) + 1][3]) == ~0u))) { \
                _Pragma("unroll 1") for (int s_ = 0; s_ < SPIN_CAP; ++s_) { NRETRY(); __builtin_amdgcn_s_sleep(3); asm volatile("s_waitcnt vmcnt(0)" ::: "memory"); unsigned m_ = 0; \
                    _Pragma("unroll") for (int q = (i); q < 8; ++q) ld16(B[q], l16, rsF, sO + q * 1024); \
                    _Pragma("unroll") for (int q = (i); q < 8; ++q) { WAITV1(7 - q, B[q]); m_ = max(m_, B[q][3]); } \
                    if (!any_lane(m_ == ~0u)) break; } } if ((i) == 0) STAMP(4); \
            acc0 = MFMA32(A0[i], B[i], acc0); acc1 = MFMA32(A1[i], B[i], acc1); M0; SBAR(); acc0 = MFMA32(A0[(i) + 1], B[(i) + 1], acc0); acc1 = MFMA32(A1[(i) + 1], B[(i) + 1], acc1); M1; SBAR(); } while (0)
        P3_PAIR(0, 6, NO, NO); P3_PAIR(2, 4, GU(0, 0), GU(0, 1)); P3_PAIR(4, 4, GU(0, 2), GU(3, 0)); P3_PAIR(6, 4, GU(3, 1), GU(3, 2));
    }
    const unsigned vc2 = WS_F2 + (g * 32 + t32) * 128;
    ld8(ck, w == 7 ? vc2 : OOB, rsF, 0);
    STAMP(27);
    const unsigned rw0 = L_A + (hh * 2 + j) * 8192 + h * 512 + t32 * 16, rw1 = rw0 ^ 128, rr = L_A + j * 8192 + u8 * 1024 + (((8 * hh + l7) ^ ((u8 & 1) << 3)) * 16);
    auto reduce = [&](f4& lo, f4& hi) {
#pragma unroll
        for (int u = 0; u < 4; ++u) {
            sts16f(((u & 1) ? rw1 : rw0) + u * 1024, (f4){acc0[4 * u], acc0[4 * u + 1], acc0[4 * u + 2], acc0[4 * u + 3]});
            sts16f(((u & 1) ? rw1 : rw0) + (4 + u) * 1024, (f4){acc1[4 * u], acc1[4 * u + 1], acc1[4 * u + 2], acc1[4 * u + 3]});
        }
        BAR_L();
        lo = (lds16f(rr) + lds16f(rr + 16384)) + (lds16f(rr + 32768) + lds16f(rr + 49152));
        hi = (lds16f(rr + 512) + lds16f(rr + 16896)) + (lds16f(rr + 33280) + lds16f(rr + 49664));
    };
    u4 Hres;
    const unsigned ga = (8 * h + q4) * 192 + (g4 & 1) * 96 + 8 * pp;
    f16 cs[3] = {};
    {
        f4 lo, hi; reduce(lo, hi);
        STAMP(28);
        Hres = (u4){pk2(lo2(lo) + (f2){blo(xres[0]), bhi(xres[0])}), pk2(hi2(lo) + (f2){blo(xres[1]), bhi(xres[1])}), pk2(lo2(hi) + (f2){blo(xres[2]), bhi(xres[2])}), pk2(hi2(hi) + (f2){blo(xres[3]), bhi(xres[3])})};
        st16(Hres, vh, rsF, sHw);
        f2 s1 = {0.f, 0.f}, s2 = {0.f, 0.f};
#pragma unroll
        for (int e = 0; e < 4; ++e) { const f2 a = {blo(Hres[e]), bhi(Hres[e])}; s1 += a; s2 += a * a; }
        const float t1 = add_x32(add_x16(add_x8(s1[0] + s1[1]))), t2 = add_x32(add_x16(add_x8(s2[0] + s2[1])));
        st16((u4){uf(t1), uf(t2), uf(t1), uf(t2)}, vs, rsF, sSw);
        STAMP(33);
        asm volatile("s_waitcnt vmcnt(2) lgkmcnt(0)\n\ts_barrier" : "+v"(ck) :: "memory");
        STAMP(40);
        const unsigned o = ga + w * 12288;
        u4 a[12];
#pragma unroll
        for (int i = 0; i < 12; ++i) a[i] = ldtr(o + (i / 3) * 3072 + (i % 3) * 32, o + (i / 3) * 3072 + (i % 3) * 32 + 768);
        SBAR();
#pragma unroll
        for (int i = 0; i < 12; ++i) cs[i % 3] = MFMA32(ones, a[i], cs[i % 3]);
        sts16f(L_CSP + w * 512 + t32 * 16, (f4){cs[0][0], cs[1][0], cs[2][0], 0.f});
    }
    STAMP(36);
    const u4 rsWd = mk_rsrc(wd + fb * 128u, 1376u * 1024u - fb * 128u);
    if (w == 7 && UNLIKELY(any_lane(ck[0] != tag[0] || ck[1] != tag[1]))) POLL(vc2);
    if (w == 5 && b == 255) {
#pragma unroll 1
        for (int s_ = 0; s_ < 4 * SPIN_CAP; ++s_) { u2 f0, f1, f2_, f3; const unsigned vc = WS_F1 + l * 128;
            ld8(f0, vc, rsF, 0); ld8(f1, vc, rsF, 8192); ld8(f2_, vc, rsF, 16384); ld8(f3, vc, rsF, 24576); WAITV4(0, f0, f1, f2_, f3);
            if (!any_lane(((f0[0] ^ tag[0]) | (f1[0] ^ tag[0]) | (f2_[0] ^ tag[0]) | (f3[0] ^ tag[0]) | (f0[1] ^ tag[1]) | (f1[1] ^ tag[1]) | (f2_[1] ^ tag[1]) | (f3[1] ^ tag[1])) != 0u)) break; }
        FLAG(0u);
    }
    BAR_VM(0);
    STAMP(6);
    if (SL3) __builtin_amdgcn_s_sleep(SL3);
    const unsigned a0 = 15 * hh, a1 = a0 + 1, jo = j * 8192;
    const unsigned wde = (48 * (a0 >> 1) + 16 * (a0 & 1)) * 1024 + jo, wdo = (48 * (a1 >> 1) + 16 * (a1 & 1)) * 1024 + jo, wdt = (336 * hh + 32) * 1024 + jo;
    const unsigned wla = L_A + hh * 14336 + j * 1024, wlb = hh * 12288 + j * 1024, wlc = 49152 + hh * 4096 + j * 1024;
#define WD(i) dma_bufi<((i) < 14 ? (i) % 7 : (i) < 20 ? (i) - 14 : (i) - 20) * 2048u, ((i) < 15 ? (i) >> 1 : (i) - 15) * 49152u>((i) < 14 ? wla : (i) < 20 ? wlb : wlc, vrow, rsWd, (i) >= 15 ? wdt : ((i) & 1) ? wdo : wde)
    u4 D0[7], D1[7];
    {
        const unsigned sH = WS_H + (8 * g + w) * OS, sS = WS_ST + (8 * g + w) * 4096u;
#define PD4 6
        constexpr int PD = PD4;
        u4 Hb[32], A0[3], A1[3], A2[4];
        u2 st[4];
        f16 ag[3] = {};
        const unsigned cf = L_CF + w * 384, vstat = h * 2048 + t32 * 16;
#pragma unroll
        for (int i = 0; i < PD; ++i) ld16(Hb[kgo(i)], l16, rsF, sH + kgo(i) * 1024);
        {
            const unsigned o = L_CSP + h * 2048 + t32 * 16; const f4 s = (lds16f(o) + lds16f(o + 512)) + (lds16f(o + 1024) + lds16f(o + 1536));
            const float c0 = add_x32(s[0]), c1 = add_x32(s[1]), c2 = add_x32(s[2]);
            sts4f(cf + l * 4, h ? c1 : c0); sts4f(cf + 256 + t32 * 4, c2); }
        A0[0] = ldtr(ga, ga + 768); A1[0] = ldtr(ga + 32, ga + 800); A0[1] = ldtr(ga + kgo(1) * 3072, ga + kgo(1) * 3072 + 768); A1[1] = ldtr(ga + kgo(1) * 3072 + 32, ga + kgo(1) * 3072 + 800);
        unsigned mx = 0;
#define HDS 10
#define P4_STEP(i) do { constexpr int c = (i) % 3, n = ((i) + 2) % 3, k = kgo(i); const unsigned o = ga + ((i) < 30 ? kgo((i) + 2) * 3072 : 0); \
            if ((i) + PD < 32) ld16(Hb[kgo((i) + PD < 32 ? (i) + PD : 0)], l16, rsF, sH + kgo((i) + PD) * 1024); \
            else if ((i) + PD < 36) ld8(st[((i) + PD - 32) & 3], vstat, rsF, sS + ((i) + PD - 32) * 512); \
            WAITV1((i) + PD < 36 ? PD : 35 - (i), Hb[k]); if ((i) == HDS) HOTB(3, wd, 43); \
            if ((i) < 8) FRESH16(Hb[k], l16, rsF, sH + k * 1024); else { mx = max(mx, Hb[k][3]); asm volatile("" : "+v"(mx)); } if ((i) == 0) STAMP(7); SBAR(); \
            ag[0] = MFMA32(A0[c], Hb[k], ag[0]); if ((i) < 30) A0[n] = ldtr(o, o + 768); else A2[((i) - 30) & 1] = ldtr(ga + 64 + (((i) - 30) & 1) * 3072, ga + 832 + (((i) - 30) & 1) * 3072); SBAR(); \
            ag[1] = MFMA32(A1[c], Hb[k], ag[1]); if ((i) < 30) A1[n] = ldtr(o + 32, o + 800); else if ((i) == 31) A2[2] = ldtr(ga + 64 + 6144, ga + 832 + 6144); SBAR(); } while (0)
#define P4_4(i) P4_STEP(i); P4_STEP((i) + 1); P4_STEP((i) + 2); P4_STEP((i) + 3)
        P4_4(0); P4_4(4); P4_4(8); P4_4(12); P4_4(16); P4_4(20); P4_4(24); P4_4(28);
        WAITV4(0, st[0], st[1], st[2], st[3]);
        if (UNLIKELY(any_lane(max(max(mx, max(st[0][1], st[1][1])), max(st[2][1], st[3][1])) == ~0u))) {
#pragma unroll
            for (int i = 0; i < 32; ++i) FRESH16(Hb[i], l16, rsF, sH + i * 1024);
#pragma unroll 1
            for (int s_ = 0; s_ < SPIN_CAP; ++s_) { NRETRY();
#pragma unroll
                for (int i = 0; i < 4; ++i) ld8(st[i], vstat, rsF, sS + i * 512);
                WAITV4(0, st[0], st[1], st[2], st[3]);
                if (!any_lane(max(max(st[0][1], st[1][1]), max(st[2][1], st[3][1])) == ~0u)) break;
            }
            ag[0] = z16; ag[1] = z16;
#pragma unroll
            for (int i = 0; i < 32; ++i) { const unsigned o = ga + kgo(i) * 3072; ag[0] = MFMA32(ldtr(o, o + 768), Hb[kgo(i)], ag[0]); ag[1] = MFMA32(ldtr(o + 32, o + 800), Hb[kgo(i)], ag[1]); }
        }
        STAMP(8);
        const float t1 = add_x32(fu(st[0].x) + fu(st[1].x) + fu(st[2].x) + fu(st[3].x)), t2 = add_x32(fu(st[0].y) + fu(st[1].y) + fu(st[2].y) + fu(st[3].y));
        const float nm = t1 * (-1.f / 512), ve = __builtin_fmaxf(t2 * (1.f / 512) - nm * nm, 0.f) + 1e-5f, rs = __builtin_amdgcn_rsqf(ve);
        const float ka_ = rs * -2.3022081986f, kb_ = rs * rs * rs * -0.1029432397f;
#define G_UNIT(u, so_, M0, M1) do { u4 y; _Pragma("unroll") for (int qd = 0; qd < 2; ++qd) { \
                const f4 cg = lds16f(cf + (u) * 128 + qd * 32 + h * 16), cu = lds16f(cf + (u) * 128 + (2 + qd) * 32 + h * 16); \
                _Pragma("unroll") for (int e = 0; e < 2; ++e) { \
                    const f2 gt = (f2){ag[u][4 * qd + 2 * e], ag[u][4 * qd + 2 * e + 1]} + (e ? hi2(cg) : lo2(cg)) * nm, up = (f2){ag[u][8 + 4 * qd + 2 * e], ag[u][9 + 4 * qd + 2 * e]} + (e ? hi2(cu) : lo2(cu)) * nm; \
                    const f2 z = gt * (gt * gt * kb_ + ka_), d = (f2){__builtin_amdgcn_exp2f(z[0]), __builtin_amdgcn_exp2f(z[1])} * ve + ve; \
                    unsigned y_ = pk2(gt * up * (f2){__builtin_amdgcn_rcpf(d[0]), __builtin_amdgcn_rcpf(d[1])}); asm volatile("" : "+v"(y_)); y[2 * qd + e] = y_; } \
                if (qd == 0) MEM(M0); } \
            MEM(st16(y, l16, rsF, so_); M1); } while (0)
        MEM(WD(0), WD(1), WD(2));
        G_UNIT(0, sGa, (WD(3), WD(4), WD(5), WD(6)), NO); G_UNIT(1, sGa + 1024, NO, NO);
        STAMP(47);
        const unsigned da0 = L_A + hh * 14336 + ta0, da1 = L_A + hh * 14336 + ta1;
        const unsigned vc3 = WS_F3 + ((U64 >> 2) * 32 + min(t32, 28u)) * 128, vc4 = vc3 + (WS_F4 - WS_F3);
#define B1 6
#define F4D 0
#define CKS (B1 + 21)
#define WDS(i, m) if ((i) == B1 + 8 + (B1 + 21 <= 31 ? (m) : (m) * (24 - B1) / 13)) WD(7 + (m))
#define P4B_STEP(i) do { constexpr int c = (i) & 3, n = ((i) + 3) & 3; const unsigned o = ga + ((i) + 3) * 3072 + 64; \
            if ((i) == B1) SSTAMP(48); if ((i) == B1 + 1) SSTAMP(49); if ((i) == B1 + 8) SSTAMP(50); if ((i) == B1 + 9) SSTAMP(51); if ((i) == B1 + 15) SSTAMP(52); if ((i) == B1 + 21) SSTAMP(53); \
            if ((i) == B1) BAR_VM(2);         \
            if ((i) == B1 + 8) BAR_VM(0);        \
            if ((i) == B1 + 8 + F4D) FLAG(w == 4 ? WS_F4 + (g * 32 + r) * 128 : OOB); SBAR(); \
            ag[2] = MFMA32(A2[c], Hb[i], ag[2]); if ((i) < 29) A2[n] = ldtr(o, o + 768); \
            if ((i) >= B1 && (i) < B1 + 7) { constexpr int q = ((i) + 14 - B1) % 7; D0[q] = ldtr(da0 + q * 2048, da0 + q * 2048 + 1024); D1[q] = ldtr(da1 + q * 2048, da1 + q * 2048 + 1024); } SBAR(); \
            WDS(i, 0); WDS(i, 1); WDS(i, 2); WDS(i, 3); WDS(i, 4); WDS(i, 5); WDS(i, 6); WDS(i, 7); WDS(i, 8); WDS(i, 9); WDS(i, 10); WDS(i, 11); WDS(i, 12); \
            if ((i) == CKS) ld8(ck, w == 7 ? vc3 : w == 6 ? vc4 : OOB, rsF, 0); SBAR(); } while (0)
#define P4B_4(i) P4B_STEP(i); P4B_STEP((i) + 1); P4B_STEP((i) + 2); P4B_STEP((i) + 3)
        P4B_4(0); P4B_4(4); P4B_4(8); P4B_4(12); P4B_4(16); P4B_4(20); P4B_4(24); P4B_4(28);
        STAMP(41);
        G_UNIT(2, sGb, NO, NO);
        STAMP(30);
        WAITV1(1, ck);
        if (w >= 6 && UNLIKELY(any_lane(ck[0] != tag[0] || ck[1] != tag[1]))) { const unsigned vc = w == 7 ? vc3 : vc4; POLL(vc); }
    }
    BAR_L();
    STAMP(9);
    {
        constexpr int PD = 6;
        u4 Gb[22], A0[2], A1[2];
        const unsigned gs = WS_G + (2 * U64 + j) * 90112u + 15 * hh * 1024, gz = hh == 3 ? OOB : gs, gt = WS_G + (2 * U64 + j) * 90112u + (60 + 7 * hh - 15) * 1024;
        const unsigned o0 = L_A + hh * 14336 + ta0, o1 = L_A + hh * 14336 + ta1, p0 = hh * 12288 + ta0, p1 = hh * 12288 + ta1, q0 = 49152 + hh * 4096 + ta0, q1 = 49152 + hh * 4096 + ta1;
#define GOFF(i) (((i) >= 15 ? gt : (i) >= 13 ? gz : gs) + (i) * 1024)
#pragma unroll
        for (int i = 0; i < PD; ++i) ld16(Gb[i], l16, rsF, GOFF(i));
        WD(20); WD(21);
        acc0 = z16; acc1 = z16;
#define P5_STEP(i) do { constexpr int c = (i) & 1, n = c ^ 1; \
            if ((i) + PD < 22) ld16(Gb[(i) + PD < 22 ? (i) + PD : 0], l16, rsF, GOFF((i) + PD)); \
            WAITV1(((i) + PD < 22 ? PD : 21 - (i)) + ((i) < PD ? 2 : 0), Gb[i]); if ((i) >= 15) FRESH16(Gb[i], l16, rsF, GOFF(i)); if ((i) == 0) STAMP(10); \
            if ((i) == 14) BAR_L(); SBAR();        \
            const unsigned a0_ = (i) < 13 ? o0 + ((i) - 6) * 2048 : (i) < 19 ? p0 + ((i) - 13) * 2048 : q0 + ((i) - 19) * 2048, a1_ = (i) < 13 ? o1 + ((i) - 6) * 2048 : (i) < 19 ? p1 + ((i) - 13) * 2048 : q1 + ((i) - 19) * 2048; \
            acc0 = MFMA32((i) < 7 ? D0[(i) < 7 ? (i) : 0] : A0[c], Gb[i], acc0); if ((i) >= 6 && (i) < 21) A0[n] = ldtr(a0_, a0_ + 1024); SBAR(); \
            acc1 = MFMA32((i) < 7 ? D1[(i) < 7 ? (i) : 0] : A1[c], Gb[i], acc1); if ((i) >= 6 && (i) < 21) A1[n] = ldtr(a1_, a1_ + 1024); SBAR(); } while (0)
        P5_STEP(0); P5_STEP(1); P5_STEP(2); P5_STEP(3); P5_STEP(4); P5_STEP(5); P5_STEP(6); P5_STEP(7); P5_STEP(8); P5_STEP(9); P5_STEP(10);
        P5_STEP(11); P5_STEP(12); P5_STEP(13); P5_STEP(14); P5_STEP(15); P5_STEP(16); P5_STEP(17); P5_STEP(18); P5_STEP(19); P5_STEP(20); P5_STEP(21);
    }
    STAMP(11);
    {
        f4 lo, hi; reduce(lo, hi);
        const u4 Ores = {pk2(lo2(lo) + (f2){blo(Hres[0]), bhi(Hres[0])}), pk2(hi2(lo) + (f2){blo(Hres[1]), bhi(Hres[1])}), pk2(lo2(hi) + (f2){blo(Hres[2]), bhi(Hres[2])}), pk2(hi2(hi) + (f2){blo(Hres[3]), bhi(Hres[3])})};
        st16_plain(Ores, vres, mk_rsrc(out + U64 * 65536u, 65536u), 0);
    }
    asm volatile(".Lfb_code1:");
    STAMP(12);
#endif
}

#define WSOFF 0
extern "C" void kernel_launch(void* const* d_in, const int* in_sizes, int n_in, void* d_out, int out_size, void* d_ws, size_t ws_size, hipStream_t stream) {
    (void)in_sizes; (void)n_in; (void)out_size; (void)ws_size;
    (void)hipFuncSetAttribute((const void*)fused_block, hipFuncAttributeMaxDynamicSharedMemorySize, LDS_BYTES);
    fused_block<<<256, 512, LDS_BYTES, stream>>>((const char*)d_in[0], (const char*)d_in[1], (const char*)d_in[2], (const char*)d_in[4], (const char*)d_in[5], (const char*)d_in[6],
                                                 (char*)d_out, (char*)d_ws + WSOFF);
}
#define LASTSTAMP 12
#define STAMP_NAMES "start", "P1 loop end", "KV drained", 0, "O first data", "O drained", "h drained", "h first data", "P4 pass1 end", "P4 end bar", "G first data", "P5 loop end", "end", 0, 0, 0, \
  "st0", "st1", "st2", "st3", "st4", "st5", "st6", "st7", "KV first data", "att done", "O stored", "P3 loop end", "P3 reduced", 0, "G stored", 0, "KV stored", "h stored", \
  "LN sums", "pre KV bar", "colsum done", "A read", 0, "setup done", "GU bar", "P4 pass2 end", "att blk3", "att blk4", "att blk0", "comb bar", 0, "G01 stored", "p2 pre B1", "p2 s7", "p2 pre B2", "p2 s15", "p2 s21", "p2 s27", "h landed", "st landed"
```

```cpp
#include <hip/hip_runtime.h>
#include <hip/hip_bf16.h>
#include <stdint.h>
#pragma clang diagnostic ignored "-Wint-to-pointer-cast"

typedef __bf16 bf8 __attribute__((ext_vector_type(8)));
typedef __bf16 bf2 __attribute__((ext_vector_type(2)));
typedef short s4 __attribute__((ext_vector_type(4)));
typedef short s8 __attribute__((ext_vector_type(8)));
typedef float f2 __attribute__((ext_vector_type(2)));
typedef float f4 __attribute__((ext_vector_type(4)));
typedef float f16 __attribute__((ext_vector_type(16)));
typedef unsigned u2 __attribute__((ext_vector_type(2)));
typedef unsigned u4 __attribute__((ext_vector_type(4)));
typedef unsigned long long u64;
#define LAS __attribute__((address_space(3)))
#define DEV __device__ __forceinline__

constexpr unsigned WS_F1 = 32768, WS_F2 = 65536, WS_F3 = 98304, WS_F4 = 131072;
#define KVS 132096u
#define OS 33792u
constexpr unsigned WS_KV = 1u << 20;
constexpr unsigned WS_O = WS_KV + 32 * KVS;
constexpr unsigned WS_H = WS_O + 64 * OS;
constexpr unsigned WS_ST = WS_H + 64 * OS;
constexpr unsigned WS_G = WS_ST + 64 * 4096;
static_assert(WS_G + 64 * 90112u <= (16u << 20), "hand-off buffers");
constexpr unsigned WS_DIAG = 32u << 20;
constexpr unsigned LDS_BYTES = 163840;
constexpr unsigned L_HOLE = 73728;
constexpr unsigned L_OC = L_HOLE;
constexpr unsigned L_A = 98304;
constexpr unsigned L_CSP = 155648, L_CF = L_CSP + 4096;
constexpr int SPIN_CAP = 1 << 14;
constexpr unsigned OOB = 0x40000000u;

#define MFMA16(a, b, c) __builtin_amdgcn_mfma_f32_16x16x32_bf16(__builtin_bit_cast(bf8, a), __builtin_bit_cast(bf8, b), c, 0, 0, 0)
#define MFMA32(a, b, c) __builtin_amdgcn_mfma_f32_32x32x16_bf16(__builtin_bit_cast(bf8, a), __builtin_bit_cast(bf8, b), c, 0, 0, 0)
#define SBAR() __builtin_amdgcn_sched_barrier(0)
#define BAR_VM(N) asm volatile("s_waitcnt vmcnt(%0) lgkmcnt(0)\n\ts_barrier" ::"i"(N) : "memory")
#define BAR_L() asm volatile("s_waitcnt lgkmcnt(0)\n\ts_barrier" ::: "memory")

DEV u4 mk_rsrc(const void* p, unsigned bytes) { const u64 a = (u64)p; return (u4){(unsigned)a, (unsigned)(a >> 32) & 0xffffu, bytes, 0x00020000u}; }
DEV void dma_buf(unsigned lds, unsigned voff, u4 rs, unsigned soff) {
    asm volatile("s_mov_b32 m0, %0\n\ts_nop 0\n\tbuffer_load_dwordx4 %1, %2, %3 offen lds" ::"s"(lds), "v"(voff), "s"(rs), "s"(soff) : "memory"); }
template <unsigned LI, unsigned SI> DEV void dma_bufi(unsigned lds, unsigned voff, u4 rs, unsigned soff) {
    unsigned t; asm volatile("s_add_u32 m0, %1, %5\n\ts_add_u32 %0, %4, %6\n\tbuffer_load_dwordx4 %2, %3, %0 offen lds" : "=&s"(t) : "s"(lds), "v"(voff), "s"(rs), "s"(soff), "i"(LI), "i"(SI) : "memory", "scc"); }
template <unsigned LI> DEV void dma_glbi(unsigned lds, const char* p) {
    asm volatile("s_add_u32 m0, %0, %2\n\ts_nop 0\n\tglobal_load_lds_dwordx4 %1, off" ::"s"(lds), "v"(p), "i"(LI) : "memory", "scc"); }
DEV void dma_glb(unsigned lds, const char* p) {
    asm volatile("s_mov_b32 m0, %0\n\ts_nop 0\n\tglobal_load_lds_dwordx4 %1, off" ::"s"(lds), "v"(p) : "memory"); }
DEV void ld16(u4& d, unsigned voff, u4 rs, unsigned soff) {
    asm volatile("buffer_load_dwordx4 %0, %1, %2, %3 offen sc1" : "=v"(d) : "v"(voff), "s"(rs), "s"(soff) : "memory"); }
DEV void ld16_plain(u4& d, unsigned voff, u4 rs, unsigned soff) {
    asm volatile("buffer_load_dwordx4 %0, %1, %2, %3 offen" : "=v"(d) : "v"(voff), "s"(rs), "s"(soff) : "memory"); }
template <int OFF> DEV void ld16_imm(u4& d, unsigned voff, u4 rs) {
    asm volatile("buffer_load_dwordx4 %0, %1, %2, 0 offen offset:%3" : "=v"(d) : "v"(voff), "s"(rs), "i"(OFF) : "memory"); }
DEV void ld8(u2& d, unsigned voff, u4 rs, unsigned soff) {
    asm volatile("buffer_load_dwordx2 %0, %1, %2, %3 offen sc1" : "=v"(d) : "v"(voff), "s"(rs), "s"(soff) : "memory"); }
DEV void ld16_v(u4& d0, u4& d1, unsigned voff, u4 rs, unsigned soff) {
    asm volatile("s_mov_b64 exec, %5\n\tbuffer_load_dwordx4 %0, %2, %3, %4 offen offset:1024 sc1\n\tbuffer_load_dwordx4 %1, %2, %3, %4 offen offset:1536 sc1\n\ts_mov_b64 exec, -1"
                 : "+v"(d0), "+v"(d1) : "v"(voff), "s"(rs), "s"(soff), "s"(0x0000FFFF0000FFFFull) : "memory"); }
DEV void st16(u4 d, unsigned voff, u4 rs, unsigned soff) {
    asm volatile("buffer_store_dwordx4 %0, %1, %2, %3 offen sc1\n\ts_nop 1" ::"v"(d), "v"(voff), "s"(rs), "s"(soff) : "memory"); }
DEV void st16_plain(u4 d, unsigned voff, u4 rs, unsigned soff) {
    asm volatile("buffer_store_dwordx4 %0, %1, %2, %3 offen\n\ts_nop 1" ::"v"(d), "v"(voff), "s"(rs), "s"(soff) : "memory"); }
DEV void st8(u2 d, unsigned voff, u4 rs, unsigned soff) {
    asm volatile("buffer_store_dwordx2 %0, %1, %2, %3 offen sc1\n\ts_nop 1" ::"v"(d), "v"(voff), "s"(rs), "s"(soff) : "memory"); }
#define WAITV1(N, a) asm volatile("s_waitcnt vmcnt(%1)" : "+v"(a) : "i"(N) : "memory")
#define WAITV1D(ND, NS, a) asm volatile("s_waitcnt vmcnt(%1)\n\ts_cmp_lt_u32 %3, 4\n\ts_cbranch_scc1 .Lw%=\n\ts_waitcnt vmcnt(%2)\n.Lw%=:" : "+v"(a) : "i"(ND), "i"(NS), "s"(w) : "memory", "scc")
#define WAITV3(N, a, b, c) asm volatile("s_waitcnt vmcnt(%3)" : "+v"(a), "+v"(b), "+v"(c) : "i"(N) : "memory")
#define WAITV4(N, a, b, c, d) asm volatile("s_waitcnt vmcnt(%4)" : "+v"(a), "+v"(b), "+v"(c), "+v"(d) : "i"(N) : "memory")

DEV u4 lds16(unsigned a) { return *(LAS const u4*)a; }
DEV f4 lds16f(unsigned a) { return *(LAS const f4*)a; }
DEV float lds4f(unsigned a) { return *(LAS const float*)a; }
DEV void sts16(unsigned a, u4 v) { *(LAS u4*)a = v; }
DEV void sts16f(unsigned a, f4 v) { *(LAS f4*)a = v; }
DEV void sts8f(unsigned a, f2 v) { *(LAS f2*)a = v; }
DEV void sts4f(unsigned a, float v) { *(LAS float*)a = v; }
DEV u4 ldtr(unsigned a0, unsigned a1) {
    const s4 lo = __builtin_amdgcn_ds_read_tr16_b64_v4i16((LAS s4*)a0), hi = __builtin_amdgcn_ds_read_tr16_b64_v4i16((LAS s4*)a1);
    return __builtin_bit_cast(u4, __builtin_shufflevector(lo, hi, 0, 1, 2, 3, 4, 5, 6, 7)); }
DEV unsigned pk2(f2 a) { return __builtin_bit_cast(unsigned, __builtin_convertvector(a, bf2)); }
DEV f2 lo2(f4 a) { return (f2){a[0], a[1]}; }
DEV f2 hi2(f4 a) { return (f2){a[2], a[3]}; }
DEV unsigned pk(float a, float b) { return __builtin_bit_cast(unsigned, __builtin_convertvector((f2){a, b}, bf2)); }
DEV float blo(unsigned v) { return __builtin_bit_cast(float, v << 16); }
DEV float bhi(unsigned v) { return __builtin_bit_cast(float, v & 0xffff0000u); }
DEV float fu(unsigned v) { return __builtin_bit_cast(float, v); }
DEV unsigned uf(float v) { return __builtin_bit_cast(unsigned, v); }
DEV float add_x32(float v) { auto r = __builtin_amdgcn_permlane32_swap(uf(v), uf(v), false, false); const unsigned a = r[0], b = r[1]; return fu(a) + fu(b); }
DEV float add_x16(float v) { auto r = __builtin_amdgcn_permlane16_swap(uf(v), uf(v), false, false); const unsigned a = r[0], b = r[1]; return fu(a) + fu(b); }
DEV float max_x32(float v) { auto r = __builtin_amdgcn_permlane32_swap(uf(v), uf(v), false, false); const unsigned a = r[0], b = r[1]; return __builtin_fmaxf(fu(a), fu(b)); }
template <int C> DEV float add_dpp(float v) { return v + fu(__builtin_amdgcn_update_dpp(0u, uf(v), C, 0xf, 0xf, true)); }
DEV float add_x8(float v) { return v + fu(__builtin_amdgcn_update_dpp(0u, uf(v), 0x128, 0xf, 0xf, true)); }
constexpr u64 diag_mask(int r) { u64 m = 0; for (int l = 0; l < 64; ++l) { const int key = 16 * ((r >> 2) & 1) + 8 * (l >> 5) + 4 * (r >> 3) + (r & 3); if (key <= (l & 31)) m |= 1ull << l; } return m; }
DEV float sel(u64 m, float t, float f) { return __builtin_amdgcn_inverse_ballot_w64(m) ? t : f; }

template <unsigned LDS> DEV void touch(unsigned voff, u4 rs, unsigned soff) {
    asm volatile("s_mov_b32 m0, %3\n\ts_nop 0\n\tbuffer_load_dword %0, %1, %2 offen lds" ::"v"(voff), "s"(rs), "s"(soff), "i"(LDS) : "memory"); }
typedef short sh2 __attribute__((ext_vector_type(2)));
constexpr int kgo(int i) { return i < 8 ? 4 * i : (i - 8) / 3 * 4 + (i - 8) % 3 + 1; }
#define RSLEEP 4
#define SL1 0
#define SL2 0
#define SL3 0
DEV bool any_lane(bool c) { return __builtin_amdgcn_ballot_w64(c) != 0; }
#define UNLIKELY(x) __builtin_expect(!!(x), 0)
#define NRETRY()
#define FRESH16(d, voff, rs, soff) do { if (UNLIKELY(any_lane((d)[3] == ~0u))) { \
        _Pragma("unroll 1") for (int s_ = 0; s_ < SPIN_CAP; ++s_) { NRETRY(); u4 t_; asm volatile("buffer_load_dwordx4 %0, %1, %2, %3 offen sc1\n\ts_waitcnt vmcnt(0)" : "=&v"(t_) : "v"(voff), "s"(rs), "s"(soff) : "memory"); \
            d = t_; if (!any_lane(t_[3] == ~0u)) break; __builtin_amdgcn_s_sleep(2); } } } while (0)
#define POLL(voff) do { \
        _Pragma("unroll 1") for (int s_ = 0; s_ < 4 * SPIN_CAP; ++s_) { u2 t_; asm volatile("buffer_load_dwordx2 %0, %1, %2, 0 offen sc1\n\ts_waitcnt vmcnt(0)" : "=&v"(t_) : "v"(voff), "s"(rsF) : "memory"); \
            if (!any_lane(t_[0] != tag[0] || t_[1] != tag[1])) break; NRETRY(); __builtin_amdgcn_s_sleep(RSLEEP); } } while (0)
#define FLAG(off) st8(tag, l == 0 ? (off) : OOB, rsF, 0)
#define STAMP(i)

__global__ __launch_bounds__(512, 2) void fused_block(const char* x_, const char* wqkv_, const char* wo_, const char* wg_, const char* wu_, const char* wd_, char* out_, char* ws_) {
#if defined(__HIP_DEVICE_COMPILE__)

    typedef u64 u64x8 __attribute__((ext_vector_type(8)));
    u64x8 ka; asm volatile("s_load_dwordx16 %0, %1, 0x0\n\ts_waitcnt lgkmcnt(0)" : "=s"(ka) : "s"(__builtin_amdgcn_kernarg_segment_ptr()) : "memory");

    const char *x = (const char*)ka[0], *wqkv = (const char*)ka[1], *wo = (const char*)ka[2], *wg = (const char*)ka[3], *wu = (const char*)ka[4], *wd = (const char*)ka[5]; char *out = (char*)ka[6], *ws = (char*)ka[7];
    const unsigned l = threadIdx.x & 63, w = __builtin_amdgcn_readfirstlane(threadIdx.x >> 6);
    const unsigned b = blockIdx.x, xc = b & 7, rk = b >> 3;
    const unsigned T64 = 8 * (xc >> 1) + (rk >> 2), hq = 4 * (xc & 1) + (rk & 3);
    STAMP(0);
    const unsigned u8 = l >> 3, l7 = l & 7;
    u4 xr[8];
    const u4 rsX = mk_rsrc(x + T64 * 65536u, 65536u);
    const unsigned vx = (8 * w + u8) * 1024 + l7 * 16;
#define CF 0
    const char* pc; unsigned nb; asm volatile("s_getpc_b64 %0\n.Lfb_code0:\n\ts_mov_b32 %1, .Lfb_code1-.Lfb_code0" : "=s"(pc), "=s"(nb));
#define TOUCH_A(CODE, WQ) do { const unsigned co = ((rk * 8 + w) * 2 + (l & 1)) * 128; \
        const char* p = (WQ && (!CODE || l < 32)) ? wqkv + ((b * 8 + w) * 6 + (l < 6 ? l : 0)) * 128 : pc + (co < nb ? co : 0); \
        asm volatile("global_load_dword v254, %0, off" ::"v"(p) : "memory", "v254"); } while (0)
    if (CF == 1) TOUCH_A(1, 0);
    if (CF == 2) TOUCH_A(1, 1);
#define HOT 7
    {
        const unsigned hs = (3u - (unsigned)((u64)wqkv >> 7)) & 7u;
        if ((hs >> 2) == (xc & 1) && w == HOT) { const unsigned z = rk * 48 + (l < 48 ? l : 0); const char* p = wqkv + (hs * 128 + z * 1024); asm volatile("global_load_dword v254, %0, off" ::"v"(p) : "memory", "v254"); } }
    ld16_imm<0>(xr[0], vx, rsX); ld16_imm<128>(xr[1], vx, rsX); ld16_imm<256>(xr[2], vx, rsX); ld16_imm<384>(xr[3], vx, rsX);
#define XB 6
    if (XB > 4) { ld16_imm<512>(xr[4], vx, rsX); ld16_imm<640>(xr[5], vx, rsX); }
    if (XB > 6) { ld16_imm<768>(xr[6], vx, rsX); ld16_imm<896>(xr[7], vx, rsX); }
    const u4 rsW = mk_rsrc(wqkv + hq * 128u, 512u * 3072u - hq * 128u);
    SBAR();
    const unsigned w3 = w & 3, wk2 = w3 * 2048;
    unsigned vw0 = (16 * w3 + u8) * 3072 + 32 * ((l7 >> 1) ^ ((l >> 4) & 1)) + 16 * (l7 & 1), vw1 = (vw0 + 24576) ^ 64;
#define SLOT(i) ((((i) + 1) & 3) * 24576u)
#define SP(i, p) dma_bufi<SLOT(i) + ((p) >> 1) * 8192 + ((p) & 1) * 1024, (i) * 196608u + ((p) >> 1) * 1024u>(wk2, ((p) & 1) ? vw1 : vw0, rsW, 0)
    const unsigned U64 = 4 * xc + (rk >> 3), fb = rk & 7;
    const unsigned g = 2 * (xc >> 1) + (rk >> 4), r = 16 * (xc & 1) + (rk & 15);
    const unsigned j = w & 1, hh = w >> 1;
    const unsigned t16 = l & 15, g4 = l >> 4, h = l >> 5, t32 = l & 31, q4 = t16 >> 2, pp = l & 3, l16 = l * 16, l128 = l * 128;
    const u4 rsF = mk_rsrc(ws, 16u << 20);
    const u4 ones = {0x3f803f80u, 0x3f803f80u, 0x3f803f80u, 0x3f803f80u};
    const unsigned kvb = WS_KV + (4 * hq + hh) * KVS;
    const unsigned skv = kvb + (2 * T64 + j) * 2048;
    const unsigned sOw = WS_O + (2 * T64 + j) * OS + (4 * hq + hh) * 1024;
    u2 tag; u4 xres;
    unsigned vres = (32 * j + 8 * hh + l7) * 1024 + fb * 128 + u8 * 16;
    unsigned xw0 = L_A + (8 * w + u8) * 1024 + 16 * (l7 ^ (8 * (w & 1) + u8)), xw1 = xw0 ^ 128;
    unsigned xa[4];
#pragma unroll
    for (int q = 0; q < 4; ++q) { xa[q] = L_A + (32 * j + t16) * 1024 + 16 * ((4 * q + g4) ^ t16); asm volatile("" : "+v"(xa[q])); }
    unsigned wa = (8 * g4 + q4) * 128 + 32 * (hh ^ ((q4 >> 1) | ((g4 & 1) << 1))) + 8 * pp;
    f4 aq[2] = {}, ak[2] = {}, av[2] = {};
    unsigned vk = (h * 32 + (16 * ((t32 >> 3) & 1) + 8 * ((t32 >> 2) & 1) + 4 * (t32 >> 4) + (t32 & 3))) * 16, vv_ = (h * 16 + t16) * 16;
    unsigned vst = 1024 + ((g4 & 1) * 32 + (g4 >> 1) * 16 + t16) * 16;
    unsigned vrow = u8 * 1024 + 16 * (l7 ^ (((l >> 4) & 1) << 2));
    const char* gp[3];
#pragma unroll
    for (int i = 0; i < 3; ++i) { const unsigned z = 64 * i + l, k = (z * 43691u) >> 19, c = z - 12 * k, m = c >= 6, cc = c - 6 * m; unsigned un = 3 * r + (cc >> 1); un = un > 85 ? 85 : un;
        gp[i] = (m ? wu : wg) + ((16 * w + k) * 2752 + un * 32 + (cc & 1) * 16); asm volatile("" : "+v"(gp[i])); }
    const unsigned wk3 = w * 3072;
#define GU(R, c) dma_glbi<(R) * 24576 + (c) * 1024>(wk3, gp[c] + (R) * 128 * 2752)
#define GUD(R, q) dma_glbi<(R) * 24576 + ((q) / 3) * 12288 + ((q) % 3) * 1024>(wk3, gp[(q) % 3] + ((R) * 128 + ((q) / 3) * 64) * 2752)
    asm volatile("" : "+v"(vw0), "+v"(vw1), "+v"(xw0), "+v"(xw1), "+v"(wa), "+v"(vres), "+v"(vk), "+v"(vv_), "+v"(vrow), "+v"(vst));
    STAMP(39);
    if (XB <= 4) { ld16_imm<512>(xr[4], vx, rsX); ld16_imm<640>(xr[5], vx, rsX); }
    if (XB <= 6) { ld16_imm<768>(xr[6], vx, rsX); ld16_imm<896>(xr[7], vx, rsX); }
    if (CF == 0) TOUCH_A(1, 1);
    if (CF == 1) TOUCH_A(0, 1);
#define XA (CF != 2)
#define NO ((void)0)
#define HOTB(wv, base, n) do { if (w == (wv)) { const char* p_ = base + ((((3u - (unsigned)((u64)base >> 7)) & 7u) * 128) + (rk * (n) + (l < (n) ? l : 0)) * 1024); asm volatile("global_load_dword v254, %0, off" ::"v"(p_) : "memory", "v254"); } } while (0)
#define TBPOS 0
#define TOUCH_B() do { const char* tp = (w == 4 ? wo : w == 5 ? wg : w == 6 ? wu : wd) + (w == 4 ? b * 16 + (l & 15) : b * 43 + (l < 43 ? l : 0)) * 128; \
        asm volatile("global_load_dword v255, %0, off" ::"v"(tp) : "memory", "v255"); } while (0)
#if !defined(NOTB) && TBPOS == 0
    if (w >= 4) TOUCH_B();
#define XW 1
#else
#define XW 0
#endif
#define SSTAMP(i)
    {
        f2 xf[32], s1 = {0.f, 0.f}, s2 = {0.f, 0.f};
#define LW 0
#define TWD() NO
#define LN_SUM(i, N, D0, D1) do { if (XW && (N) < 8 - (i)) WAITV1((N) + XA, xr[i]); else WAITV1D((N) + XA, 7 + XA - (i) + XW - LW, xr[i]); _Pragma("unroll") for (int e = 0; e < 4; ++e) { const unsigned d_ = xr[i][e]; f2 v_ = {blo(d_), bhi(d_)}; asm volatile("" : "+v"(v_)); xf[4 * (i) + e] = v_; \
        s1 += v_; s2 += v_ * v_; } asm volatile("" : "+v"(s1), "+v"(s2)); if (w < 4) { D0; D1; } SBAR(); } while (0)
        LN_SUM(0, 7, NO, NO); LN_SUM(1, 6, NO, NO); LN_SUM(2, 5, NO, NO); LN_SUM(3, 4, NO, NO);
        LN_SUM(4, 3, SP(0, 0), SP(0, 1)); LN_SUM(5, 4, SP(0, 2), SP(0, 3)); LN_SUM(6, 5, SP(0, 4), NO); LN_SUM(7, 5, SP(0, 5), NO);
        SSTAMP(34);
        if (w < 4) { SP(1, 0); SP(1, 1); SP(1, 2); SP(1, 3); SP(1, 4); SP(1, 5); }
#if !defined(NOTB) && TBPOS == 1
        else TOUCH_B();
#endif
        const float t1 = add_dpp<0x141>(add_dpp<0x4e>(add_dpp<0xb1>(s1[0] + s1[1]))), t2 = add_dpp<0x141>(add_dpp<0x4e>(add_dpp<0xb1>(s2[0] + s2[1])));
        const float mu = t1 * (1.f / 512), rs = __builtin_amdgcn_rsqf(__builtin_fmaxf(t2 * (1.f / 512) - mu * mu, 0.f) + 1e-5f), mr = -mu * rs;
#pragma unroll
        for (int i = 0; i < 8; ++i) sts16(((i & 1) ? xw1 : xw0) + (i >> 1) * 256, (u4){pk2(xf[4 * i] * rs + mr), pk2(xf[4 * i + 1] * rs + mr), pk2(xf[4 * i + 2] * rs + mr), pk2(xf[4 * i + 3] * rs + mr)});
    }
#define P1_STEP(n, D0, D1, D2) do { constexpr int c_ = (n) & 1, x_ = c_ ^ 1; constexpr bool m_ = (n) < 15; \
        const unsigned xb = xa[((n) + 1) & 3] + (((n) + 1) >> 2) * 256, wb = wa + SLOT(((n) + 1) >> 1) + x_ * 4096; \
        if (m_) X0[x_] = lds16(xb); SBAR(); \
        aq[0] = MFMA16(Wq[c_], X0[c_], aq[0]); if (m_) Wq[x_] = ldtr(wb, wb + 512); SBAR(); \
        aq[1] = MFMA16(Wq[c_], X1[c_], aq[1]); if (m_) X1[x_] = lds16(xb + 16384); D0; SBAR(); \
        ak[0] = MFMA16(Wk[c_], X0[c_], ak[0]); if (m_) Wk[x_] = ldtr(wb + 8192, wb + 8704); SBAR(); \
        ak[1] = MFMA16(Wk[c_], X1[c_], ak[1]); D1; SBAR(); \
        av[0] = MFMA16(X0[c_], Wv[c_], av[0]); if (m_) Wv[x_] = ldtr(wb + 16384, wb + 16896); SBAR(); \
        av[1] = MFMA16(X1[c_], Wv[c_], av[1]); D2; SBAR(); } while (0)
#define P1_FIRST() do { const unsigned wb = wa + SLOT(0); X0[0] = lds16(xa[0]); Wq[0] = ldtr(wb, wb + 512); X1[0] = lds16(xa[0] + 16384); Wk[0] = ldtr(wb + 8192, wb + 8704); Wv[0] = ldtr(wb + 16384, wb + 16896); } while (0)
#define XRES() ld16_plain(xres, vres, mk_rsrc(x + U64 * 65536u, 65536u), 0)
    if (w < 4) {
        u4 X0[2], X1[2], Wq[2], Wk[2], Wv[2];
        SP(2, 0); SP(2, 1); SP(2, 2); SP(2, 3); SP(2, 4); SP(2, 5);
        BAR_VM(12); STAMP(16);  P1_FIRST(); P1_STEP(0, (SP(3, 0), SP(3, 1)), (SP(3, 2), SP(3, 3)), (SP(3, 4), SP(3, 5)));
        BAR_VM(12); SSTAMP(17); P1_STEP(1, SP(4, 0), SP(4, 1), SP(4, 2)); P1_STEP(2, SP(4, 3), SP(4, 4), SP(4, 5));
        BAR_VM(12); SSTAMP(18); P1_STEP(3, SP(5, 0), SP(5, 1), SP(5, 2)); P1_STEP(4, SP(5, 3), SP(5, 4), SP(5, 5));
        BAR_VM(12); SSTAMP(19); P1_STEP(5, SP(6, 0), SP(6, 1), SP(6, 2)); P1_STEP(6, SP(6, 3), SP(6, 4), SP(6, 5));
        BAR_VM(12); SSTAMP(20); P1_STEP(7, SP(7, 0), SP(7, 1), SP(7, 2)); P1_STEP(8, SP(7, 3), SP(7, 4), SP(7, 5));
        BAR_VM(12); SSTAMP(21); P1_STEP(9, XRES(), GUD(1, 0), NO); P1_STEP(10, GUD(1, 1), GUD(1, 2), NO);
        BAR_VM(10); SSTAMP(22); P1_STEP(11, GUD(1, 3), GUD(1, 4), NO); P1_STEP(12, GUD(1, 5), NO, NO);
        BAR_VM(7);  SSTAMP(23); P1_STEP(13, NO, NO, NO); P1_STEP(14, NO, NO, NO); P1_STEP(15, NO, NO, NO);
        WAITV1(6, xres); tag = (u2){0u, 0u};
    } else {
        u4 X0[2], X1[2], Wq[2], Wk[2], Wv[2];
        const u4 bad = {~0u, ~0u, ~0u, ~0u};
#define MK(v, s_) st16(bad, v, rsF, s_)
#if !defined(NOTB) && TBPOS == 2
#define TCH(c) if (w == (c)) TOUCH_B()
#else
#define TCH(c) NO
#endif
        BAR_L(); STAMP(16);  P1_FIRST(); P1_STEP(0, ld8(tag, 0u * l, rsF, 0), MK(l16, skv), TCH(5));
        BAR_L(); SSTAMP(17); P1_STEP(1, NO, MK(l16 + 1024, skv), TCH(6)); P1_STEP(2, NO, MK(l16, skv - 2 * KVS), NO);
        BAR_L(); SSTAMP(18); P1_STEP(3, NO, MK(l16 + 1024, skv - 2 * KVS), TCH(4)); P1_STEP(4, NO, MK(l16, sOw), NO);
        BAR_L(); SSTAMP(19); P1_STEP(5, NO, MK(l16, sOw - 2048), TCH(7)); P1_STEP(6, XRES(), NO, NO);
        BAR_L(); SSTAMP(20); P1_STEP(7, NO, NO, NO); P1_STEP(8, NO, NO, NO);
        BAR_L(); SSTAMP(21); P1_STEP(9, NO, NO, NO); P1_STEP(10, NO, NO, NO);
        asm volatile("s_waitcnt vmcnt(0)" : "+v"(tag), "+v"(xres) :: "memory");
        BAR_L(); SSTAMP(22);
        { const unsigned lo = tag.x + 1u; tag.y += (lo == 0u); tag.x = lo; }
                             P1_STEP(11, FLAG(w == 4 ? WS_F1 + (T64 * 8 + hq) * 128 : OOB), NO, NO); P1_STEP(12, HOTB(6, wo, 16), NO, NO);
        BAR_L(); SSTAMP(23); P1_STEP(13, NO, NO, NO); P1_STEP(14, NO, NO, NO); P1_STEP(15, NO, NO, NO);
    }
    STAMP(1);
    u4 Qf;
    {
        { auto a = __builtin_amdgcn_permlane16_swap(pk2(lo2(ak[0])), pk2(lo2(ak[1])), false, false); auto c = __builtin_amdgcn_permlane16_swap(pk2(hi2(ak[0])), pk2(hi2(ak[1])), false, false);
          st16((u4){a[0], c[0], a[1], c[1]}, l16, rsF, skv); }
        st16((u4){pk2(lo2(av[0])), pk2(hi2(av[0])), pk2(lo2(av[1])), pk2(hi2(av[1]))}, vst, rsF, skv);
        STAMP(32);
        SBAR();
        constexpr float QS = 0.25f * 1.4426950408889634f;
        { auto a = __builtin_amdgcn_permlane16_swap(pk2(lo2(aq[0]) * QS), pk2(lo2(aq[1]) * QS), false, false); auto c = __builtin_amdgcn_permlane16_swap(pk2(hi2(aq[0]) * QS), pk2(hi2(aq[1]) * QS), false, false);
          Qf = (u4){a[0], c[0], a[1], c[1]}; }
        sts16(L_OC + (w ^ 1) * 2304 + l16, Qf);
    }
    const int Tb = 2 * (int)T64 - 8;
    u4 K[5], V0[5], V1[5];
    const int blk[5] = {Tb + 8 + (int)j, Tb + (int)j, Tb + 2 + 3 * (int)j, Tb + 3 + 3 * (int)j, Tb + 4 + 3 * (int)j};
#pragma unroll
    for (int i = 0; i < 5; ++i) { const unsigned o = blk[i] < 0 ? 0u : 0x3f803f80u; V0[i] = (u4){o, o, o, o}; V1[i] = V0[i]; asm volatile("" : "+v"(V0[i]), "+v"(V1[i])); }
    unsigned pm[8];
    { const short uq = (short)(t32 - 8 * h);
#pragma unroll
      for (int i = 0; i < 8; ++i) { const short c = (short)(16 * ((i >> 1) & 1) + 4 * (i >> 2) + 2 * (i & 1)); pm[i] = __builtin_bit_cast(unsigned, ((sh2){uq, uq} - (sh2){c, (short)(c + 1)}) >> (sh2){15, 15}); asm volatile("" : "+v"(pm[i])); } }
    unsigned so[5];
#pragma unroll
    for (int i = 0; i < 5; ++i) so[i] = blk[i] < 0 ? OOB : kvb + (unsigned)blk[i] * 2048;
    if (w == 7) {
        const int Tp = (int)T64 - 1 - (int)(l & 3); const unsigned vc = WS_F1 + ((Tp < 0 ? T64 : (unsigned)Tp) * 8 + hq) * 128; POLL(vc); }
    STAMP(35);
    BAR_VM(0);
    STAMP(2);
#define KVLD(i) do { ld16(K[i], vk, rsF, so[i]); ld16_v(V0[i], V1[i], vv_, rsF, so[i]); } while (0)
#define KVOK(N, i) do { WAITV3(N, K[i], V0[i], V1[i]); \
        if (UNLIKELY(any_lane(max(max(K[i][3], V0[i][3]), V1[i][3]) == ~0u))) { \
            _Pragma("unroll 1") for (int s_ = 0; s_ < SPIN_CAP; ++s_) { NRETRY(); KVLD(i); WAITV3(0, K[i], V0[i], V1[i]); if (!any_lane(max(max(K[i][3], V0[i][3]), V1[i][3]) == ~0u)) break; __builtin_amdgcn_s_sleep(2); } } } while (0)
    if (SL1) __builtin_amdgcn_s_sleep(SL1);
    KVLD(2); KVLD(3);
    const u4 Qm = Qf, Qo = lds16(L_OC + w * 2304 + l16);
    f16 Om = {}, Oo = {};
    const f16 z16 = {};
    auto pv_tile = [&](const f16& S, const u4& va, const u4& vb, f16& O) {
        float p[16];
#pragma unroll
        for (int e = 0; e < 16; ++e) p[e] = __builtin_amdgcn_exp2f(S[e]);
        const u4 P0 = {pk(p[0], p[1]), pk(p[2], p[3]), pk(p[4], p[5]), pk(p[6], p[7])}, P1 = {pk(p[8], p[9]), pk(p[10], p[11]), pk(p[12], p[13]), pk(p[14], p[15])};
        O = MFMA32(va, P0, O); O = MFMA32(vb, P1, O);
    };
    const u4 rsWo = mk_rsrc(wo + fb * 128u, 512u * 1024u - fb * 128u);
    const unsigned wol = L_A + w * 8192, wos = w * 65536;
    const unsigned vh = (u8 >> 1) * 1024 + ((u8 & 1) * 32 + 8 * hh + l7) * 16, vs = l < 8 ? (8 * hh + l) * 16 : OOB;
    const unsigned sHw = WS_H + (2 * U64 + j) * OS + fb * 4096, sSw = WS_ST + (2 * U64 + j) * 4096u + fb * 512;
    const unsigned sGa = r < 29 ? WS_G + (8 * g + w) * 90112u + r * 2048 : OOB, sGb = r < 28 ? WS_G + (8 * g + w) * 90112u + (60 + r) * 1024 : OOB;
#define WO_(i) dma_bufi<(i) * 1024, (i) * 8192>(wol, vrow, rsWo, wos)
#define PIN16(a) asm volatile("" : "+v"(a))
#define MEM(...) do { SBAR(); __VA_ARGS__; SBAR(); } while (0)
#define HALF(S, o, va, O) do { float p_[8]; _Pragma("unroll") for (int e = 0; e < 8; ++e) p_[e] = __builtin_amdgcn_exp2f(S[(o) + e]); \
        u4 q_ = {pk(p_[0], p_[1]), pk(p_[2], p_[3]), pk(p_[4], p_[5]), pk(p_[6], p_[7])}; asm volatile("" : "+v"(q_)); O = MFMA32(va, q_, O); PIN16(O); SBAR(); } while (0)
#define SCORE(S, k, q) do { S = MFMA32(k, q, z16); PIN16(S); SBAR(); } while (0)
    f16 Sa, Sb, Sc;
    u2 ck; const unsigned vc1 = WS_F1 + (U64 * 8 + l7) * 128;
    const u4 bad = {~0u, ~0u, ~0u, ~0u};
    KVOK(3, 2);
    STAMP(24);
    SCORE(Sa, K[2], Qm); SCORE(Sb, K[2], Qo); MEM(KVLD(4));
    HALF(Sa, 0, V0[2], Om); MEM(KVLD(0)); HALF(Sa, 8, V1[2], Om); MEM(KVLD(1)); KVOK(9, 3); TWD();
    SCORE(Sa, K[3], Qm); HALF(Sb, 0, V0[2], Oo); MEM(WO_(0)); HALF(Sb, 8, V1[2], Oo); MEM(WO_(1));
    SSTAMP(42);
    SCORE(Sb, K[3], Qo); HALF(Sa, 0, V0[3], Om); MEM(WO_(2)); HALF(Sa, 8, V1[3], Om); MEM(WO_(3)); KVOK(10, 4);
    SCORE(Sa, K[4], Qm); HALF(Sb, 0, V0[3], Oo); MEM(WO_(4)); HALF(Sb, 8, V1[3], Oo); MEM(WO_(5));
    SSTAMP(43);
    SCORE(Sb, K[4], Qo); HALF(Sa, 0, V0[4], Om); MEM(WO_(6)); HALF(Sa, 8, V1[4], Om); MEM(WO_(7)); KVOK(11, 0); KVOK(8, 1);
    if (j == 0) { SCORE(Sa, K[0], Qo); HALF(Sb, 0, V0[4], Oo); MEM(st16(bad, vh, rsF, sHw)); HALF(Sb, 8, V1[4], Oo); MEM(st16(bad, vs, rsF, sSw), st16(bad, l16, rsF, sGb));
                  SCORE(Sb, K[0], Qm); SCORE(Sc, K[1], Qm); HALF(Sa, 0, V0[0], Oo); HALF(Sa, 8, V1[0], Oo); }
    else        { SCORE(Sa, K[1], Qo); HALF(Sb, 0, V0[4], Oo); MEM(st16(bad, vh, rsF, sHw)); HALF(Sb, 8, V1[4], Oo); MEM(st16(bad, vs, rsF, sSw), st16(bad, l16, rsF, sGb));
                  SCORE(Sb, K[0], Qm); SCORE(Sc, K[1], Qm); HALF(Sa, 0, V0[1], Oo); HALF(Sa, 8, V1[1], Oo); }
    SSTAMP(44);
    MEM(ld8(ck, w == 7 ? vc1 : OOB, rsF, 0), GU(2, 0), GU(2, 1), GU(2, 2));
    const unsigned oc = L_OC + w * 2304;
    {
        const f16 Sx = Sb, Sy = Sc;
        float p[16];
#pragma unroll
        for (int e = 0; e < 8; ++e) p[e] = __builtin_amdgcn_exp2f(sel(diag_mask(e), Sx[e], Sy[e]));
        SBAR();
        sts16f(oc + l16, (f4){Oo[0], Oo[1], Oo[2], Oo[3]}); sts16f(oc + 1024 + l16, (f4){Oo[4], Oo[5], Oo[6], Oo[7]}); sts4f(oc + 2048 + l * 4, Oo[8]);
#pragma unroll
        for (int e = 8; e < 16; ++e) p[e] = __builtin_amdgcn_exp2f(sel(diag_mask(e), Sx[e], Sy[e]));
        unsigned P[8];
#pragma unroll
        for (int e = 0; e < 8; ++e) P[e] = pk(p[2 * e], p[2 * e + 1]);
        const u4 Y0 = {P[0] & pm[0], P[1] & pm[1], P[2] & pm[2], P[3] & pm[3]}, Y1 = {P[4] & pm[4], P[5] & pm[5], P[6] & pm[6], P[7] & pm[7]};
        const u4 X0_ = {P[0] ^ Y0[0], P[1] ^ Y0[1], P[2] ^ Y0[2], P[3] ^ Y0[3]}, X1_ = {P[4] ^ Y1[0], P[5] ^ Y1[1], P[6] ^ Y1[2], P[7] ^ Y1[3]};
        Om = MFMA32(V0[0], X0_, Om); Om = MFMA32(V1[0], X1_, Om); Om = MFMA32(V0[1], Y0, Om); Om = MFMA32(V1[1], Y1, Om);
    }
    SBAR();
    STAMP(25);
    asm volatile("s_waitcnt vmcnt(3) lgkmcnt(0)\n\ts_barrier" : "+v"(ck) :: "memory");
    SSTAMP(45);
    const unsigned op = L_OC + (w ^ 1) * 2304;
    f4 oa = lds16f(op + l16) + (f4){Om[0], Om[1], Om[2], Om[3]}, ob = lds16f(op + 1024 + l16) + (f4){Om[4], Om[5], Om[6], Om[7]};
    float den = Om[8] + lds4f(op + 2048 + l * 4);
    if (UNLIKELY(any_lane(!(den >= 0x1p-60f && den <= 0x1p60f)))) {
        f16 O = {}; float m = -__builtin_inff();
#pragma unroll 1
        for (int kb = 8; kb >= 0; --kb) {
            const int T = 2 * (int)T64 + (int)j - 8 + kb;
            if (T < 0) break;
            u4 Ks, Va = ones, Vb = ones;
            ld16(Ks, vk, rsF, kvb + (unsigned)T * 2048); ld16_v(Va, Vb, vv_, rsF, kvb + (unsigned)T * 2048);
            WAITV3(0, Ks, Va, Vb);
            f16 S = MFMA32(Ks, Qm, z16);
            const int up = kb == 8 ? (int)(t32 - 8 * h) : 99, dn = kb == 0 ? (int)(t32 - 8 * h) : -99;
            float mx = m;
#pragma unroll
            for (int e = 0; e < 16; ++e) { const int c = 16 * ((e >> 2) & 1) + 4 * (e >> 3) + (e & 3); S[e] = (c <= up && c > dn) ? S[e] : -__builtin_inff(); mx = __builtin_fmaxf(mx, S[e]); }
            mx = max_x32(mx);
            const float al = __builtin_amdgcn_exp2f(m - mx); m = mx;
#pragma unroll
            for (int e = 0; e < 16; ++e) { O[e] *= al; S[e] -= mx; }
            pv_tile(S, Va, Vb, O);
        }
        oa = (f4){O[0], O[1], O[2], O[3]}; ob = (f4){O[4], O[5], O[6], O[7]}; den = O[8];
    }
    const float rd = __builtin_amdgcn_rcpf(den);
    st16((u4){pk2(lo2(oa) * rd), pk2(hi2(oa) * rd), pk2(lo2(ob) * rd), pk2(hi2(ob) * rd)}, l16, rsF, sOw);
#define F23() FLAG(w == 4 ? WS_F2 + (U64 * 8 + fb) * 128 : w == 5 ? WS_F3 + (g * 32 + r) * 128 : OOB)
    F23();
    STAMP(26);
    const unsigned ta0 = (4 * h + q4) * 128 + 64 * (q4 >> 1) + 32 * (g4 & 1) + 8 * pp, ta1 = ta0 ^ 64;
    f16 acc0 = {}, acc1 = {};
    {
        const unsigned sO = WS_O + (2 * U64 + j) * OS + hh * 8192;
        const unsigned oa0 = L_A + hh * 16384 + ta0, oa1 = L_A + hh * 16384 + ta1;
        u4 A0[8], A1[8], B[8];
#pragma unroll
        for (int i = 0; i < 8; ++i) { A0[i] = ldtr(oa0 + i * 2048, oa0 + i * 2048 + 1024); A1[i] = ldtr(oa1 + i * 2048, oa1 + i * 2048 + 1024); }
        if (w == 7 && UNLIKELY(any_lane(ck[0] != tag[0] || ck[1] != tag[1]))) POLL(vc1);
        STAMP(37);
        BAR_VM(1);
        STAMP(5);
        if (SL2) __builtin_amdgcn_s_sleep(SL2);
#pragma unroll
        for (int i = 0; i < 8; ++i) ld16(B[i], l16, rsF, sO + i * 1024);
#define P3_PAIR(i, NW, M0, M1) do { asm volatile("s_waitcnt vmcnt(%2)" : "+v"(B[i]), "+v"(B[(i) + 1]) : "i"(NW) : "memory"); \
            if (UNLIKELY(any_lane(max(B[i][3], B[(i) + 1][3]) == ~0u))) { \
                _Pragma("unroll 1") for (int s_ = 0; s_ < SPIN_CAP; ++s_) { NRETRY(); __builtin_amdgcn_s_sleep(3); asm volatile("s_waitcnt vmcnt(0)" ::: "memory"); unsigned m_ = 0; \
                    _Pragma("unroll") for (int q = (i); q < 8; ++q) ld16(B[q], l16, rsF, sO + q * 1024); \
                    _Pragma("unroll") for (int q = (i); q < 8; ++q) { WAITV1(7 - q, B[q]); m_ = max(m_, B[q][3]); } \
                    if (!any_lane(m_ == ~0u)) break; } } if ((i) == 0) STAMP(4); \
            acc0 = MFMA32(A0[i], B[i], acc0); acc1 = MFMA32(A1[i], B[i], acc1); M0; SBAR(); acc0 = MFMA32(A0[(i) + 1], B[(i) + 1], acc0); acc1 = MFMA32(A1[(i) + 1], B[(i) + 1], acc1); M1; SBAR(); } while (0)
        P3_PAIR(0, 6, NO, NO); P3_PAIR(2, 4, NO, NO); P3_PAIR(4, 2, GU(0, 0), GU(0, 1)); P3_PAIR(6, 2, GU(0, 2), NO);
    }
    const unsigned vc2 = WS_F2 + (g * 32 + t32) * 128;
    ld8(ck, w == 7 ? vc2 : OOB, rsF, 0);
    STAMP(27);
    const unsigned rw0 = L_A + (hh * 2 + j) * 8192 + h * 512 + t32 * 16, rw1 = rw0 ^ 128, rr = L_A + j * 8192 + u8 * 1024 + (((8 * hh + l7) ^ ((u8 & 1) << 3)) * 16);
    auto reduce = [&](f4& lo, f4& hi) {
#pragma unroll
        for (int u = 0; u < 4; ++u) {
            sts16f(((u & 1) ? rw1 : rw0) + u * 1024, (f4){acc0[4 * u], acc0[4 * u + 1], acc0[4 * u + 2], acc0[4 * u + 3]});
            sts16f(((u & 1) ? rw1 : rw0) + (4 + u) * 1024, (f4){acc1[4 * u], acc1[4 * u + 1], acc1[4 * u + 2], acc1[4 * u + 3]});
        }
        BAR_L();
        lo = (lds16f(rr) + lds16f(rr + 16384)) + (lds16f(rr + 32768) + lds16f(rr + 49152));
        hi = (lds16f(rr + 512) + lds16f(rr + 16896)) + (lds16f(rr + 33280) + lds16f(rr + 49664));
    };
    u4 Hres;
    const unsigned ga = (8 * h + q4) * 192 + (g4 & 1) * 96 + 8 * pp;
    f16 cs[3] = {};
    {
        f4 lo, hi; reduce(lo, hi);
        STAMP(28);
        Hres = (u4){pk2(lo2(lo) + (f2){blo(xres[0]), bhi(xres[0])}), pk2(hi2(lo) + (f2){blo(xres[1]), bhi(xres[1])}), pk2(lo2(hi) + (f2){blo(xres[2]), bhi(xres[2])}), pk2(hi2(hi) + (f2){blo(xres[3]), bhi(xres[3])})};
        st16(Hres, vh, rsF, sHw);
        f2 s1 = {0.f, 0.f}, s2 = {0.f, 0.f};
#pragma unroll
        for (int e = 0; e < 4; ++e) { const f2 a = {blo(Hres[e]), bhi(Hres[e])}; s1 += a; s2 += a * a; }
        const float t1 = add_x32(add_x16(add_x8(s1[0] + s1[1]))), t2 = add_x32(add_x16(add_x8(s2[0] + s2[1])));
        st16((u4){uf(t1), uf(t2), uf(t1), uf(t2)}, vs, rsF, sSw);
        STAMP(33);
        GU(3, 0); GU(3, 1); GU(3, 2);
        asm volatile("s_waitcnt vmcnt(5) lgkmcnt(0)\n\ts_barrier" : "+v"(ck) :: "memory");
        STAMP(40);
#define COLSUM(k0, N) do { const unsigned o = ga + (k0) * 3072; u4 a[3 * (N)]; \
        _Pragma("unroll") for (int i = 0; i < 3 * (N); ++i) a[i] = ldtr(o + (i / 3) * 3072 + (i % 3) * 32, o + (i / 3) * 3072 + (i % 3) * 32 + 768); SBAR(); \
        _Pragma("unroll") for (int i = 0; i < 3 * (N); ++i) cs[i % 3] = MFMA32(ones, a[i], cs[i % 3]); PIN16(cs[0]); PIN16(cs[1]); PIN16(cs[2]); SBAR(); } while (0)
        COLSUM(3 * w, 3);
    }
    STAMP(36);
    const u4 rsWd = mk_rsrc(wd + fb * 128u, 1376u * 1024u - fb * 128u);
    if (w == 7 && UNLIKELY(any_lane(ck[0] != tag[0] || ck[1] != tag[1]))) POLL(vc2);
    if (w == 5 && b == 255) {
#pragma unroll 1
        for (int s_ = 0; s_ < 4 * SPIN_CAP; ++s_) { u2 f0, f1, f2_, f3; const unsigned vc = WS_F1 + l * 128;
            ld8(f0, vc, rsF, 0); ld8(f1, vc, rsF, 8192); ld8(f2_, vc, rsF, 16384); ld8(f3, vc, rsF, 24576); WAITV4(0, f0, f1, f2_, f3);
            if (!any_lane(((f0[0] ^ tag[0]) | (f1[0] ^ tag[0]) | (f2_[0] ^ tag[0]) | (f3[0] ^ tag[0]) | (f0[1] ^ tag[1]) | (f1[1] ^ tag[1]) | (f2_[1] ^ tag[1]) | (f3[1] ^ tag[1])) != 0u)) break; }
        FLAG(0u);
    }
    BAR_VM(0);
    STAMP(6);
    if (SL3) __builtin_amdgcn_s_sleep(SL3);
    const unsigned rot = 0;
    const unsigned a0 = 15 * hh, a1 = a0 + 1, jo = j * 8192;
    const unsigned wde = (48 * (a0 >> 1) + 16 * (a0 & 1)) * 1024 + jo, wdo = (48 * (a1 >> 1) + 16 * (a1 & 1)) * 1024 + jo, wdt = (336 * hh + 32) * 1024 + jo;
    const unsigned wla = L_A + hh * 14336 + j * 1024, wlb = rot * 49152 + hh * 12288 + j * 1024, wlc = 49152 - rot * 49152 + hh * 4096 + j * 1024;
#define WD(i) dma_bufi<((i) < 14 ? (i) % 7 : (i) < 20 ? (i) - 14 : (i) - 20) * 2048u, ((i) < 15 ? (i) >> 1 : (i) - 15) * 49152u>((i) < 14 ? wla : (i) < 20 ? wlb : wlc, vrow, rsWd, (i) >= 15 ? wdt : ((i) & 1) ? wdo : wde)
    u4 D0[7], D1[7];
    {
        const unsigned sH = WS_H + (8 * g + w) * OS, sS = WS_ST + (8 * g + w) * 4096u;
#define PD4 6
        constexpr int PD = PD4;
        u4 Hb[32], A0[3], A1[3], A2[4];
        u2 st[4];
        f16 ag[3] = {};
        const unsigned cf = L_CF + w * 384, vstat = h * 2048 + t32 * 16;
        const unsigned gl = ga + rot * 49152, gh = ga - rot * 49152, sl = sH + rot * 16384, sh = sH - rot * 16384;
#define GA(k) (((k) < 16 ? gl : gh) + (k) * 3072)
#define HO(k) (((k) < 16 ? sl : sh) + (k) * 1024)
#pragma unroll
        for (int i = 0; i < PD; ++i) ld16(Hb[kgo(i)], l16, rsF, HO(kgo(i)));
        COLSUM(24 + w, 1); sts16f(L_CSP + w * 512 + t32 * 16, (f4){cs[0][0], cs[1][0], cs[2][0], 0.f}); BAR_L();
        {
            const unsigned o = L_CSP + h * 2048 + t32 * 16; const f4 s = (lds16f(o) + lds16f(o + 512)) + (lds16f(o + 1024) + lds16f(o + 1536));
            const float c0 = add_x32(s[0]), c1 = add_x32(s[1]), c2 = add_x32(s[2]);
            sts4f(cf + l * 4, h ? c1 : c0); sts4f(cf + 256 + t32 * 4, c2); }
        A0[0] = ldtr(GA(0), GA(0) + 768); A1[0] = ldtr(GA(0) + 32, GA(0) + 800); A0[1] = ldtr(GA(kgo(1)), GA(kgo(1)) + 768); A1[1] = ldtr(GA(kgo(1)) + 32, GA(kgo(1)) + 800);
        unsigned mx = 0;
#define HDS 10
#define P4_STEP(i) do { constexpr int c = (i) % 3, n = ((i) + 2) % 3, k = kgo(i); const unsigned o = GA((i) < 30 ? kgo((i) + 2) : 0); \
            if ((i) + PD < 32) ld16(Hb[kgo((i) + PD < 32 ? (i) + PD : 0)], l16, rsF, HO(kgo((i) + PD))); \
            else if ((i) + PD < 36) ld8(st[((i) + PD - 32) & 3], vstat, rsF, sS + ((i) + PD - 32) * 512); \
            WAITV1((i) + PD < 36 ? PD : 35 - (i), Hb[k]); if ((i) == HDS) HOTB(3, wd, 43); \
            if ((i) < 8) FRESH16(Hb[k], l16, rsF, HO(k)); else { mx = max(mx, Hb[k][3]); asm volatile("" : "+v"(mx)); } if ((i) == 0) STAMP(7); SBAR(); \
            ag[0] = MFMA32(A0[c], Hb[k], ag[0]); if ((i) < 30) A0[n] = ldtr(o, o + 768); else A2[((i) - 30) & 1] = ldtr(GA(((i) - 30) & 1) + 64, GA(((i) - 30) & 1) + 832); SBAR(); \
            ag[1] = MFMA32(A1[c], Hb[k], ag[1]); if ((i) < 30) A1[n] = ldtr(o + 32, o + 800); else if ((i) == 31) A2[2] = ldtr(GA(2) + 64, GA(2) + 832); SBAR(); } while (0)
#define P4_4(i) P4_STEP(i); P4_STEP((i) + 1); P4_STEP((i) + 2); P4_STEP((i) + 3)
        P4_4(0); P4_4(4); P4_4(8); P4_4(12); P4_4(16); P4_4(20); P4_4(24); P4_4(28);
        WAITV4(0, st[0], st[1], st[2], st[3]);
        if (UNLIKELY(any_lane(max(max(mx, max(st[0][1], st[1][1])), max(st[2][1], st[3][1])) == ~0u))) {
#pragma unroll
            for (int i = 0; i < 32; ++i) FRESH16(Hb[i], l16, rsF, HO(i));
#pragma unroll 1
            for (int s_ = 0; s_ < SPIN_CAP; ++s_) { NRETRY();
#pragma unroll
                for (int i = 0; i < 4; ++i) ld8(st[i], vstat, rsF, sS + i * 512);
                WAITV4(0, st[0], st[1], st[2], st[3]);
                if (!any_lane(max(max(st[0][1], st[1][1]), max(st[2][1], st[3][1])) == ~0u)) break;
            }
            ag[0] = z16; ag[1] = z16;
#pragma unroll
            for (int i = 0; i < 32; ++i) { const unsigned o = GA(kgo(i)); ag[0] = MFMA32(ldtr(o, o + 768), Hb[kgo(i)], ag[0]); ag[1] = MFMA32(ldtr(o + 32, o + 800), Hb[kgo(i)], ag[1]); }
        }
        STAMP(8);
        const float t1 = add_x32(fu(st[0].x) + fu(st[1].x) + fu(st[2].x) + fu(st[3].x)), t2 = add_x32(fu(st[0].y) + fu(st[1].y) + fu(st[2].y) + fu(st[3].y));
        const float nm = t1 * (-1.f / 512), ve = __builtin_fmaxf(t2 * (1.f / 512) - nm * nm, 0.f) + 1e-5f, rs = __builtin_amdgcn_rsqf(ve);
        const float ka_ = rs * -2.3022081986f, kb_ = rs * rs * rs * -0.1029432397f;
#define G_UNIT(u, so_, M0, M1) do { u4 y; _Pragma("unroll") for (int qd = 0; qd < 2; ++qd) { \
                const f4 cg = lds16f(cf + (u) * 128 + qd * 32 + h * 16), cu = lds16f(cf + (u) * 128 + (2 + qd) * 32 + h * 16); \
                _Pragma("unroll") for (int e = 0; e < 2; ++e) { \
                    const f2 gt = (f2){ag[u][4 * qd + 2 * e], ag[u][4 * qd + 2 * e + 1]} + (e ? hi2(cg) : lo2(cg)) * nm, up = (f2){ag[u][8 + 4 * qd + 2 * e], ag[u][9 + 4 * qd + 2 * e]} + (e ? hi2(cu) : lo2(cu)) * nm; \
                    const f2 z = gt * (gt * gt * kb_ + ka_), d = (f2){__builtin_amdgcn_exp2f(z[0]), __builtin_amdgcn_exp2f(z[1])} * ve + ve; \
                    unsigned y_ = pk2(gt * up * (f2){__builtin_amdgcn_rcpf(d[0]), __builtin_amdgcn_rcpf(d[1])}); asm volatile("" : "+v"(y_)); y[2 * qd + e] = y_; } \
                if (qd == 0) MEM(M0); } \
            MEM(st16(y, l16, rsF, so_); M1); } while (0)
        MEM(WD(0), WD(1), WD(2));
        G_UNIT(0, sGa, (WD(3), WD(4), WD(5), WD(6)), NO); G_UNIT(1, sGa + 1024, NO, NO);
        STAMP(47);
        const unsigned da0 = L_A + hh * 14336 + ta0, da1 = L_A + hh * 14336 + ta1;
        const unsigned vc3 = WS_F3 + ((U64 >> 2) * 32 + min(t32, 28u)) * 128, vc4 = vc3 + (WS_F4 - WS_F3);
#define B1 6
#define F4D 0
#define CKS (B1 + 21)
#define WDS(i, m) if ((i) == B1 + 8 + (B1 + 21 <= 31 ? (m) : (m) * (24 - B1) / 13)) WD(7 + (m))
#define P4B_STEP(i) do { constexpr int c = (i) & 3, n = ((i) + 3) & 3; const unsigned o = GA(((i) + 3) & 31) + 64; \
            if ((i) == B1) SSTAMP(48); if ((i) == B1 + 1) SSTAMP(49); if ((i) == B1 + 8) SSTAMP(50); if ((i) == B1 + 9) SSTAMP(51); if ((i) == B1 + 15) SSTAMP(52); if ((i) == B1 + 21) SSTAMP(53); \
            if ((i) == B1) BAR_VM(2);         \
            if ((i) == B1 + 8) BAR_VM(0);        \
            if ((i) == B1 + 8 + F4D) FLAG(w == 4 ? WS_F4 + (g * 32 + r) * 128 : OOB); SBAR(); \
            ag[2] = MFMA32(A2[c], Hb[i], ag[2]); if ((i) < 29) A2[n] = ldtr(o, o + 768); \
            if ((i) >= B1 && (i) < B1 + 7) { constexpr int q = ((i) + 14 - B1) % 7; D0[q] = ldtr(da0 + q * 2048, da0 + q * 2048 + 1024); D1[q] = ldtr(da1 + q * 2048, da1 + q * 2048 + 1024); } SBAR(); \
            WDS(i, 0); WDS(i, 1); WDS(i, 2); WDS(i, 3); WDS(i, 4); WDS(i, 5); WDS(i, 6); WDS(i, 7); WDS(i, 8); WDS(i, 9); WDS(i, 10); WDS(i, 11); WDS(i, 12); \
            if ((i) == CKS) ld8(ck, w == 7 ? vc3 : w == 6 ? vc4 : OOB, rsF, 0); SBAR(); } while (0)
#define P4B_4(i) P4B_STEP(i); P4B_STEP((i) + 1); P4B_STEP((i) + 2); P4B_STEP((i) + 3)
        P4B_4(0); P4B_4(4); P4B_4(8); P4B_4(12); P4B_4(16); P4B_4(20); P4B_4(24); P4B_4(28);
        STAMP(41);
        G_UNIT(2, sGb, NO, NO);
        STAMP(30);
        WAITV1(1, ck);
        if (w >= 6 && UNLIKELY(any_lane(ck[0] != tag[0] || ck[1] != tag[1]))) { const unsigned vc = w == 7 ? vc3 : vc4; POLL(vc); }
    }
    BAR_L();
    STAMP(9);
    {
#define PD5 5
        constexpr int PD = PD5;
        u4 Gb[22], A0[2], A1[2];
        const unsigned gs = WS_G + (2 * U64 + j) * 90112u + 15 * hh * 1024, gz = hh == 3 ? OOB : gs, gt = WS_G + (2 * U64 + j) * 90112u + (60 + 7 * hh - 15) * 1024;
        const unsigned o0 = L_A + hh * 14336 + ta0, o1 = L_A + hh * 14336 + ta1, p0 = rot * 49152 + hh * 12288 + ta0, p1 = p0 - ta0 + ta1, q0 = 49152 - rot * 49152 + hh * 4096 + ta0, q1 = q0 - ta0 + ta1;
#define GOFF(i) (((i) >= 15 ? gt : (i) >= 13 ? gz : gs) + (i) * 1024)
#pragma unroll
        for (int i = 0; i < PD; ++i) ld16(Gb[i], l16, rsF, GOFF(i));
        WD(20); WD(21);
        acc0 = z16; acc1 = z16;
#define P5_STEP(i) do { constexpr int c = (i) & 1, n = c ^ 1; \
            if ((i) + PD < 22) ld16(Gb[(i) + PD < 22 ? (i) + PD : 0], l16, rsF, GOFF((i) + PD)); \
            WAITV1(((i) + PD < 22 ? PD : 21 - (i)) + ((i) < PD ? 2 : 0), Gb[i]); if ((i) >= 15) FRESH16(Gb[i], l16, rsF, GOFF(i)); if ((i) == 0) STAMP(10); \
            if ((i) == 14) BAR_L(); SBAR();        \
            const unsigned a0_ = (i) < 13 ? o0 + ((i) - 6) * 2048 : (i) < 19 ? p0 + ((i) - 13) * 2048 : q0 + ((i) - 19) * 2048, a1_ = (i) < 13 ? o1 + ((i) - 6) * 2048 : (i) < 19 ? p1 + ((i) - 13) * 2048 : q1 + ((i) - 19) * 2048; \
            acc0 = MFMA32((i) < 7 ? D0[(i) < 7 ? (i) : 0] : A0[c], Gb[i], acc0); if ((i) >= 6 && (i) < 21) A0[n] = ldtr(a0_, a0_ + 1024); SBAR(); \
            acc1 = MFMA32((i) < 7 ? D1[(i) < 7 ? (i) : 0] : A1[c], Gb[i], acc1); if ((i) >= 6 && (i) < 21) A1[n] = ldtr(a1_, a1_ + 1024); SBAR(); } while (0)
        P5_STEP(0); P5_STEP(1); P5_STEP(2); P5_STEP(3); P5_STEP(4); P5_STEP(5); P5_STEP(6); P5_STEP(7); P5_STEP(8); P5_STEP(9); P5_STEP(10);
        P5_STEP(11); P5_STEP(12); P5_STEP(13); P5_STEP(14); P5_STEP(15); P5_STEP(16); P5_STEP(17); P5_STEP(18); P5_STEP(19); P5_STEP(20); P5_STEP(21);
    }
    STAMP(11);
    {
        f4 lo, hi; reduce(lo, hi);
        const u4 Ores = {pk2(lo2(lo) + (f2){blo(Hres[0]), bhi(Hres[0])}), pk2(hi2(lo) + (f2){blo(Hres[1]), bhi(Hres[1])}), pk2(lo2(hi) + (f2){blo(Hres[2]), bhi(Hres[2])}), pk2(hi2(hi) + (f2){blo(Hres[3]), bhi(Hres[3])})};
        st16_plain(Ores, vres, mk_rsrc(out + U64 * 65536u, 65536u), 0);
    }
    asm volatile(".Lfb_code1:");
    STAMP(12);
#endif
}

#define WSOFF 0
extern "C" void kernel_launch(void* const* d_in, const int* in_sizes, int n_in, void* d_out, int out_size, void* d_ws, size_t ws_size, hipStream_t stream) {
    (void)in_sizes; (void)n_in; (void)out_size; (void)ws_size;
    (void)hipFuncSetAttribute((const void*)fused_block, hipFuncAttributeMaxDynamicSharedMemorySize, LDS_BYTES);
    fused_block<<<256, 512, LDS_BYTES, stream>>>((const char*)d_in[0], (const char*)d_in[1], (const char*)d_in[2], (const char*)d_in[4], (const char*)d_in[5], (const char*)d_in[6],
                                                 (char*)d_out, (char*)d_ws + WSOFF);
}
#define LASTSTAMP 12
#define STAMP_NAMES "start", "P1 loop end", "KV drained", 0, "O first data", "O drained", "h drained", "h first data", "P4 pass1 end", "P4 end bar", "G first data", "P5 loop end", "end", 0, 0, 0, \
  "st0", "st1", "st2", "st3", "st4", "st5", "st6", "st7", "KV first data", "att done", "O stored", "P3 loop end", "P3 reduced", 0, "G stored", 0, "KV stored", "h stored", \
  "LN sums", "pre KV bar", "colsum done", "A read", 0, "setup done", "GU bar", "P4 pass2 end", "att blk3", "att blk4", "att blk0", "comb bar", 0, "G01 stored", "p2 pre B1", "p2 s7", "p2 pre B2", "p2 s15", "p2 s21", "p2 s27", "h landed", "st landed"
```

```cpp
#define TBPOS 1
#include <hip/hip_runtime.h>
#include <hip/hip_bf16.h>
#include <stdint.h>
#pragma clang diagnostic ignored "-Wint-to-pointer-cast"

typedef __bf16 bf8 __attribute__((ext_vector_type(8)));
typedef __bf16 bf2 __attribute__((ext_vector_type(2)));
typedef short s4 __attribute__((ext_vector_type(4)));
typedef short s8 __attribute__((ext_vector_type(8)));
typedef float f2 __attribute__((ext_vector_type(2)));
typedef float f4 __attribute__((ext_vector_type(4)));
typedef float f16 __attribute__((ext_vector_type(16)));
typedef unsigned u2 __attribute__((ext_vector_type(2)));
typedef unsigned u4 __attribute__((ext_vector_type(4)));
typedef unsigned long long u64;
#define LAS __attribute__((address_space(3)))
#define DEV __device__ __forceinline__

constexpr unsigned WS_F1 = 32768, WS_F2 = 65536, WS_F3 = 98304, WS_F4 = 131072;
#define KVS 132096u
#define OS 33792u
constexpr unsigned WS_KV = 1u << 20;
constexpr unsigned WS_O = WS_KV + 32 * KVS;
constexpr unsigned WS_H = WS_O + 64 * OS;
constexpr unsigned WS_ST = WS_H + 64 * OS;
constexpr unsigned WS_G = WS_ST + 64 * 4096;
static_assert(WS_G + 64 * 90112u <= (16u << 20), "hand-off buffers");
constexpr unsigned WS_DIAG = 32u << 20;
constexpr unsigned LDS_BYTES = 163840;
constexpr unsigned L_HOLE = 73728;
constexpr unsigned L_OC = L_HOLE;
constexpr unsigned L_A = 98304;
constexpr unsigned L_CSP = 155648, L_CF = L_CSP + 4096;
constexpr int SPIN_CAP = 1 << 14;
constexpr unsigned OOB = 0x40000000u;

#define MFMA16(a, b, c) __builtin_amdgcn_mfma_f32_16x16x32_bf16(__builtin_bit_cast(bf8, a), __builtin_bit_cast(bf8, b), c, 0, 0, 0)
#define MFMA32(a, b, c) __builtin_amdgcn_mfma_f32_32x32x16_bf16(__builtin_bit_cast(bf8, a), __builtin_bit_cast(bf8, b), c, 0, 0, 0)
#define SBAR() __builtin_amdgcn_sched_barrier(0)
#define BAR_VM(N) asm volatile("s_waitcnt vmcnt(%0) lgkmcnt(0)\n\ts_barrier" ::"i"(N) : "memory")
#define BAR_L() asm volatile("s_waitcnt lgkmcnt(0)\n\ts_barrier" ::: "memory")

DEV u4 mk_rsrc(const void* p, unsigned bytes) { const u64 a = (u64)p; return (u4){(unsigned)a, (unsigned)(a >> 32) & 0xffffu, bytes, 0x00020000u}; }
DEV void dma_buf(unsigned lds, unsigned voff, u4 rs, unsigned soff) {
    asm volatile("s_mov_b32 m0, %0\n\ts_nop 0\n\tbuffer_load_dwordx4 %1, %2, %3 offen lds" ::"s"(lds), "v"(voff), "s"(rs), "s"(soff) : "memory"); }
template <unsigned LI, unsigned SI> DEV void dma_bufi(unsigned lds, unsigned voff, u4 rs, unsigned soff) {
    unsigned t; asm volatile("s_add_u32 m0, %1, %5\n\ts_add_u32 %0, %4, %6\n\tbuffer_load_dwordx4 %2, %3, %0 offen lds" : "=&s"(t) : "s"(lds), "v"(voff), "s"(rs), "s"(soff), "i"(LI), "i"(SI) : "memory", "scc"); }
template <unsigned LI> DEV void dma_glbi(unsigned lds, const char* p) {
    asm volatile("s_add_u32 m0, %0, %2\n\ts_nop 0\n\tglobal_load_lds_dwordx4 %1, off" ::"s"(lds), "v"(p), "i"(LI) : "memory", "scc"); }
DEV void dma_glb(unsigned lds, const char* p) {
    asm volatile("s_mov_b32 m0, %0\n\ts_nop 0\n\tglobal_load_lds_dwordx4 %1, off" ::"s"(lds), "v"(p) : "memory"); }
DEV void ld16(u4& d, unsigned voff, u4 rs, unsigned soff) {
    asm volatile("buffer_load_dwordx4 %0, %1, %2, %3 offen sc1" : "=v"(d) : "v"(voff), "s"(rs), "s"(soff) : "memory"); }
DEV void ld16_plain(u4& d, unsigned voff, u4 rs, unsigned soff) {
    asm volatile("buffer_load_dwordx4 %0, %1, %2, %3 offen" : "=v"(d) : "v"(voff), "s"(rs), "s"(soff) : "memory"); }
template <int OFF> DEV void ld16_imm(u4& d, unsigned voff, u4 rs) {
    asm volatile("buffer_load_dwordx4 %0, %1, %2, 0 offen offset:%3" : "=v"(d) : "v"(voff), "s"(rs), "i"(OFF) : "memory"); }
DEV void ld8(u2& d, unsigned voff, u4 rs, unsigned soff) {
    asm volatile("buffer_load_dwordx2 %0, %1, %2, %3 offen sc1" : "=v"(d) : "v"(voff), "s"(rs), "s"(soff) : "memory"); }
DEV void ld16_v(u4& d0, u4& d1, unsigned voff, u4 rs, unsigned soff) {
    asm volatile("s_mov_b64 exec, %5\n\tbuffer_load_dwordx4 %0, %2, %3, %4 offen offset:1024 sc1\n\tbuffer_load_dwordx4 %1, %2, %3, %4 offen offset:1536 sc1\n\ts_mov_b64 exec, -1"
                 : "+v"(d0), "+v"(d1) : "v"(voff), "s"(rs), "s"(soff), "s"(0x0000FFFF0000FFFFull) : "memory"); }
DEV void st16(u4 d, unsigned voff, u4 rs, unsigned soff) {
    asm volatile("buffer_store_dwordx4 %0, %1, %2, %3 offen sc1\n\ts_nop 1" ::"v"(d), "v"(voff), "s"(rs), "s"(soff) : "memory"); }
DEV void st16_plain(u4 d, unsigned voff, u4 rs, unsigned soff) {
    asm volatile("buffer_store_dwordx4 %0, %1, %2, %3 offen\n\ts_nop 1" ::"v"(d), "v"(voff), "s"(rs), "s"(soff) : "memory"); }
DEV void st8(u2 d, unsigned voff, u4 rs, unsigned soff) {
    asm volatile("buffer_store_dwordx2 %0, %1, %2, %3 offen sc1\n\ts_nop 1" ::"v"(d), "v"(voff), "s"(rs), "s"(soff) : "memory"); }
#define WAITV1(N, a) asm volatile("s_waitcnt vmcnt(%1)" : "+v"(a) : "i"(N) : "memory")
#define WAITV1D(ND, NS, a) asm volatile("s_waitcnt vmcnt(%1)\n\ts_cmp_lt_u32 %3, 4\n\ts_cbranch_scc1 .Lw%=\n\ts_waitcnt vmcnt(%2)\n.Lw%=:" : "+v"(a) : "i"(ND), "i"(NS), "s"(w) : "memory", "scc")
#define WAITV3(N, a, b, c) asm volatile("s_waitcnt vmcnt(%3)" : "+v"(a), "+v"(b), "+v"(c) : "i"(N) : "memory")
#define WAITV4(N, a, b, c, d) asm volatile("s_waitcnt vmcnt(%4)" : "+v"(a), "+v"(b), "+v"(c), "+v"(d) : "i"(N) : "memory")

DEV u4 lds16(unsigned a) { return *(LAS const u4*)a; }
DEV f4 lds16f(unsigned a) { return *(LAS const f4*)a; }
DEV float lds4f(unsigned a) { return *(LAS const float*)a; }
DEV void sts16(unsigned a, u4 v) { *(LAS u4*)a = v; }
DEV void sts16f(unsigned a, f4 v) { *(LAS f4*)a = v; }
DEV void sts8f(unsigned a, f2 v) { *(LAS f2*)a = v; }
DEV void sts4f(unsigned a, float v) { *(LAS float*)a = v; }
DEV u4 ldtr(unsigned a0, unsigned a1) {
    const s4 lo = __builtin_amdgcn_ds_read_tr16_b64_v4i16((LAS s4*)a0), hi = __builtin_amdgcn_ds_read_tr16_b64_v4i16((LAS s4*)a1);
    return __builtin_bit_cast(u4, __builtin_shufflevector(lo, hi, 0, 1, 2, 3, 4, 5, 6, 7)); }
DEV unsigned pk2(f2 a) { return __builtin_bit_cast(unsigned, __builtin_convertvector(a, bf2)); }
DEV f2 lo2(f4 a) { return (f2){a[0], a[1]}; }
DEV f2 hi2(f4 a) { return (f2){a[2], a[3]}; }
DEV unsigned pk(float a, float b) { return __builtin_bit_cast(unsigned, __builtin_convertvector((f2){a, b}, bf2)); }
DEV float blo(unsigned v) { return __builtin_bit_cast(float, v << 16); }
DEV float bhi(unsigned v) { return __builtin_bit_cast(float, v & 0xffff0000u); }
DEV float fu(unsigned v) { return __builtin_bit_cast(float, v); }
DEV unsigned uf(float v) { return __builtin_bit_cast(unsigned, v); }
DEV float add_x32(float v) { auto r = __builtin_amdgcn_permlane32_swap(uf(v), uf(v), false, false); const unsigned a = r[0], b = r[1]; return fu(a) + fu(b); }
DEV float add_x16(float v) { auto r = __builtin_amdgcn_permlane16_swap(uf(v), uf(v), false, false); const unsigned a = r[0], b = r[1]; return fu(a) + fu(b); }
DEV float max_x32(float v) { auto r = __builtin_amdgcn_permlane32_swap(uf(v), uf(v), false, false); const unsigned a = r[0], b = r[1]; return __builtin_fmaxf(fu(a), fu(b)); }
template <int C> DEV float add_dpp(float v) { return v + fu(__builtin_amdgcn_update_dpp(0u, uf(v), C, 0xf, 0xf, true)); }
DEV float add_x8(float v) { return v + fu(__builtin_amdgcn_update_dpp(0u, uf(v), 0x128, 0xf, 0xf, true)); }
constexpr u64 diag_mask(int r) { u64 m = 0; for (int l = 0; l < 64; ++l) { const int key = 16 * ((r >> 2) & 1) + 8 * (l >> 5) + 4 * (r >> 3) + (r & 3); if (key <= (l & 31)) m |= 1ull << l; } return m; }
DEV float sel(u64 m, float t, float f) { return __builtin_amdgcn_inverse_ballot_w64(m) ? t : f; }

template <unsigned LDS> DEV void touch(unsigned voff, u4 rs, unsigned soff) {
    asm volatile("s_mov_b32 m0, %3\n\ts_nop 0\n\tbuffer_load_dword %0, %1, %2 offen lds" ::"v"(voff), "s"(rs), "s"(soff), "i"(LDS) : "memory"); }
typedef short sh2 __attribute__((ext_vector_type(2)));
constexpr int kgo(int i) { return i < 8 ? 4 * i : (i - 8) / 3 * 4 + (i - 8) % 3 + 1; }
#define RSLEEP 4
#define SL1 0
#define SL2 0
#define SL3 0
#define IDLE(r) ((r) > 28)
DEV bool any_lane(bool c) { return __builtin_amdgcn_ballot_w64(c) != 0; }
#define UNLIKELY(x) __builtin_expect(!!(x), 0)
#define NRETRY()
#define FRESH16(d, voff, rs, soff) do { if (UNLIKELY(any_lane((d)[3] == ~0u))) { \
        _Pragma("unroll 1") for (int s_ = 0; s_ < SPIN_CAP; ++s_) { NRETRY(); u4 t_; asm volatile("buffer_load_dwordx4 %0, %1, %2, %3 offen sc1\n\ts_waitcnt vmcnt(0)" : "=&v"(t_) : "v"(voff), "s"(rs), "s"(soff) : "memory"); \
            d = t_; if (!any_lane(t_[3] == ~0u)) break; __builtin_amdgcn_s_sleep(2); } } } while (0)
#define POLL(voff) do { \
        _Pragma("unroll 1") for (int s_ = 0; s_ < 4 * SPIN_CAP; ++s_) { u2 t_; asm volatile("buffer_load_dwordx2 %0, %1, %2, 0 offen sc1\n\ts_waitcnt vmcnt(0)" : "=&v"(t_) : "v"(voff), "s"(rsF) : "memory"); \
            if (!any_lane(t_[0] != tag[0] || t_[1] != tag[1])) break; NRETRY(); __builtin_amdgcn_s_sleep(RSLEEP); } } while (0)
#define FLAG(off) st8(tag, l == 0 ? (off) : OOB, rsF, 0)
#define STAMP(i)

__global__ __launch_bounds__(512, 2) void fused_block(const char* x_, const char* wqkv_, const char* wo_, const char* wg_, const char* wu_, const char* wd_, char* out_, char* ws_) {
#if defined(__HIP_DEVICE_COMPILE__)

    typedef u64 u64x8 __attribute__((ext_vector_type(8)));
    u64x8 ka; asm volatile("s_load_dwordx16 %0, %1, 0x0\n\ts_waitcnt lgkmcnt(0)" : "=s"(ka) : "s"(__builtin_amdgcn_kernarg_segment_ptr()) : "memory");

    const char *x = (const char*)ka[0], *wqkv = (const char*)ka[1], *wo = (const char*)ka[2], *wg = (const char*)ka[3], *wu = (const char*)ka[4], *wd = (const char*)ka[5]; char *out = (char*)ka[6], *ws = (char*)ka[7];
    const unsigned l = threadIdx.x & 63, w = __builtin_amdgcn_readfirstlane(threadIdx.x >> 6);
    const unsigned b = blockIdx.x, xc = b & 7, rk = b >> 3;
    const unsigned T64 = 8 * (xc >> 1) + (rk >> 2), hq = 4 * (xc & 1) + (rk & 3);
    STAMP(0);
    const unsigned u8 = l >> 3, l7 = l & 7;
    u4 xr[8];
    const u4 rsX = mk_rsrc(x + T64 * 65536u, 65536u);
    const unsigned vx = (8 * w + u8) * 1024 + l7 * 16;
#define CF 0
    const char* pc; unsigned nb; asm volatile("s_getpc_b64 %0\n.Lfb_code0:\n\ts_mov_b32 %1, .Lfb_code1-.Lfb_code0" : "=s"(pc), "=s"(nb));
#define TOUCH_A(CODE, WQ) do { const unsigned co = ((rk * 8 + w) * 2 + (l & 1)) * 128; \
        const char* p = (WQ && (!CODE || l < 32)) ? wqkv + ((b * 8 + w) * 6 + (l < 6 ? l : 0)) * 128 : pc + (co < nb ? co : 0); \
        asm volatile("global_load_dword v254, %0, off" ::"v"(p) : "memory", "v254"); } while (0)
    if (CF == 1) TOUCH_A(1, 0);
    if (CF == 2) TOUCH_A(1, 1);
#define HOT 7
    {
        const unsigned hs = (3u - (unsigned)((u64)wqkv >> 7)) & 7u;
        if ((hs >> 2) == (xc & 1) && w == HOT) { const unsigned z = rk * 48 + (l < 48 ? l : 0); const char* p = wqkv + (hs * 128 + z * 1024); asm volatile("global_load_dword v254, %0, off" ::"v"(p) : "memory", "v254"); } }
    ld16_imm<0>(xr[0], vx, rsX); ld16_imm<128>(xr[1], vx, rsX); ld16_imm<256>(xr[2], vx, rsX); ld16_imm<384>(xr[3], vx, rsX);
#define XB 6
    if (XB > 4) { ld16_imm<512>(xr[4], vx, rsX); ld16_imm<640>(xr[5], vx, rsX); }
    if (XB > 6) { ld16_imm<768>(xr[6], vx, rsX); ld16_imm<896>(xr[7], vx, rsX); }
    const u4 rsW = mk_rsrc(wqkv + hq * 128u, 512u * 3072u - hq * 128u);
    SBAR();
    const unsigned w3 = w & 3, wk2 = w3 * 2048;
    unsigned vw0 = (16 * w3 + u8) * 3072 + 32 * ((l7 >> 1) ^ ((l >> 4) & 1)) + 16 * (l7 & 1), vw1 = (vw0 + 24576) ^ 64;
#define SLOT(i) ((((i) + 1) & 3) * 24576u)
#define SP(i, p) dma_bufi<SLOT(i) + ((p) >> 1) * 8192 + ((p) & 1) * 1024, (i) * 196608u + ((p) >> 1) * 1024u>(wk2, ((p) & 1) ? vw1 : vw0, rsW, 0)
    const unsigned U64 = 4 * xc + (rk >> 3), fb = rk & 7;
    const unsigned g = 2 * (xc >> 1) + (rk >> 4), r = 16 * (xc & 1) + (rk & 15);
    const unsigned j = w & 1, hh = w >> 1;
    const unsigned t16 = l & 15, g4 = l >> 4, h = l >> 5, t32 = l & 31, q4 = t16 >> 2, pp = l & 3, l16 = l * 16, l128 = l * 128;
    const u4 rsF = mk_rsrc(ws, 16u << 20);
    const u4 ones = {0x3f803f80u, 0x3f803f80u, 0x3f803f80u, 0x3f803f80u};
    const unsigned kvb = WS_KV + (4 * hq + hh) * KVS;
    const unsigned skv = kvb + (2 * T64 + j) * 2048;
    const unsigned sOw = WS_O + (2 * T64 + j) * OS + (4 * hq + hh) * 1024;
    u2 tag; u4 xres;
    unsigned vres = (32 * j + 8 * hh + l7) * 1024 + fb * 128 + u8 * 16;
    unsigned xw0 = L_A + (8 * w + u8) * 1024 + 16 * (l7 ^ (8 * (w & 1) + u8)), xw1 = xw0 ^ 128;
    unsigned xa[4];
#pragma unroll
    for (int q = 0; q < 4; ++q) { xa[q] = L_A + (32 * j + t16) * 1024 + 16 * ((4 * q + g4) ^ t16); asm volatile("" : "+v"(xa[q])); }
    unsigned wa = (8 * g4 + q4) * 128 + 32 * (hh ^ ((q4 >> 1) | ((g4 & 1) << 1))) + 8 * pp;
    f4 aq[2] = {}, ak[2] = {}, av[2] = {};
    unsigned vk = (h * 32 + (16 * ((t32 >> 3) & 1) + 8 * ((t32 >> 2) & 1) + 4 * (t32 >> 4) + (t32 & 3))) * 16, vv_ = (h * 16 + t16) * 16;
    unsigned vst = 1024 + ((g4 & 1) * 32 + (g4 >> 1) * 16 + t16) * 16;
    unsigned vrow = u8 * 1024 + 16 * (l7 ^ (((l >> 4) & 1) << 2));
    const char* gp[3];
#pragma unroll
    for (int i = 0; i < 3; ++i) { const unsigned z = 64 * i + l, k = (z * 43691u) >> 19, c = z - 12 * k, m = c >= 6, cc = c - 6 * m; unsigned un = 3 * r + (cc >> 1); un = un > 85 ? 85 : un;
        gp[i] = (m ? wu : wg) + (IDLE(r) ? 0 : (16 * w + k) * 2752 + un * 32 + (cc & 1) * 16); asm volatile("" : "+v"(gp[i])); }
    const unsigned wk3 = w * 3072;
#define GU(R, c) dma_glbi<(R) * 24576 + (c) * 1024>(wk3, gp[c] + (R) * 128 * 2752)
#define GUD(R, q) dma_glbi<(R) * 24576 + ((q) / 3) * 12288 + ((q) % 3) * 1024>(wk3, gp[(q) % 3] + ((R) * 128 + ((q) / 3) * 64) * 2752)
    asm volatile("" : "+v"(vw0), "+v"(vw1), "+v"(xw0), "+v"(xw1), "+v"(wa), "+v"(vres), "+v"(vk), "+v"(vv_), "+v"(vrow), "+v"(vst));
    STAMP(39);
    if (XB <= 4) { ld16_imm<512>(xr[4], vx, rsX); ld16_imm<640>(xr[5], vx, rsX); }
    if (XB <= 6) { ld16_imm<768>(xr[6], vx, rsX); ld16_imm<896>(xr[7], vx, rsX); }
    if (CF == 0) TOUCH_A(1, 1);
    if (CF == 1) TOUCH_A(0, 1);
#define XA (CF != 2)
#define NO ((void)0)
#define HOTB(wv, base, n) do { if (w == (wv)) { const char* p_ = base + ((((3u - (unsigned)((u64)base >> 7)) & 7u) * 128) + (rk * (n) + (l < (n) ? l : 0)) * 1024); asm volatile("global_load_dword v254, %0, off" ::"v"(p_) : "memory", "v254"); } } while (0)
#define TB3 0
#define TOUCH_B() do { const char* tp = (w == 4 ? wo : w == 5 ? wg : w == 6 ? wu : wd) + (w == 4 ? b * 16 + (l & 15) : b * 43 + (l < 43 ? l : 0)) * 128; \
        asm volatile("global_load_dword v255, %0, off" ::"v"(tp) : "memory", "v255"); } while (0)
#define TOUCH_W(base, n) do { const char* tp = base + (b * (n) + (l < (n) ? l : 0)) * 128; asm volatile("global_load_dword v255, %0, off" ::"v"(tp) : "memory", "v255"); } while (0)
#define XW 0
#define SSTAMP(i)
    {
        f2 xf[32], s1 = {0.f, 0.f}, s2 = {0.f, 0.f};
#define LW 0
#define TWD() NO
#define LN_SUM(i, N, D0, D1) do { if (XW && (N) < 8 - (i)) WAITV1((N) + XA, xr[i]); else WAITV1D((N) + XA, 7 + XA - (i) + XW - LW, xr[i]); _Pragma("unroll") for (int e = 0; e < 4; ++e) { const unsigned d_ = xr[i][e]; f2 v_ = {blo(d_), bhi(d_)}; asm volatile("" : "+v"(v_)); xf[4 * (i) + e] = v_; \
        s1 += v_; s2 += v_ * v_; } asm volatile("" : "+v"(s1), "+v"(s2)); if (w < 4) { D0; D1; } SBAR(); } while (0)
        LN_SUM(0, 7, NO, NO); LN_SUM(1, 6, NO, NO); LN_SUM(2, 5, NO, NO); LN_SUM(3, 4, NO, NO);
        LN_SUM(4, 3, SP(0, 0), SP(0, 1)); LN_SUM(5, 4, SP(0, 2), SP(0, 3)); LN_SUM(6, 5, SP(0, 4), NO); LN_SUM(7, 5, SP(0, 5), NO);
        SSTAMP(34);
        if (w < 4) { SP(1, 0); SP(1, 1); SP(1, 2); SP(1, 3); SP(1, 4); SP(1, 5); }
        else TOUCH_B();
        const float t1 = add_dpp<0x141>(add_dpp<0x4e>(add_dpp<0xb1>(s1[0] + s1[1]))), t2 = add_dpp<0x141>(add_dpp<0x4e>(add_dpp<0xb1>(s2[0] + s2[1])));
        const float mu = t1 * (1.f / 512), rs = __builtin_amdgcn_rsqf(__builtin_fmaxf(t2 * (1.f / 512) - mu * mu, 0.f) + 1e-5f), mr = -mu * rs;
#pragma unroll
        for (int i = 0; i < 8; ++i) sts16(((i & 1) ? xw1 : xw0) + (i >> 1) * 256, (u4){pk2(xf[4 * i] * rs + mr), pk2(xf[4 * i + 1] * rs + mr), pk2(xf[4 * i + 2] * rs + mr), pk2(xf[4 * i + 3] * rs + mr)});
    }
#define P1_STEP(n, D0, D1, D2) do { constexpr int c_ = (n) & 1, x_ = c_ ^ 1; constexpr bool m_ = (n) < 15; \
        const unsigned xb = xa[((n) + 1) & 3] + (((n) + 1) >> 2) * 256, wb = wa + SLOT(((n) + 1) >> 1) + x_ * 4096; \
        if (m_) X0[x_] = lds16(xb); SBAR(); \
        aq[0] = MFMA16(Wq[c_], X0[c_], aq[0]); if (m_) Wq[x_] = ldtr(wb, wb + 512); SBAR(); \
        aq[1] = MFMA16(Wq[c_], X1[c_], aq[1]); if (m_) X1[x_] = lds16(xb + 16384); D0; SBAR(); \
        ak[0] = MFMA16(Wk[c_], X0[c_], ak[0]); if (m_) Wk[x_] = ldtr(wb + 8192, wb + 8704); SBAR(); \
        ak[1] = MFMA16(Wk[c_], X1[c_], ak[1]); D1; SBAR(); \
        av[0] = MFMA16(X0[c_], Wv[c_], av[0]); if (m_) Wv[x_] = ldtr(wb + 16384, wb + 16896); SBAR(); \
        av[1] = MFMA16(X1[c_], Wv[c_], av[1]); D2; SBAR(); } while (0)
#define P1_FIRST() do { const unsigned wb = wa + SLOT(0); X0[0] = lds16(xa[0]); Wq[0] = ldtr(wb, wb + 512); X1[0] = lds16(xa[0] + 16384); Wk[0] = ldtr(wb + 8192, wb + 8704); Wv[0] = ldtr(wb + 16384, wb + 16896); } while (0)
#define XRES() ld16_plain(xres, vres, mk_rsrc(x + U64 * 65536u, 65536u), 0)
    if (w < 4) {
        u4 X0[2], X1[2], Wq[2], Wk[2], Wv[2];
        SP(2, 0); SP(2, 1); SP(2, 2); SP(2, 3); SP(2, 4); SP(2, 5);
        BAR_VM(12); STAMP(16);  P1_FIRST(); P1_STEP(0, (SP(3, 0), SP(3, 1)), (SP(3, 2), SP(3, 3)), (SP(3, 4), SP(3, 5)));
        BAR_VM(12); SSTAMP(17); P1_STEP(1, SP(4, 0), SP(4, 1), SP(4, 2)); P1_STEP(2, SP(4, 3), SP(4, 4), SP(4, 5));
        BAR_VM(12); SSTAMP(18); P1_STEP(3, SP(5, 0), SP(5, 1), SP(5, 2)); P1_STEP(4, SP(5, 3), SP(5, 4), SP(5, 5));
        BAR_VM(12); SSTAMP(19); P1_STEP(5, SP(6, 0), SP(6, 1), SP(6, 2)); P1_STEP(6, SP(6, 3), SP(6, 4), SP(6, 5));
        BAR_VM(12); SSTAMP(20); P1_STEP(7, SP(7, 0), SP(7, 1), SP(7, 2)); P1_STEP(8, SP(7, 3), SP(7, 4), SP(7, 5));
        BAR_VM(12); SSTAMP(21); P1_STEP(9, XRES(), GUD(1, 0), NO); P1_STEP(10, GUD(1, 1), GUD(1, 2), NO);
        BAR_VM(10); SSTAMP(22); P1_STEP(11, GUD(1, 3), GUD(1, 4), NO); P1_STEP(12, GUD(1, 5), NO, NO);
        BAR_VM(7);  SSTAMP(23); P1_STEP(13, NO, NO, NO); P1_STEP(14, NO, NO, NO); P1_STEP(15, NO, NO, NO);
        WAITV1(6, xres); tag = (u2){0u, 0u};
    } else {
        u4 X0[2], X1[2], Wq[2], Wk[2], Wv[2];
        const u4 bad = {~0u, ~0u, ~0u, ~0u};
#define MK(v, s_) st16(bad, v, rsF, s_)
#define TCH(c) NO
        BAR_L(); STAMP(16);  P1_FIRST(); P1_STEP(0, ld8(tag, 0u * l, rsF, 0), MK(l16, skv), TCH(5));
        BAR_L(); SSTAMP(17); P1_STEP(1, NO, MK(l16 + 1024, skv), TCH(6)); P1_STEP(2, NO, MK(l16, skv - 2 * KVS), NO);
        BAR_L(); SSTAMP(18); P1_STEP(3, NO, MK(l16 + 1024, skv - 2 * KVS), TCH(4)); P1_STEP(4, NO, MK(l16, sOw), NO);
        BAR_L(); SSTAMP(19); P1_STEP(5, NO, MK(l16, sOw - 2048), TCH(7)); P1_STEP(6, XRES(), NO, NO);
        BAR_L(); SSTAMP(20); P1_STEP(7, NO, NO, NO); P1_STEP(8, NO, NO, NO);
        BAR_L(); SSTAMP(21); P1_STEP(9, NO, NO, NO); P1_STEP(10, NO, NO, NO);
        asm volatile("s_waitcnt vmcnt(0)" : "+v"(tag), "+v"(xres) :: "memory");
        BAR_L(); SSTAMP(22);
        { const unsigned lo = tag.x + 1u; tag.y += (lo == 0u); tag.x = lo; }
                             P1_STEP(11, FLAG(w == 4 ? WS_F1 + (T64 * 8 + hq) * 128 : OOB), NO, NO); P1_STEP(12, HOTB(6, wo, 16), NO, NO);
        BAR_L(); SSTAMP(23); P1_STEP(13, NO, NO, NO); P1_STEP(14, NO, NO, NO); P1_STEP(15, NO, NO, NO);
    }
    STAMP(1);
    u4 Qf;
    {
        { auto a = __builtin_amdgcn_permlane16_swap(pk2(lo2(ak[0])), pk2(lo2(ak[1])), false, false); auto c = __builtin_amdgcn_permlane16_swap(pk2(hi2(ak[0])), pk2(hi2(ak[1])), false, false);
          st16((u4){a[0], c[0], a[1], c[1]}, l16, rsF, skv); }
        st16((u4){pk2(lo2(av[0])), pk2(hi2(av[0])), pk2(lo2(av[1])), pk2(hi2(av[1]))}, vst, rsF, skv);
        STAMP(32);
        SBAR();
        constexpr float QS = 0.25f * 1.4426950408889634f;
        { auto a = __builtin_amdgcn_permlane16_swap(pk2(lo2(aq[0]) * QS), pk2(lo2(aq[1]) * QS), false, false); auto c = __builtin_amdgcn_permlane16_swap(pk2(hi2(aq[0]) * QS), pk2(hi2(aq[1]) * QS), false, false);
          Qf = (u4){a[0], c[0], a[1], c[1]}; }
        sts16(L_OC + (w ^ 1) * 2304 + l16, Qf);
    }
    const int Tb = 2 * (int)T64 - 8;
    u4 K[5], V0[5], V1[5];
    const int blk[5] = {Tb + 8 + (int)j, Tb + (int)j, Tb + 2 + 3 * (int)j, Tb + 3 + 3 * (int)j, Tb + 4 + 3 * (int)j};
#pragma unroll
    for (int i = 0; i < 5; ++i) { const unsigned o = blk[i] < 0 ? 0u : 0x3f803f80u; V0[i] = (u4){o, o, o, o}; V1[i] = V0[i]; asm volatile("" : "+v"(V0[i]), "+v"(V1[i])); }
    unsigned pm[8];
    { const short uq = (short)(t32 - 8 * h);
#pragma unroll
      for (int i = 0; i < 8; ++i) { const short c = (short)(16 * ((i >> 1) & 1) + 4 * (i >> 2) + 2 * (i & 1)); pm[i] = __builtin_bit_cast(unsigned, ((sh2){uq, uq} - (sh2){c, (short)(c + 1)}) >> (sh2){15, 15}); asm volatile("" : "+v"(pm[i])); } }
    unsigned so[5];
#pragma unroll
    for (int i = 0; i < 5; ++i) so[i] = blk[i] < 0 ? OOB : kvb + (unsigned)blk[i] * 2048;
    if (w == 7) {
        const int Tp = (int)T64 - 1 - (int)(l & 3); const unsigned vc = WS_F1 + ((Tp < 0 ? T64 : (unsigned)Tp) * 8 + hq) * 128; POLL(vc); }
    STAMP(35);
    BAR_VM(0);
    STAMP(2);
#define KVLD(i) do { ld16(K[i], vk, rsF, so[i]); ld16_v(V0[i], V1[i], vv_, rsF, so[i]); } while (0)
#define KVOK(N, i) do { WAITV3(N, K[i], V0[i], V1[i]); \
        if (UNLIKELY(any_lane(max(max(K[i][3], V0[i][3]), V1[i][3]) == ~0u))) { \
            _Pragma("unroll 1") for (int s_ = 0; s_ < SPIN_CAP; ++s_) { NRETRY(); KVLD(i); WAITV3(0, K[i], V0[i], V1[i]); if (!any_lane(max(max(K[i][3], V0[i][3]), V1[i][3]) == ~0u)) break; __builtin_amdgcn_s_sleep(2); } } } while (0)
    if (SL1) __builtin_amdgcn_s_sleep(SL1);
    KVLD(2); KVLD(3);
    const u4 Qm = Qf, Qo = lds16(L_OC + w * 2304 + l16);
    f16 Om = {}, Oo = {};
    const f16 z16 = {};
    auto pv_tile = [&](const f16& S, const u4& va, const u4& vb, f16& O) {
        float p[16];
#pragma unroll
        for (int e = 0; e < 16; ++e) p[e] = __builtin_amdgcn_exp2f(S[e]);
        const u4 P0 = {pk(p[0], p[1]), pk(p[2], p[3]), pk(p[4], p[5]), pk(p[6], p[7])}, P1 = {pk(p[8], p[9]), pk(p[10], p[11]), pk(p[12], p[13]), pk(p[14], p[15])};
        O = MFMA32(va, P0, O); O = MFMA32(vb, P1, O);
    };
    const u4 rsWo = mk_rsrc(wo + fb * 128u, 512u * 1024u - fb * 128u);
    const unsigned wol = L_A + w * 8192, wos = w * 65536;
    const unsigned vh = (u8 >> 1) * 1024 + ((u8 & 1) * 32 + 8 * hh + l7) * 16, vs = l < 8 ? (8 * hh + l) * 16 : OOB;
    const unsigned sHw = WS_H + (2 * U64 + j) * OS + fb * 4096, sSw = WS_ST + (2 * U64 + j) * 4096u + fb * 512;
    const unsigned sGa = r < 29 ? WS_G + (8 * g + w) * 90112u + r * 2048 : OOB, sGb = r < 28 ? WS_G + (8 * g + w) * 90112u + (60 + r) * 1024 : OOB;
#define WO_(i) dma_bufi<(i) * 1024, (i) * 8192>(wol, vrow, rsWo, wos)
#define PIN16(a) asm volatile("" : "+v"(a))
#define MEM(...) do { SBAR(); __VA_ARGS__; SBAR(); } while (0)
#define HALF(S, o, va, O) do { float p_[8]; _Pragma("unroll") for (int e = 0; e < 8; ++e) p_[e] = __builtin_amdgcn_exp2f(S[(o) + e]); \
        u4 q_ = {pk(p_[0], p_[1]), pk(p_[2], p_[3]), pk(p_[4], p_[5]), pk(p_[6], p_[7])}; asm volatile("" : "+v"(q_)); O = MFMA32(va, q_, O); PIN16(O); SBAR(); } while (0)
#define SCORE(S, k, q) do { S = MFMA32(k, q, z16); PIN16(S); SBAR(); } while (0)
    f16 Sa, Sb, Sc;
    u2 ck; const unsigned vc1 = WS_F1 + (U64 * 8 + l7) * 128;
    const u4 bad = {~0u, ~0u, ~0u, ~0u};
    KVOK(3, 2);
    STAMP(24);
    SCORE(Sa, K[2], Qm); SCORE(Sb, K[2], Qo); MEM(KVLD(4));
    HALF(Sa, 0, V0[2], Om); MEM(KVLD(0)); HALF(Sa, 8, V1[2], Om); MEM(KVLD(1)); KVOK(9, 3); TWD();
    SCORE(Sa, K[3], Qm); HALF(Sb, 0, V0[2], Oo); MEM(WO_(0)); HALF(Sb, 8, V1[2], Oo); MEM(WO_(1));
    SSTAMP(42);
    SCORE(Sb, K[3], Qo); HALF(Sa, 0, V0[3], Om); MEM(WO_(2)); HALF(Sa, 8, V1[3], Om); MEM(WO_(3)); KVOK(10, 4);
    SCORE(Sa, K[4], Qm); HALF(Sb, 0, V0[3], Oo); MEM(WO_(4)); HALF(Sb, 8, V1[3], Oo); MEM(WO_(5));
    SSTAMP(43);
    SCORE(Sb, K[4], Qo); HALF(Sa, 0, V0[4], Om); MEM(WO_(6)); HALF(Sa, 8, V1[4], Om); MEM(WO_(7)); KVOK(11, 0); KVOK(8, 1);
    if (j == 0) { SCORE(Sa, K[0], Qo); HALF(Sb, 0, V0[4], Oo); MEM(st16(bad, vh, rsF, sHw)); HALF(Sb, 8, V1[4], Oo); MEM(st16(bad, vs, rsF, sSw), st16(bad, l16, rsF, sGb));
                  SCORE(Sb, K[0], Qm); SCORE(Sc, K[1], Qm); HALF(Sa, 0, V0[0], Oo); HALF(Sa, 8, V1[0], Oo); }
    else        { SCORE(Sa, K[1], Qo); HALF(Sb, 0, V0[4], Oo); MEM(st16(bad, vh, rsF, sHw)); HALF(Sb, 8, V1[4], Oo); MEM(st16(bad, vs, rsF, sSw), st16(bad, l16, rsF, sGb));
                  SCORE(Sb, K[0], Qm); SCORE(Sc, K[1], Qm); HALF(Sa, 0, V0[1], Oo); HALF(Sa, 8, V1[1], Oo); }
    SSTAMP(44);
    MEM(ld8(ck, w == 7 ? vc1 : OOB, rsF, 0), GU(2, 0), GU(2, 1), GU(2, 2));
    const unsigned oc = L_OC + w * 2304;
    {
        const f16 Sx = Sb, Sy = Sc;
        float p[16];
#pragma unroll
        for (int e = 0; e < 8; ++e) p[e] = __builtin_amdgcn_exp2f(sel(diag_mask(e), Sx[e], Sy[e]));
        SBAR();
        sts16f(oc + l16, (f4){Oo[0], Oo[1], Oo[2], Oo[3]}); sts16f(oc + 1024 + l16, (f4){Oo[4], Oo[5], Oo[6], Oo[7]}); sts4f(oc + 2048 + l * 4, Oo[8]);
#pragma unroll
        for (int e = 8; e < 16; ++e) p[e] = __builtin_amdgcn_exp2f(sel(diag_mask(e), Sx[e], Sy[e]));
        unsigned P[8];
#pragma unroll
        for (int e = 0; e < 8; ++e) P[e] = pk(p[2 * e], p[2 * e + 1]);
        const u4 Y0 = {P[0] & pm[0], P[1] & pm[1], P[2] & pm[2], P[3] & pm[3]}, Y1 = {P[4] & pm[4], P[5] & pm[5], P[6] & pm[6], P[7] & pm[7]};
        const u4 X0_ = {P[0] ^ Y0[0], P[1] ^ Y0[1], P[2] ^ Y0[2], P[3] ^ Y0[3]}, X1_ = {P[4] ^ Y1[0], P[5] ^ Y1[1], P[6] ^ Y1[2], P[7] ^ Y1[3]};
        Om = MFMA32(V0[0], X0_, Om); Om = MFMA32(V1[0], X1_, Om); Om = MFMA32(V0[1], Y0, Om); Om = MFMA32(V1[1], Y1, Om);
    }
    SBAR();
    STAMP(25);
    asm volatile("s_waitcnt vmcnt(3) lgkmcnt(0)\n\ts_barrier" : "+v"(ck) :: "memory");
    SSTAMP(45);
    const unsigned op = L_OC + (w ^ 1) * 2304;
    f4 oa = lds16f(op + l16) + (f4){Om[0], Om[1], Om[2], Om[3]}, ob = lds16f(op + 1024 + l16) + (f4){Om[4], Om[5], Om[6], Om[7]};
    float den = Om[8] + lds4f(op + 2048 + l * 4);
    if (UNLIKELY(any_lane(!(den >= 0x1p-60f && den <= 0x1p60f)))) {
        f16 O = {}; float m = -__builtin_inff();
#pragma unroll 1
        for (int kb = 8; kb >= 0; --kb) {
            const int T = 2 * (int)T64 + (int)j - 8 + kb;
            if (T < 0) break;
            u4 Ks, Va = ones, Vb = ones;
            ld16(Ks, vk, rsF, kvb + (unsigned)T * 2048); ld16_v(Va, Vb, vv_, rsF, kvb + (unsigned)T * 2048);
            WAITV3(0, Ks, Va, Vb);
            f16 S = MFMA32(Ks, Qm, z16);
            const int up = kb == 8 ? (int)(t32 - 8 * h) : 99, dn = kb == 0 ? (int)(t32 - 8 * h) : -99;
            float mx = m;
#pragma unroll
            for (int e = 0; e < 16; ++e) { const int c = 16 * ((e >> 2) & 1) + 4 * (e >> 3) + (e & 3); S[e] = (c <= up && c > dn) ? S[e] : -__builtin_inff(); mx = __builtin_fmaxf(mx, S[e]); }
            mx = max_x32(mx);
            const float al = __builtin_amdgcn_exp2f(m - mx); m = mx;
#pragma unroll
            for (int e = 0; e < 16; ++e) { O[e] *= al; S[e] -= mx; }
            pv_tile(S, Va, Vb, O);
        }
        oa = (f4){O[0], O[1], O[2], O[3]}; ob = (f4){O[4], O[5], O[6], O[7]}; den = O[8];
    }
    const float rd = __builtin_amdgcn_rcpf(den);
    st16((u4){pk2(lo2(oa) * rd), pk2(hi2(oa) * rd), pk2(lo2(ob) * rd), pk2(hi2(ob) * rd)}, l16, rsF, sOw);
#define F23() FLAG(w == 4 ? WS_F2 + (U64 * 8 + fb) * 128 : w == 5 ? WS_F3 + (g * 32 + r) * 128 : OOB)
    F23();
    STAMP(26);
    const unsigned ta0 = (4 * h + q4) * 128 + 64 * (q4 >> 1) + 32 * (g4 & 1) + 8 * pp, ta1 = ta0 ^ 64;
    f16 acc0 = {}, acc1 = {};
    {
        const unsigned sO = WS_O + (2 * U64 + j) * OS + hh * 8192;
        const unsigned oa0 = L_A + hh * 16384 + ta0, oa1 = L_A + hh * 16384 + ta1;
        u4 A0[8], A1[8], B[8];
#pragma unroll
        for (int i = 0; i < 8; ++i) { A0[i] = ldtr(oa0 + i * 2048, oa0 + i * 2048 + 1024); A1[i] = ldtr(oa1 + i * 2048, oa1 + i * 2048 + 1024); }
        if (w == 7 && UNLIKELY(any_lane(ck[0] != tag[0] || ck[1] != tag[1]))) POLL(vc1);
        STAMP(37);
        BAR_VM(1);
        STAMP(5);
        if (SL2) __builtin_amdgcn_s_sleep(SL2);
#pragma unroll
        for (int i = 0; i < 8; ++i) ld16(B[i], l16, rsF, sO + i * 1024);
#define P3_PAIR(i, NW, M0, M1) do { asm volatile("s_waitcnt vmcnt(%2)" : "+v"(B[i]), "+v"(B[(i) + 1]) : "i"(NW) : "memory"); \
            if (UNLIKELY(any_lane(max(B[i][3], B[(i) + 1][3]) == ~0u))) { \
                _Pragma("unroll 1") for (int s_ = 0; s_ < SPIN_CAP; ++s_) { NRETRY(); __builtin_amdgcn_s_sleep(3); asm volatile("s_waitcnt vmcnt(0)" ::: "memory"); unsigned m_ = 0; \
                    _Pragma("unroll") for (int q = (i); q < 8; ++q) ld16(B[q], l16, rsF, sO + q * 1024); \
                    _Pragma("unroll") for (int q = (i); q < 8; ++q) { WAITV1(7 - q, B[q]); m_ = max(m_, B[q][3]); } \
                    if (!any_lane(m_ == ~0u)) break; } } if ((i) == 0) STAMP(4); \
            acc0 = MFMA32(A0[i], B[i], acc0); acc1 = MFMA32(A1[i], B[i], acc1); M0; SBAR(); acc0 = MFMA32(A0[(i) + 1], B[(i) + 1], acc0); acc1 = MFMA32(A1[(i) + 1], B[(i) + 1], acc1); M1; SBAR(); } while (0)
        P3_PAIR(0, 6, NO, NO); P3_PAIR(2, 4, NO, NO); P3_PAIR(4, 2, GU(0, 0), GU(0, 1)); P3_PAIR(6, 2, GU(0, 2), NO);
    }
    const unsigned vc2 = WS_F2 + (g * 32 + t32) * 128;
    ld8(ck, w == 7 ? vc2 : OOB, rsF, 0);
    STAMP(27);
    const unsigned rw0 = L_A + (hh * 2 + j) * 8192 + h * 512 + t32 * 16, rw1 = rw0 ^ 128, rr = L_A + j * 8192 + u8 * 1024 + (((8 * hh + l7) ^ ((u8 & 1) << 3)) * 16);
    auto reduce = [&](f4& lo, f4& hi) {
#pragma unroll
        for (int u = 0; u < 4; ++u) {
            sts16f(((u & 1) ? rw1 : rw0) + u * 1024, (f4){acc0[4 * u], acc0[4 * u + 1], acc0[4 * u + 2], acc0[4 * u + 3]});
            sts16f(((u & 1) ? rw1 : rw0) + (4 + u) * 1024, (f4){acc1[4 * u], acc1[4 * u + 1], acc1[4 * u + 2], acc1[4 * u + 3]});
        }
        BAR_L();
        lo = (lds16f(rr) + lds16f(rr + 16384)) + (lds16f(rr + 32768) + lds16f(rr + 49152));
        hi = (lds16f(rr + 512) + lds16f(rr + 16896)) + (lds16f(rr + 33280) + lds16f(rr + 49664));
    };
    u4 Hres;
    const unsigned ga = (8 * h + q4) * 192 + (g4 & 1) * 96 + 8 * pp;
    f16 cs[3] = {};
    {
        f4 lo, hi; reduce(lo, hi);
        STAMP(28);
        Hres = (u4){pk2(lo2(lo) + (f2){blo(xres[0]), bhi(xres[0])}), pk2(hi2(lo) + (f2){blo(xres[1]), bhi(xres[1])}), pk2(lo2(hi) + (f2){blo(xres[2]), bhi(xres[2])}), pk2(hi2(hi) + (f2){blo(xres[3]), bhi(xres[3])})};
        st16(Hres, vh, rsF, sHw);
        f2 s1 = {0.f, 0.f}, s2 = {0.f, 0.f};
#pragma unroll
        for (int e = 0; e < 4; ++e) { const f2 a = {blo(Hres[e]), bhi(Hres[e])}; s1 += a; s2 += a * a; }
        const float t1 = add_x32(add_x16(add_x8(s1[0] + s1[1]))), t2 = add_x32(add_x16(add_x8(s2[0] + s2[1])));
        st16((u4){uf(t1), uf(t2), uf(t1), uf(t2)}, vs, rsF, sSw);
        STAMP(33);
        GU(3, 0); GU(3, 1); GU(3, 2);
        asm volatile("s_waitcnt vmcnt(5) lgkmcnt(0)\n\ts_barrier" : "+v"(ck) :: "memory");
        STAMP(40);
#define COLSUM(k0, N) do { const unsigned o = ga + (k0) * 3072; u4 a[3 * (N)]; \
        _Pragma("unroll") for (int i = 0; i < 3 * (N); ++i) a[i] = ldtr(o + (i / 3) * 3072 + (i % 3) * 32, o + (i / 3) * 3072 + (i % 3) * 32 + 768); SBAR(); \
        _Pragma("unroll") for (int i = 0; i < 3 * (N); ++i) cs[i % 3] = MFMA32(ones, a[i], cs[i % 3]); PIN16(cs[0]); PIN16(cs[1]); PIN16(cs[2]); SBAR(); } while (0)
        COLSUM(3 * w, 3);
    }
    STAMP(36);
    const u4 rsWd = mk_rsrc(wd + fb * 128u, 1376u * 1024u - fb * 128u);
    if (w == 7 && UNLIKELY(any_lane(ck[0] != tag[0] || ck[1] != tag[1]))) POLL(vc2);
    if (w == 5 && b == 255) {
#pragma unroll 1
        for (int s_ = 0; s_ < 4 * SPIN_CAP; ++s_) { u2 f0, f1, f2_, f3; const unsigned vc = WS_F1 + l * 128;
            ld8(f0, vc, rsF, 0); ld8(f1, vc, rsF, 8192); ld8(f2_, vc, rsF, 16384); ld8(f3, vc, rsF, 24576); WAITV4(0, f0, f1, f2_, f3);
            if (!any_lane(((f0[0] ^ tag[0]) | (f1[0] ^ tag[0]) | (f2_[0] ^ tag[0]) | (f3[0] ^ tag[0]) | (f0[1] ^ tag[1]) | (f1[1] ^ tag[1]) | (f2_[1] ^ tag[1]) | (f3[1] ^ tag[1])) != 0u)) break; }
        FLAG(0u);
    }
    BAR_VM(0);
    STAMP(6);
    if (SL3) __builtin_amdgcn_s_sleep(SL3);
    const unsigned rot = 0;
    const unsigned a0 = 15 * hh, a1 = a0 + 1, jo = j * 8192;
    const unsigned wde = (48 * (a0 >> 1) + 16 * (a0 & 1)) * 1024 + jo, wdo = (48 * (a1 >> 1) + 16 * (a1 & 1)) * 1024 + jo, wdt = (336 * hh + 32) * 1024 + jo;
    const unsigned wla = L_A + hh * 14336 + j * 1024, wlb = rot * 49152 + hh * 12288 + j * 1024, wlc = 49152 - rot * 49152 + hh * 4096 + j * 1024;
#define WD(i) dma_bufi<((i) < 14 ? (i) % 7 : (i) < 20 ? (i) - 14 : (i) - 20) * 2048u, ((i) < 15 ? (i) >> 1 : (i) - 15) * 49152u>((i) < 14 ? wla : (i) < 20 ? wlb : wlc, vrow, rsWd, (i) >= 15 ? wdt : ((i) & 1) ? wdo : wde)
    u4 D0[7], D1[7];
    {
        const unsigned sH = IDLE(r) ? OOB : WS_H + (8 * g + w) * OS, sS = IDLE(r) ? OOB : WS_ST + (8 * g + w) * 4096u;
#define PD4 6
        constexpr int PD = PD4;
        u4 Hb[32], A0[3], A1[3], A2[4];
        u2 st[4];
        f16 ag[3] = {};
        const unsigned cf = L_CF + w * 384, vstat = h * 2048 + t32 * 16;
        const unsigned gl = ga + rot * 49152, gh = ga - rot * 49152, sl = sH + rot * 16384, sh = sH - rot * 16384;
#define GA(k) (((k) < 16 ? gl : gh) + (k) * 3072)
#define HO(k) (((k) < 16 ? sl : sh) + (k) * 1024)
#pragma unroll
        for (int i = 0; i < PD; ++i) ld16(Hb[kgo(i)], l16, rsF, HO(kgo(i)));
        COLSUM(24 + w, 1); sts16f(L_CSP + w * 512 + t32 * 16, (f4){cs[0][0], cs[1][0], cs[2][0], 0.f}); BAR_L();
        {
            const unsigned o = L_CSP + h * 2048 + t32 * 16; const f4 s = (lds16f(o) + lds16f(o + 512)) + (lds16f(o + 1024) + lds16f(o + 1536));
            const float c0 = add_x32(s[0]), c1 = add_x32(s[1]), c2 = add_x32(s[2]);
            sts4f(cf + l * 4, h ? c1 : c0); sts4f(cf + 256 + t32 * 4, c2); }
        A0[0] = ldtr(GA(0), GA(0) + 768); A1[0] = ldtr(GA(0) + 32, GA(0) + 800); A0[1] = ldtr(GA(kgo(1)), GA(kgo(1)) + 768); A1[1] = ldtr(GA(kgo(1)) + 32, GA(kgo(1)) + 800);
        unsigned mx = 0;
#define HDS 10
#define P4_STEP(i) do { constexpr int c = (i) % 3, n = ((i) + 2) % 3, k = kgo(i); const unsigned o = GA((i) < 30 ? kgo((i) + 2) : 0); \
            if ((i) + PD < 32) ld16(Hb[kgo((i) + PD < 32 ? (i) + PD : 0)], l16, rsF, HO(kgo((i) + PD))); \
            else if ((i) + PD < 36) ld8(st[((i) + PD - 32) & 3], vstat, rsF, sS + ((i) + PD - 32) * 512); \
            WAITV1((i) + PD < 36 ? PD : 35 - (i), Hb[k]); if ((i) == HDS) HOTB(3, wd, 43); \
            if ((i) < 8) FRESH16(Hb[k], l16, rsF, HO(k)); else { mx = max(mx, Hb[k][3]); asm volatile("" : "+v"(mx)); } if ((i) == 0) STAMP(7); SBAR(); \
            ag[0] = MFMA32(A0[c], Hb[k], ag[0]); if ((i) < 30) A0[n] = ldtr(o, o + 768); else A2[((i) - 30) & 1] = ldtr(GA(((i) - 30) & 1) + 64, GA(((i) - 30) & 1) + 832); SBAR(); \
            ag[1] = MFMA32(A1[c], Hb[k], ag[1]); if ((i) < 30) A1[n] = ldtr(o + 32, o + 800); else if ((i) == 31) A2[2] = ldtr(GA(2) + 64, GA(2) + 832); SBAR(); } while (0)
#define P4_4(i) P4_STEP(i); P4_STEP((i) + 1); P4_STEP((i) + 2); P4_STEP((i) + 3)
        P4_4(0); P4_4(4); P4_4(8); P4_4(12); P4_4(16); P4_4(20); P4_4(24); P4_4(28);
        WAITV4(0, st[0], st[1], st[2], st[3]);
        if (UNLIKELY(any_lane(max(max(mx, max(st[0][1], st[1][1])), max(st[2][1], st[3][1])) == ~0u))) {
#pragma unroll
            for (int i = 0; i < 32; ++i) FRESH16(Hb[i], l16, rsF, HO(i));
#pragma unroll 1
            for (int s_ = 0; s_ < SPIN_CAP; ++s_) { NRETRY();
#pragma unroll
                for (int i = 0; i < 4; ++i) ld8(st[i], vstat, rsF, sS + i * 512);
                WAITV4(0, st[0], st[1], st[2], st[3]);
                if (!any_lane(max(max(st[0][1], st[1][1]), max(st[2][1], st[3][1])) == ~0u)) break;
            }
            ag[0] = z16; ag[1] = z16;
#pragma unroll
            for (int i = 0; i < 32; ++i) { const unsigned o = GA(kgo(i)); ag[0] = MFMA32(ldtr(o, o + 768), Hb[kgo(i)], ag[0]); ag[1] = MFMA32(ldtr(o + 32, o + 800), Hb[kgo(i)], ag[1]); }
        }
        STAMP(8);
        const float t1 = add_x32(fu(st[0].x) + fu(st[1].x) + fu(st[2].x) + fu(st[3].x)), t2 = add_x32(fu(st[0].y) + fu(st[1].y) + fu(st[2].y) + fu(st[3].y));
        const float nm = t1 * (-1.f / 512), ve = __builtin_fmaxf(t2 * (1.f / 512) - nm * nm, 0.f) + 1e-5f, rs = __builtin_amdgcn_rsqf(ve);
        const float ka_ = rs * -2.3022081986f, kb_ = rs * rs * rs * -0.1029432397f;
#define G_UNIT(u, so_, M0, M1) do { u4 y; _Pragma("unroll") for (int qd = 0; qd < 2; ++qd) { \
                const f4 cg = lds16f(cf + (u) * 128 + qd * 32 + h * 16), cu = lds16f(cf + (u) * 128 + (2 + qd) * 32 + h * 16); \
                _Pragma("unroll") for (int e = 0; e < 2; ++e) { \
                    const f2 gt = (f2){ag[u][4 * qd + 2 * e], ag[u][4 * qd + 2 * e + 1]} + (e ? hi2(cg) : lo2(cg)) * nm, up = (f2){ag[u][8 + 4 * qd + 2 * e], ag[u][9 + 4 * qd + 2 * e]} + (e ? hi2(cu) : lo2(cu)) * nm; \
                    const f2 z = gt * (gt * gt * kb_ + ka_), d = (f2){__builtin_amdgcn_exp2f(z[0]), __builtin_amdgcn_exp2f(z[1])} * ve + ve; \
                    unsigned y_ = pk2(gt * up * (f2){__builtin_amdgcn_rcpf(d[0]), __builtin_amdgcn_rcpf(d[1])}); asm volatile("" : "+v"(y_)); y[2 * qd + e] = y_; } \
                if (qd == 0) MEM(M0); } \
            MEM(st16(y, l16, rsF, so_); M1); } while (0)
        MEM(WD(0), WD(1), WD(2));
        G_UNIT(0, sGa, (WD(3), WD(4), WD(5), WD(6)), NO); G_UNIT(1, sGa + 1024, NO, NO);
        STAMP(47);
        const unsigned da0 = L_A + hh * 14336 + ta0, da1 = L_A + hh * 14336 + ta1;
        const unsigned vc3 = WS_F3 + ((U64 >> 2) * 32 + min(t32, 28u)) * 128, vc4 = vc3 + (WS_F4 - WS_F3);
#define B1 6
#define F4D 0
#define CKS (B1 + 21)
#define WDS(i, m) if ((i) == B1 + 8 + (B1 + 21 <= 31 ? (m) : (m) * (24 - B1) / 13)) WD(7 + (m))
#define P4B_STEP(i) do { constexpr int c = (i) & 3, n = ((i) + 3) & 3; const unsigned o = GA(((i) + 3) & 31) + 64; \
            if ((i) == B1) SSTAMP(48); if ((i) == B1 + 1) SSTAMP(49); if ((i) == B1 + 8) SSTAMP(50); if ((i) == B1 + 9) SSTAMP(51); if ((i) == B1 + 15) SSTAMP(52); if ((i) == B1 + 21) SSTAMP(53); \
            if ((i) == B1) BAR_VM(2);         \
            if ((i) == B1 + 8) BAR_VM(0);        \
            if ((i) == B1 + 8 + F4D) FLAG(w == 4 ? WS_F4 + (g * 32 + r) * 128 : OOB); SBAR(); \
            ag[2] = MFMA32(A2[c], Hb[i], ag[2]); if ((i) < 29) A2[n] = ldtr(o, o + 768); \
            if ((i) >= B1 && (i) < B1 + 7) { constexpr int q = ((i) + 14 - B1) % 7; D0[q] = ldtr(da0 + q * 2048, da0 + q * 2048 + 1024); D1[q] = ldtr(da1 + q * 2048, da1 + q * 2048 + 1024); } SBAR(); \
            WDS(i, 0); WDS(i, 1); WDS(i, 2); WDS(i, 3); WDS(i, 4); WDS(i, 5); WDS(i, 6); WDS(i, 7); WDS(i, 8); WDS(i, 9); WDS(i, 10); WDS(i, 11); WDS(i, 12); \
            if ((i) == CKS) ld8(ck, w == 7 ? vc3 : w == 6 ? vc4 : OOB, rsF, 0); SBAR(); } while (0)
#define P4B_4(i) P4B_STEP(i); P4B_STEP((i) + 1); P4B_STEP((i) + 2); P4B_STEP((i) + 3)
        P4B_4(0); P4B_4(4); P4B_4(8); P4B_4(12); P4B_4(16); P4B_4(20); P4B_4(24); P4B_4(28);
        STAMP(41);
        G_UNIT(2, sGb, NO, NO);
        STAMP(30);
        WAITV1(1, ck);
        if (w >= 6 && UNLIKELY(any_lane(ck[0] != tag[0] || ck[1] != tag[1]))) { const unsigned vc = w == 7 ? vc3 : vc4; POLL(vc); }
    }
    BAR_L();
    STAMP(9);
    {
#define PD5 5
        constexpr int PD = PD5;
        u4 Gb[22], A0[2], A1[2];
        const unsigned gs = WS_G + (2 * U64 + j) * 90112u + 15 * hh * 1024, gz = hh == 3 ? OOB : gs, gt = WS_G + (2 * U64 + j) * 90112u + (60 + 7 * hh - 15) * 1024;
        const unsigned o0 = L_A + hh * 14336 + ta0, o1 = L_A + hh * 14336 + ta1, p0 = rot * 49152 + hh * 12288 + ta0, p1 = p0 - ta0 + ta1, q0 = 49152 - rot * 49152 + hh * 4096 + ta0, q1 = q0 - ta0 + ta1;
#define GOFF(i) (((i) >= 15 ? gt : (i) >= 13 ? gz : gs) + (i) * 1024)
#pragma unroll
        for (int i = 0; i < PD; ++i) ld16(Gb[i], l16, rsF, GOFF(i));
        WD(20); WD(21);
        acc0 = z16; acc1 = z16;
#define P5_STEP(i) do { constexpr int c = (i) & 1, n = c ^ 1; \
            if ((i) + PD < 22) ld16(Gb[(i) + PD < 22 ? (i) + PD : 0], l16, rsF, GOFF((i) + PD)); \
            WAITV1(((i) + PD < 22 ? PD : 21 - (i)) + ((i) < PD ? 2 : 0), Gb[i]); if ((i) >= 15) FRESH16(Gb[i], l16, rsF, GOFF(i)); if ((i) == 0) STAMP(10); \
            if ((i) == 14) BAR_L(); SBAR();        \
            const unsigned a0_ = (i) < 13 ? o0 + ((i) - 6) * 2048 : (i) < 19 ? p0 + ((i) - 13) * 2048 : q0 + ((i) - 19) * 2048, a1_ = (i) < 13 ? o1 + ((i) - 6) * 2048 : (i) < 19 ? p1 + ((i) - 13) * 2048 : q1 + ((i) - 19) * 2048; \
            acc0 = MFMA32((i) < 7 ? D0[(i) < 7 ? (i) : 0] : A0[c], Gb[i], acc0); if ((i) >= 6 && (i) < 21) A0[n] = ldtr(a0_, a0_ + 1024); SBAR(); \
            acc1 = MFMA32((i) < 7 ? D1[(i) < 7 ? (i) : 0] : A1[c], Gb[i], acc1); if ((i) >= 6 && (i) < 21) A1[n] = ldtr(a1_, a1_ + 1024); SBAR(); } while (0)
        P5_STEP(0); P5_STEP(1); P5_STEP(2); P5_STEP(3); P5_STEP(4); P5_STEP(5); P5_STEP(6); P5_STEP(7); P5_STEP(8); P5_STEP(9); P5_STEP(10);
        P5_STEP(11); P5_STEP(12); P5_STEP(13); P5_STEP(14); P5_STEP(15); P5_STEP(16); P5_STEP(17); P5_STEP(18); P5_STEP(19); P5_STEP(20); P5_STEP(21);
    }
    STAMP(11);
    {
        f4 lo, hi; reduce(lo, hi);
        const u4 Ores = {pk2(lo2(lo) + (f2){blo(Hres[0]), bhi(Hres[0])}), pk2(hi2(lo) + (f2){blo(Hres[1]), bhi(Hres[1])}), pk2(lo2(hi) + (f2){blo(Hres[2]), bhi(Hres[2])}), pk2(hi2(hi) + (f2){blo(Hres[3]), bhi(Hres[3])})};
        st16_plain(Ores, vres, mk_rsrc(out + U64 * 65536u, 65536u), 0);
    }
    asm volatile(".Lfb_code1:");
    STAMP(12);
#endif
}

#define WSOFF 0
extern "C" void kernel_launch(void* const* d_in, const int* in_sizes, int n_in, void* d_out, int out_size, void* d_ws, size_t ws_size, hipStream_t stream) {
    (void)in_sizes; (void)n_in; (void)out_size; (void)ws_size;
    (void)hipFuncSetAttribute((const void*)fused_block, hipFuncAttributeMaxDynamicSharedMemorySize, LDS_BYTES);
    fused_block<<<256, 512, LDS_BYTES, stream>>>((const char*)d_in[0], (const char*)d_in[1], (const char*)d_in[2], (const char*)d_in[4], (const char*)d_in[5], (const char*)d_in[6],
                                                 (char*)d_out, (char*)d_ws + WSOFF);
}
#define LASTSTAMP 12
#define STAMP_NAMES "start", "P1 loop end", "KV drained", 0, "O first data", "O drained", "h drained", "h first data", "P4 pass1 end", "P4 end bar", "G first data", "P5 loop end", "end", 0, 0, 0, \
  "st0", "st1", "st2", "st3", "st4", "st5", "st6", "st7", "KV first data", "att done", "O stored", "P3 loop end", "P3 reduced", 0, "G stored", 0, "KV stored", "h stored", \
  "LN sums", "pre KV bar", "colsum done", "A read", 0, "setup done", "GU bar", "P4 pass2 end", "att blk3", "att blk4", "att blk0", "comb bar", 0, "G01 stored", "p2 pre B1", "p2 s7", "p2 pre B2", "p2 s15", "p2 s21", "p2 s27", "h landed", "st landed"
```

```cpp
#include <hip/hip_runtime.h>
#include <hip/hip_bf16.h>
#include <stdint.h>
#pragma clang diagnostic ignored "-Wint-to-pointer-cast"

typedef __bf16 bf8 __attribute__((ext_vector_type(8)));
typedef __bf16 bf2 __attribute__((ext_vector_type(2)));
typedef short s4 __attribute__((ext_vector_type(4)));
typedef float f2 __attribute__((ext_vector_type(2)));
typedef float f4 __attribute__((ext_vector_type(4)));
typedef float f16 __attribute__((ext_vector_type(16)));
typedef unsigned u2 __attribute__((ext_vector_type(2)));
typedef unsigned u4 __attribute__((ext_vector_type(4)));
typedef unsigned long long u64;
#define LAS __attribute__((address_space(3)))
#define DEV __device__ __forceinline__

constexpr unsigned WS_F1 = 32768, WS_F2 = 65536, WS_F3 = 98304, WS_F4 = 131072;
constexpr unsigned KVS = 132096u, OS = 33792u;
constexpr unsigned WS_KV = 1u << 20;
constexpr unsigned WS_O = WS_KV + 32 * KVS;
constexpr unsigned WS_H = WS_O + 64 * OS;
constexpr unsigned WS_ST = WS_H + 64 * OS;
constexpr unsigned WS_G = WS_ST + 64 * 4096;
static_assert(WS_G + 64 * 90112u <= (16u << 20), "hand-off buffers");
constexpr unsigned LDS_BYTES = 163840;
constexpr unsigned L_OC = 73728;
constexpr unsigned L_A = 98304;
constexpr unsigned L_CSP = 155648, L_CF = L_CSP + 4096;
constexpr int SPIN_CAP = 1 << 14;
constexpr unsigned OOB = 0x40000000u;

#define MFMA16(a, b, c) __builtin_amdgcn_mfma_f32_16x16x32_bf16(__builtin_bit_cast(bf8, a), __builtin_bit_cast(bf8, b), c, 0, 0, 0)
#define MFMA32(a, b, c) __builtin_amdgcn_mfma_f32_32x32x16_bf16(__builtin_bit_cast(bf8, a), __builtin_bit_cast(bf8, b), c, 0, 0, 0)
#define SBAR() __builtin_amdgcn_sched_barrier(0)
#define BAR_VM(N) asm volatile("s_waitcnt vmcnt(%0) lgkmcnt(0)\n\ts_barrier" ::"i"(N) : "memory")
#define BAR_L() asm volatile("s_waitcnt lgkmcnt(0)\n\ts_barrier" ::: "memory")

DEV u4 mk_rsrc(const void* p, unsigned bytes) { const u64 a = (u64)p; return (u4){(unsigned)a, (unsigned)(a >> 32) & 0xffffu, bytes, 0x00020000u}; }
template <unsigned LI, unsigned SI> DEV void dma_bufi(unsigned lds, unsigned voff, u4 rs, unsigned soff) {
    unsigned t; asm volatile("s_add_u32 m0, %1, %5\n\ts_add_u32 %0, %4, %6\n\tbuffer_load_dwordx4 %2, %3, %0 offen lds" : "=&s"(t) : "s"(lds), "v"(voff), "s"(rs), "s"(soff), "i"(LI), "i"(SI) : "memory", "scc"); }
template <unsigned LI> DEV void dma_glbi(unsigned lds, const char* p) {
    asm volatile("s_add_u32 m0, %0, %2\n\ts_nop 0\n\tglobal_load_lds_dwordx4 %1, off" ::"s"(lds), "v"(p), "i"(LI) : "memory", "scc"); }
DEV void ld16(u4& d, unsigned voff, u4 rs, unsigned soff) {
    asm volatile("buffer_load_dwordx4 %0, %1, %2, %3 offen sc1" : "=v"(d) : "v"(voff), "s"(rs), "s"(soff) : "memory"); }
DEV void ld16_plain(u4& d, unsigned voff, u4 rs, unsigned soff) {
    asm volatile("buffer_load_dwordx4 %0, %1, %2, %3 offen" : "=v"(d) : "v"(voff), "s"(rs), "s"(soff) : "memory"); }
template <int OFF> DEV void ld16_imm(u4& d, unsigned voff, u4 rs) {
    asm volatile("buffer_load_dwordx4 %0, %1, %2, 0 offen offset:%3" : "=v"(d) : "v"(voff), "s"(rs), "i"(OFF) : "memory"); }
DEV void ld8(u2& d, unsigned voff, u4 rs, unsigned soff) {
    asm volatile("buffer_load_dwordx2 %0, %1, %2, %3 offen sc1" : "=v"(d) : "v"(voff), "s"(rs), "s"(soff) : "memory"); }
DEV void ld16_v(u4& d0, u4& d1, unsigned voff, u4 rs, unsigned soff) {
    asm volatile("s_mov_b64 exec, %5\n\tbuffer_load_dwordx4 %0, %2, %3, %4 offen offset:1024 sc1\n\tbuffer_load_dwordx4 %1, %2, %3, %4 offen offset:1536 sc1\n\ts_mov_b64 exec, -1"
                 : "+v"(d0), "+v"(d1) : "v"(voff), "s"(rs), "s"(soff), "s"(0x0000FFFF0000FFFFull) : "memory"); }
DEV void st16(u4 d, unsigned voff, u4 rs, unsigned soff) {
    asm volatile("buffer_store_dwordx4 %0, %1, %2, %3 offen sc1\n\ts_nop 1" ::"v"(d), "v"(voff), "s"(rs), "s"(soff) : "memory"); }
DEV void st16m(u4 d, unsigned voff, u4 rs, unsigned soff) { d[3] = min(d[3], 0xfffffffeu); st16(d, voff, rs, soff); }
DEV void st16_plain(u4 d, unsigned voff, u4 rs, unsigned soff) {
    asm volatile("buffer_store_dwordx4 %0, %1, %2, %3 offen\n\ts_nop 1" ::"v"(d), "v"(voff), "s"(rs), "s"(soff) : "memory"); }
DEV void st8(u2 d, unsigned voff, u4 rs, unsigned soff) {
    asm volatile("buffer_store_dwordx2 %0, %1, %2, %3 offen sc1\n\ts_nop 1" ::"v"(d), "v"(voff), "s"(rs), "s"(soff) : "memory"); }
#define WAITV1(N, a) asm volatile("s_waitcnt vmcnt(%1)" : "+v"(a) : "i"(N) : "memory")
#define WAITV1D(ND, NS, a) asm volatile("s_waitcnt vmcnt(%1)\n\ts_cmp_lt_u32 %3, 4\n\ts_cbranch_scc1 .Lw%=\n\ts_waitcnt vmcnt(%2)\n.Lw%=:" : "+v"(a) : "i"(ND), "i"(NS), "s"(w) : "memory", "scc")
#define WAITV3(N, a, b, c) asm volatile("s_waitcnt vmcnt(%3)" : "+v"(a), "+v"(b), "+v"(c) : "i"(N) : "memory")
#define WAITV4(N, a, b, c, d) asm volatile("s_waitcnt vmcnt(%4)" : "+v"(a), "+v"(b), "+v"(c), "+v"(d) : "i"(N) : "memory")

DEV u4 lds16(unsigned a) { return *(LAS const u4*)a; }
DEV f4 lds16f(unsigned a) { return *(LAS const f4*)a; }
DEV float lds4f(unsigned a) { return *(LAS const float*)a; }
DEV void sts16(unsigned a, u4 v) { *(LAS u4*)a = v; }
DEV void sts16f(unsigned a, f4 v) { *(LAS f4*)a = v; }
DEV void sts4f(unsigned a, float v) { *(LAS float*)a = v; }
DEV u4 ldtr(unsigned a0, unsigned a1) {
    const s4 lo = __builtin_amdgcn_ds_read_tr16_b64_v4i16((LAS s4*)a0), hi = __builtin_amdgcn_ds_read_tr16_b64_v4i16((LAS s4*)a1);
    return __builtin_bit_cast(u4, __builtin_shufflevector(lo, hi, 0, 1, 2, 3, 4, 5, 6, 7)); }
DEV unsigned pk2(f2 a) { return __builtin_bit_cast(unsigned, __builtin_convertvector(a, bf2)); }
DEV f2 lo2(f4 a) { return (f2){a[0], a[1]}; }
DEV f2 hi2(f4 a) { return (f2){a[2], a[3]}; }
DEV unsigned pk(float a, float b) { return __builtin_bit_cast(unsigned, __builtin_convertvector((f2){a, b}, bf2)); }
DEV float blo(unsigned v) { return __builtin_bit_cast(float, v << 16); }
DEV float bhi(unsigned v) { return __builtin_bit_cast(float, v & 0xffff0000u); }
DEV float fu(unsigned v) { return __builtin_bit_cast(float, v); }
DEV unsigned uf(float v) { return __builtin_bit_cast(unsigned, v); }
DEV float add_x32(float v) { auto r = __builtin_amdgcn_permlane32_swap(uf(v), uf(v), false, false); const unsigned a = r[0], b = r[1]; return fu(a) + fu(b); }
DEV float add_x16(float v) { auto r = __builtin_amdgcn_permlane16_swap(uf(v), uf(v), false, false); const unsigned a = r[0], b = r[1]; return fu(a) + fu(b); }
DEV float max_x32(float v) { auto r = __builtin_amdgcn_permlane32_swap(uf(v), uf(v), false, false); const unsigned a = r[0], b = r[1]; return __builtin_fmaxf(fu(a), fu(b)); }
template <int C> DEV float add_dpp(float v) { return v + fu(__builtin_amdgcn_update_dpp(0u, uf(v), C, 0xf, 0xf, true)); }
DEV float add_x8(float v) { return v + fu(__builtin_amdgcn_update_dpp(0u, uf(v), 0x128, 0xf, 0xf, true)); }
constexpr u64 diag_mask(int r) { u64 m = 0; for (int l = 0; l < 64; ++l) { const int key = 16 * ((r >> 2) & 1) + 8 * (l >> 5) + 4 * (r >> 3) + (r & 3); if (key <= (l & 31)) m |= 1ull << l; } return m; }
DEV float sel(u64 m, float t, float f) { return __builtin_amdgcn_inverse_ballot_w64(m) ? t : f; }

typedef short sh2 __attribute__((ext_vector_type(2)));
constexpr int kgo(int i) { return i < 8 ? 4 * i : (i - 8) / 3 * 4 + (i - 8) % 3 + 1; }
#define YOUNG_FIRST() do { if (w >= 4) __builtin_amdgcn_s_setprio(1); } while (0)
#define EVEN() __builtin_amdgcn_s_setprio(0)
DEV bool any_lane(bool c) { return __builtin_amdgcn_ballot_w64(c) != 0; }
#define UNLIKELY(x) __builtin_expect(!!(x), 0)
#define FRESH16(d, voff, rs, soff) do { if (UNLIKELY(any_lane((d)[3] == ~0u))) { \
        _Pragma("unroll 1") for (int s_ = 0; s_ < SPIN_CAP; ++s_) { __builtin_amdgcn_s_sleep(10); u4 t_; asm volatile("buffer_load_dwordx4 %0, %1, %2, %3 offen sc1\n\ts_waitcnt vmcnt(0)" : "=&v"(t_) : "v"(voff), "s"(rs), "s"(soff) : "memory"); \
            d = t_; if (!any_lane(t_[3] == ~0u)) break; } } } while (0)
#define POLL(voff) do { \
        _Pragma("unroll 1") for (int s_ = 0; s_ < 4 * SPIN_CAP; ++s_) { u2 t_; asm volatile("buffer_load_dwordx2 %0, %1, %2, 0 offen sc1\n\ts_waitcnt vmcnt(0)" : "=&v"(t_) : "v"(voff), "s"(rsF) : "memory"); \
            if (!any_lane(t_[0] != tag[0] || t_[1] != tag[1])) break; __builtin_amdgcn_s_sleep(4); } } while (0)
#define FLAG(off) st8(tag, l == 0 ? (off) : OOB, rsF, 0)

__global__ __launch_bounds__(512, 2) void fused_block(const char* x_, const char* wqkv_, const char* wo_, const char* wg_, const char* wu_, const char* wd_, char* out_, char* ws_) {
#if defined(__HIP_DEVICE_COMPILE__)
    typedef u64 u64x8 __attribute__((ext_vector_type(8)));
    u64x8 ka; asm volatile("s_load_dwordx16 %0, %1, 0x0\n\ts_waitcnt lgkmcnt(0)" : "=s"(ka) : "s"(__builtin_amdgcn_kernarg_segment_ptr()) : "memory");
    const char *x = (const char*)ka[0], *wqkv = (const char*)ka[1], *wo = (const char*)ka[2], *wg = (const char*)ka[3], *wu = (const char*)ka[4], *wd = (const char*)ka[5]; char *out = (char*)ka[6], *ws = (char*)ka[7];
    const unsigned l = threadIdx.x & 63, w = __builtin_amdgcn_readfirstlane(threadIdx.x >> 6);
    const unsigned b = blockIdx.x, xc = b & 7, rk = b >> 3;
    const unsigned T64 = 8 * (xc >> 1) + (rk >> 2), hq = 4 * (xc & 1) + (rk & 3);
    const unsigned u8 = l >> 3, l7 = l & 7;
    u4 xr[8];
    const u4 rsX = mk_rsrc(x + T64 * 65536u, 65536u);
    const unsigned vx = (8 * w + u8) * 1024 + l7 * 16;
#define TOUCH(R, p) asm volatile("global_load_dword " R ", %0, off" ::"v"(p) : "memory", R)
#define SLOWBLK(base) ((3u - (unsigned)((u64)(base) >> 7)) & 7u)
    if ((SLOWBLK(wqkv) >> 2) == (xc & 1) && w == 7) TOUCH("v254", wqkv + (SLOWBLK(wqkv) * 128 + (rk * 48 + (l < 48 ? l : 0)) * 1024));
    ld16_imm<0>(xr[0], vx, rsX); ld16_imm<128>(xr[1], vx, rsX); ld16_imm<256>(xr[2], vx, rsX); ld16_imm<384>(xr[3], vx, rsX); ld16_imm<512>(xr[4], vx, rsX); ld16_imm<640>(xr[5], vx, rsX);
    const u4 rsW = mk_rsrc(wqkv + hq * 128u, 512u * 3072u - hq * 128u);
    SBAR();
    const unsigned w3 = w & 3, wk2 = w3 * 2048;
    unsigned vw0 = (16 * w3 + u8) * 3072 + 32 * ((l7 >> 1) ^ ((l >> 4) & 1)) + 16 * (l7 & 1), vw1 = (vw0 + 24576) ^ 64;
#define SLOT(i) ((((i) + 1) & 3) * 24576u)
#define SP(i, p) dma_bufi<SLOT(i) + ((p) >> 1) * 8192 + ((p) & 1) * 1024, (i) * 196608u + ((p) >> 1) * 1024u>(wk2, ((p) & 1) ? vw1 : vw0, rsW, 0)
    const unsigned U64 = 4 * xc + (rk >> 3), fb = rk & 7;
    const unsigned g = 2 * (xc >> 1) + (rk >> 4), r = (rk & 15) + (xc & 1) > 14 ? 31 + (xc & 1) * ((rk & 15) - 16) : 15 * (xc & 1) + (rk & 15);
    const unsigned j = w & 1, hh = w >> 1;
    const unsigned t16 = l & 15, g4 = l >> 4, h = l >> 5, t32 = l & 31, q4 = t16 >> 2, pp = l & 3, l16 = l * 16;
    const u4 rsF = mk_rsrc(ws, 16u << 20);
    const u4 ones = {0x3f803f80u, 0x3f803f80u, 0x3f803f80u, 0x3f803f80u};
    const unsigned kvb = WS_KV + (4 * hq + hh) * KVS;
    const unsigned skv = kvb + (2 * T64 + j) * 2048;
    const unsigned sOw = WS_O + (2 * T64 + j) * OS + (4 * hq + hh) * 1024;
    u2 tag; u4 xres;
    unsigned vres = (32 * j + 8 * hh + l7) * 1024 + fb * 128 + u8 * 16;
    unsigned xw0 = L_A + (8 * w + u8) * 1024 + 16 * (l7 ^ (8 * (w & 1) + u8)), xw1 = xw0 ^ 128;
    unsigned xa[4];
#pragma unroll
    for (int q = 0; q < 4; ++q) { xa[q] = L_A + (32 * j + t16) * 1024 + 16 * ((4 * q + g4) ^ t16); asm volatile("" : "+v"(xa[q])); }
    unsigned wa = (8 * g4 + q4) * 128 + 32 * (hh ^ ((q4 >> 1) | ((g4 & 1) << 1))) + 8 * pp;
    f4 aq[2] = {}, ak[2] = {}, av[2] = {};
    unsigned vk = (h * 32 + (16 * ((t32 >> 3) & 1) + 8 * ((t32 >> 2) & 1) + 4 * (t32 >> 4) + (t32 & 3))) * 16, vv_ = (h * 16 + t16) * 16;
    unsigned vst = 1024 + ((g4 & 1) * 32 + (g4 >> 1) * 16 + t16) * 16;
    unsigned vrow = u8 * 1024 + 16 * (l7 ^ (((l >> 4) & 1) << 2));
    const char* gp[3];
#pragma unroll
    for (int i = 0; i < 3; ++i) { const unsigned z = 64 * i + l, k = (z * 43691u) >> 19, c = z - 12 * k, m = c >= 6, cc = c - 6 * m; unsigned un = 3 * r + (cc >> 1); un = un > 85 ? 85 : un;
        gp[i] = (m ? wu : wg) + (r > 28 ? 0 : (16 * w + k) * 2752 + un * 32 + (cc & 1) * 16); asm volatile("" : "+v"(gp[i])); }
    const unsigned wk3 = w * 3072;
#define GU(R, c) dma_glbi<(R) * 24576 + (c) * 1024>(wk3, gp[c] + (R) * 128 * 2752)
#define GUD(R, q) dma_glbi<(R) * 24576 + ((q) / 3) * 12288 + ((q) % 3) * 1024>(wk3, gp[(q) % 3] + ((R) * 128 + ((q) / 3) * 64) * 2752)
    asm volatile("" : "+v"(vw0), "+v"(vw1), "+v"(xw0), "+v"(xw1), "+v"(wa), "+v"(vres), "+v"(vk), "+v"(vv_), "+v"(vrow), "+v"(vst));
    ld16_imm<768>(xr[6], vx, rsX); ld16_imm<896>(xr[7], vx, rsX);
    {
        const char* pc; unsigned nb; asm volatile("s_getpc_b64 %0\n.Lfb_code0:\n\ts_mov_b32 %1, .Lfb_code1-.Lfb_code0" : "=s"(pc), "=s"(nb));
        const unsigned co = ((rk * 8 + w) * 2 + (l & 1)) * 128;
        TOUCH("v254", l < 32 ? wqkv + ((b * 8 + w) * 6 + (l < 6 ? l : 0)) * 128 : pc + (co < nb ? co : 0)); }
#define NO ((void)0)
#define HOTB(wv, base, n) do { if (w == (wv)) TOUCH("v254", base + (SLOWBLK(base) * 128 + (rk * (n) + (l < (n) ? l : 0)) * 1024)); } while (0)
    {
        f2 xf[32], s1 = {0.f, 0.f}, s2 = {0.f, 0.f};
#define LN_SUM(i, N, D0, D1) do { WAITV1D((N) + 1, 8 - (i), xr[i]); _Pragma("unroll") for (int e = 0; e < 4; ++e) { const unsigned d_ = xr[i][e]; f2 v_ = {blo(d_), bhi(d_)}; asm volatile("" : "+v"(v_)); xf[4 * (i) + e] = v_; \
        s1 += v_; s2 += v_ * v_; } asm volatile("" : "+v"(s1), "+v"(s2)); if (w < 4) { D0; D1; } SBAR(); } while (0)
        LN_SUM(0, 7, NO, NO); LN_SUM(1, 6, NO, NO); LN_SUM(2, 5, NO, NO); LN_SUM(3, 4, NO, NO);
        LN_SUM(4, 3, SP(0, 0), SP(0, 1)); LN_SUM(5, 4, SP(0, 2), SP(0, 3)); LN_SUM(6, 5, SP(0, 4), NO); LN_SUM(7, 5, SP(0, 5), NO);
        if (w < 4) { SP(1, 0); SP(1, 1); SP(1, 2); SP(1, 3); SP(1, 4); SP(1, 5); }
        else TOUCH("v255", (w == 4 ? wo : w == 5 ? wg : w == 6 ? wu : wd) + (w == 4 ? b * 16 + (l & 15) : b * 43 + (l < 43 ? l : 0)) * 128);
        const float t1 = add_dpp<0x141>(add_dpp<0x4e>(add_dpp<0xb1>(s1[0] + s1[1]))), t2 = add_dpp<0x141>(add_dpp<0x4e>(add_dpp<0xb1>(s2[0] + s2[1])));
        const float mu = t1 * (1.f / 512), rs = __builtin_amdgcn_rsqf(__builtin_fmaxf(t2 * (1.f / 512) - mu * mu, 0.f) + 1e-5f), mr = -mu * rs;
#pragma unroll
        for (int i = 0; i < 8; ++i) sts16(((i & 1) ? xw1 : xw0) + (i >> 1) * 256, (u4){pk2(xf[4 * i] * rs + mr), pk2(xf[4 * i + 1] * rs + mr), pk2(xf[4 * i + 2] * rs + mr), pk2(xf[4 * i + 3] * rs + mr)});
    }
#define P1_STEP(n, D0, D1, D2) do { constexpr int c_ = (n) & 1, x_ = c_ ^ 1; constexpr bool m_ = (n) < 15; \
        const unsigned xb = xa[((n) + 1) & 3] + (((n) + 1) >> 2) * 256, wb = wa + SLOT(((n) + 1) >> 1) + x_ * 4096; \
        if (m_) X0[x_] = lds16(xb); SBAR(); \
        aq[0] = MFMA16(Wq[c_], X0[c_], aq[0]); if (m_) Wq[x_] = ldtr(wb, wb + 512); SBAR(); \
        aq[1] = MFMA16(Wq[c_], X1[c_], aq[1]); if (m_) X1[x_] = lds16(xb + 16384); D0; SBAR(); \
        ak[0] = MFMA16(Wk[c_], X0[c_], ak[0]); if (m_) Wk[x_] = ldtr(wb + 8192, wb + 8704); SBAR(); \
        ak[1] = MFMA16(Wk[c_], X1[c_], ak[1]); D1; SBAR(); \
        av[0] = MFMA16(X0[c_], Wv[c_], av[0]); if (m_) Wv[x_] = ldtr(wb + 16384, wb + 16896); SBAR(); \
        av[1] = MFMA16(X1[c_], Wv[c_], av[1]); D2; SBAR(); } while (0)
#define P1_FIRST() do { const unsigned wb = wa + SLOT(0); X0[0] = lds16(xa[0]); Wq[0] = ldtr(wb, wb + 512); X1[0] = lds16(xa[0] + 16384); Wk[0] = ldtr(wb + 8192, wb + 8704); Wv[0] = ldtr(wb + 16384, wb + 16896); } while (0)
#define XRES() ld16_plain(xres, vres, mk_rsrc(x + U64 * 65536u, 65536u), 0)
    if (w < 4) {
        u4 X0[2], X1[2], Wq[2], Wk[2], Wv[2];
        SP(2, 0); SP(2, 1); SP(2, 2); SP(2, 3); SP(2, 4); SP(2, 5);
        BAR_VM(12); P1_FIRST(); P1_STEP(0, (SP(3, 0), SP(3, 1)), (SP(3, 2), SP(3, 3)), (SP(3, 4), SP(3, 5)));
        BAR_VM(12); P1_STEP(1, SP(4, 0), SP(4, 1), SP(4, 2)); P1_STEP(2, SP(4, 3), SP(4, 4), SP(4, 5));
        BAR_VM(12); P1_STEP(3, SP(5, 0), SP(5, 1), SP(5, 2)); P1_STEP(4, SP(5, 3), SP(5, 4), SP(5, 5));
        BAR_VM(12); P1_STEP(5, SP(6, 0), SP(6, 1), SP(6, 2)); P1_STEP(6, SP(6, 3), SP(6, 4), SP(6, 5));
        BAR_VM(12); P1_STEP(7, SP(7, 0), SP(7, 1), SP(7, 2)); P1_STEP(8, SP(7, 3), SP(7, 4), SP(7, 5));
        BAR_VM(12); P1_STEP(9, XRES(), GUD(1, 0), NO); P1_STEP(10, GUD(1, 1), GUD(1, 2), NO);
        BAR_VM(10); P1_STEP(11, GUD(1, 3), GUD(1, 4), NO); P1_STEP(12, GUD(1, 5), NO, NO);
        BAR_VM(7);  P1_STEP(13, NO, NO, NO); P1_STEP(14, NO, NO, NO); P1_STEP(15, NO, NO, NO);
        WAITV1(6, xres); tag = (u2){0u, 0u};
    } else {
        u4 X0[2], X1[2], Wq[2], Wk[2], Wv[2];
        const u4 bad = {~0u, ~0u, ~0u, ~0u};
#define MK(v, s_) st16(bad, v, rsF, s_)
        BAR_L(); P1_FIRST(); P1_STEP(0, ld8(tag, 0u * l, rsF, 0), MK(l16, skv), NO);
        BAR_L(); P1_STEP(1, NO, MK(l16 + 1024, skv), NO); P1_STEP(2, NO, MK(l16, skv - 2 * KVS), NO);
        BAR_L(); P1_STEP(3, NO, MK(l16 + 1024, skv - 2 * KVS), NO); P1_STEP(4, NO, MK(l16, sOw), NO);
        BAR_L(); P1_STEP(5, NO, MK(l16, sOw - 2048), NO); P1_STEP(6, XRES(), NO, NO);
        BAR_L(); P1_STEP(7, NO, NO, NO); P1_STEP(8, NO, NO, NO);
        BAR_L(); P1_STEP(9, NO, NO, NO); P1_STEP(10, NO, NO, NO);
        asm volatile("s_waitcnt vmcnt(0)" : "+v"(tag), "+v"(xres) :: "memory");
        BAR_L();
        { const unsigned lo = tag.x + 1u; tag.y += (lo == 0u); tag.x = lo; }
                             P1_STEP(11, FLAG(w == 4 ? WS_F1 + (T64 * 8 + hq) * 128 : OOB), NO, NO); P1_STEP(12, HOTB(6, wo, 16), NO, NO);
        BAR_L(); P1_STEP(13, NO, NO, NO); P1_STEP(14, NO, NO, NO); P1_STEP(15, NO, NO, NO);
    }
    u4 Qf;
    {
        { auto a = __builtin_amdgcn_permlane16_swap(pk2(lo2(ak[0])), pk2(lo2(ak[1])), false, false); auto c = __builtin_amdgcn_permlane16_swap(pk2(hi2(ak[0])), pk2(hi2(ak[1])), false, false);
          st16m((u4){a[0], c[0], a[1], c[1]}, l16, rsF, skv); }
        st16m((u4){pk2(lo2(av[0])), pk2(hi2(av[0])), pk2(lo2(av[1])), pk2(hi2(av[1]))}, vst, rsF, skv);
        SBAR();
        constexpr float QS = 0.25f * 1.4426950408889634f;
        { auto a = __builtin_amdgcn_permlane16_swap(pk2(lo2(aq[0]) * QS), pk2(lo2(aq[1]) * QS), false, false); auto c = __builtin_amdgcn_permlane16_swap(pk2(hi2(aq[0]) * QS), pk2(hi2(aq[1]) * QS), false, false);
          Qf = (u4){a[0], c[0], a[1], c[1]}; }
        sts16(L_OC + (w ^ 1) * 2304 + l16, Qf);
    }
    const int Tb = 2 * (int)T64 - 8;
    u4 K[5], V0[5], V1[5];
    const int blk[5] = {Tb + 8 + (int)j, Tb + (int)j, Tb + 2 + 3 * (int)j, Tb + 3 + 3 * (int)j, Tb + 4 + 3 * (int)j};
#pragma unroll
    for (int i = 0; i < 5; ++i) { const unsigned o = blk[i] < 0 ? 0u : 0x3f803f80u; V0[i] = (u4){o, o, o, o}; V1[i] = V0[i]; asm volatile("" : "+v"(V0[i]), "+v"(V1[i])); }
    unsigned pm[8];
    { const short uq = (short)(t32 - 8 * h);
#pragma unroll
      for (int i = 0; i < 8; ++i) { const short c = (short)(16 * ((i >> 1) & 1) + 4 * (i >> 2) + 2 * (i & 1)); pm[i] = __builtin_bit_cast(unsigned, ((sh2){uq, uq} - (sh2){c, (short)(c + 1)}) >> (sh2){15, 15}); asm volatile("" : "+v"(pm[i])); } }
    unsigned so[5];
#pragma unroll
    for (int i = 0; i < 5; ++i) so[i] = blk[i] < 0 ? OOB : kvb + (unsigned)blk[i] * 2048;
    if (w == 7) {
        const int Tp = (int)T64 - 1 - (int)(l & 3); const unsigned vc = WS_F1 + ((Tp < 0 ? T64 : (unsigned)Tp) * 8 + hq) * 128; POLL(vc); }
    BAR_VM(0);
#define KVLD(i) do { ld16(K[i], vk, rsF, so[i]); ld16_v(V0[i], V1[i], vv_, rsF, so[i]); } while (0)
#define KVOK(N, i) do { WAITV3(N, K[i], V0[i], V1[i]); \
        if (UNLIKELY(any_lane(max(max(K[i][3], V0[i][3]), V1[i][3]) == ~0u))) { \
            _Pragma("unroll 1") for (int s_ = 0; s_ < SPIN_CAP; ++s_) { __builtin_amdgcn_s_sleep(10); KVLD(i); WAITV3(0, K[i], V0[i], V1[i]); if (!any_lane(max(max(K[i][3], V0[i][3]), V1[i][3]) == ~0u)) break; } } } while (0)
    KVLD(2); KVLD(3);
    const u4 Qm = Qf, Qo = lds16(L_OC + w * 2304 + l16);
    f16 Om = {}, Oo = {};
    const f16 z16 = {};
    auto pv_tile = [&](const f16& S, const u4& va, const u4& vb, f16& O) {
        float p[16];
#pragma unroll
        for (int e = 0; e < 16; ++e) p[e] = __builtin_amdgcn_exp2f(S[e]);
        const u4 P0 = {pk(p[0], p[1]), pk(p[2], p[3]), pk(p[4], p[5]), pk(p[6], p[7])}, P1 = {pk(p[8], p[9]), pk(p[10], p[11]), pk(p[12], p[13]), pk(p[14], p[15])};
        O = MFMA32(va, P0, O); O = MFMA32(vb, P1, O);
    };
    const u4 rsWo = mk_rsrc(wo + fb * 128u, 512u * 1024u - fb * 128u);
    const unsigned wol = L_A + w * 8192, wos = w * 65536;
    const unsigned vh = (u8 >> 1) * 1024 + ((u8 & 1) * 32 + 8 * hh + l7) * 16, vs = l < 8 ? (8 * hh + l) * 16 : OOB;
    const unsigned sHw = WS_H + (2 * U64 + j) * OS + fb * 4096, sSw = WS_ST + (2 * U64 + j) * 4096u + fb * 512;
    const unsigned sGa = r < 29 ? WS_G + (8 * g + w) * 90112u + r * 2048 : OOB, sGb = r < 28 ? WS_G + (8 * g + w) * 90112u + (60 + r) * 1024 : OOB;
#define WO_(i) dma_bufi<(i) * 1024, (i) * 8192>(wol, vrow, rsWo, wos)
#define PIN16(a) asm volatile("" : "+v"(a))
#define MEM(...) do { SBAR(); __VA_ARGS__; SBAR(); } while (0)
#define HALF(S, o, va, O) do { float p_[8]; _Pragma("unroll") for (int e = 0; e < 8; ++e) p_[e] = __builtin_amdgcn_exp2f(S[(o) + e]); \
        u4 q_ = {pk(p_[0], p_[1]), pk(p_[2], p_[3]), pk(p_[4], p_[5]), pk(p_[6], p_[7])}; asm volatile("" : "+v"(q_)); O = MFMA32(va, q_, O); PIN16(O); SBAR(); } while (0)
#define SCORE(S, k, q) do { S = MFMA32(k, q, z16); PIN16(S); SBAR(); } while (0)
    f16 Sa, Sb, Sc;
    u2 ck; const unsigned vc1 = WS_F1 + (U64 * 8 + l7) * 128;
    const u4 bad = {~0u, ~0u, ~0u, ~0u};
    YOUNG_FIRST();
    KVOK(3, 2);
    SCORE(Sa, K[2], Qm); SCORE(Sb, K[2], Qo); MEM(KVLD(4));
    HALF(Sa, 0, V0[2], Om); MEM(KVLD(0)); HALF(Sa, 8, V1[2], Om); MEM(KVLD(1)); KVOK(9, 3);
    SCORE(Sa, K[3], Qm); HALF(Sb, 0, V0[2], Oo); MEM(WO_(0)); HALF(Sb, 8, V1[2], Oo); MEM(WO_(1));
    SCORE(Sb, K[3], Qo); HALF(Sa, 0, V0[3], Om); MEM(WO_(2)); HALF(Sa, 8, V1[3], Om); MEM(WO_(3)); KVOK(10, 4);
    SCORE(Sa, K[4], Qm); HALF(Sb, 0, V0[3], Oo); MEM(WO_(4)); HALF(Sb, 8, V1[3], Oo); MEM(WO_(5));
    EVEN();
    SCORE(Sb, K[4], Qo); HALF(Sa, 0, V0[4], Om); MEM(WO_(6)); HALF(Sa, 8, V1[4], Om); MEM(WO_(7)); KVOK(11, 0); KVOK(8, 1);
    if (j == 0) { SCORE(Sa, K[0], Qo); HALF(Sb, 0, V0[4], Oo); MEM(st16(bad, vh, rsF, sHw)); HALF(Sb, 8, V1[4], Oo); MEM(st16(bad, vs, rsF, sSw), st16(bad, l16, rsF, sGb));
                  SCORE(Sb, K[0], Qm); SCORE(Sc, K[1], Qm); HALF(Sa, 0, V0[0], Oo); HALF(Sa, 8, V1[0], Oo); }
    else        { SCORE(Sa, K[1], Qo); HALF(Sb, 0, V0[4], Oo); MEM(st16(bad, vh, rsF, sHw)); HALF(Sb, 8, V1[4], Oo); MEM(st16(bad, vs, rsF, sSw), st16(bad, l16, rsF, sGb));
                  SCORE(Sb, K[0], Qm); SCORE(Sc, K[1], Qm); HALF(Sa, 0, V0[1], Oo); HALF(Sa, 8, V1[1], Oo); }
    MEM(ld8(ck, w == 7 ? vc1 : OOB, rsF, 0), GU(2, 0), GU(2, 1), GU(2, 2));
    const unsigned oc = L_OC + w * 2304;
    {
        const f16 Sx = Sb, Sy = Sc;
        float p[16];
#pragma unroll
        for (int e = 0; e < 8; ++e) p[e] = __builtin_amdgcn_exp2f(sel(diag_mask(e), Sx[e], Sy[e]));
        SBAR();
        sts16f(oc + l16, (f4){Oo[0], Oo[1], Oo[2], Oo[3]}); sts16f(oc + 1024 + l16, (f4){Oo[4], Oo[5], Oo[6], Oo[7]}); sts4f(oc + 2048 + l * 4, Oo[8]);
#pragma unroll
        for (int e = 8; e < 16; ++e) p[e] = __builtin_amdgcn_exp2f(sel(diag_mask(e), Sx[e], Sy[e]));
        unsigned P[8];
#pragma unroll
        for (int e = 0; e < 8; ++e) P[e] = pk(p[2 * e], p[2 * e + 1]);
        const u4 Y0 = {P[0] & pm[0], P[1] & pm[1], P[2] & pm[2], P[3] & pm[3]}, Y1 = {P[4] & pm[4], P[5] & pm[5], P[6] & pm[6], P[7] & pm[7]};
        const u4 X0_ = {P[0] ^ Y0[0], P[1] ^ Y0[1], P[2] ^ Y0[2], P[3] ^ Y0[3]}, X1_ = {P[4] ^ Y1[0], P[5] ^ Y1[1], P[6] ^ Y1[2], P[7] ^ Y1[3]};
        Om = MFMA32(V0[0], X0_, Om); Om = MFMA32(V1[0], X1_, Om); Om = MFMA32(V0[1], Y0, Om); Om = MFMA32(V1[1], Y1, Om);
    }
    SBAR();
    asm volatile("s_waitcnt vmcnt(3) lgkmcnt(0)\n\ts_barrier" : "+v"(ck) :: "memory");
    const unsigned op = L_OC + (w ^ 1) * 2304;
    f4 oa = lds16f(op + l16) + (f4){Om[0], Om[1], Om[2], Om[3]}, ob = lds16f(op + 1024 + l16) + (f4){Om[4], Om[5], Om[6], Om[7]};
    float den = Om[8] + lds4f(op + 2048 + l * 4);
    const f2 os = (lo2(oa) + hi2(oa)) + (lo2(ob) + hi2(ob));
    if (UNLIKELY(any_lane(!(den >= 0x1p-100f) | !(den <= 0x1p100f) | !(__builtin_fabsf(os[0] + os[1]) < __builtin_inff())))) {
        f16 O = {}; float m = -__builtin_inff();
#pragma unroll 1
        for (int kb = 8; kb >= 0; --kb) {
            const int T = 2 * (int)T64 + (int)j - 8 + kb;
            if (T < 0) break;
            u4 Ks, Va = ones, Vb = ones;
            ld16(Ks, vk, rsF, kvb + (unsigned)T * 2048); ld16_v(Va, Vb, vv_, rsF, kvb + (unsigned)T * 2048);
            WAITV3(0, Ks, Va, Vb);
            f16 S = MFMA32(Ks, Qm, z16);
            const int up = kb == 8 ? (int)(t32 - 8 * h) : 99, dn = kb == 0 ? (int)(t32 - 8 * h) : -99;
            float mx = m;
#pragma unroll
            for (int e = 0; e < 16; ++e) { const int c = 16 * ((e >> 2) & 1) + 4 * (e >> 3) + (e & 3); S[e] = (c <= up && c > dn) ? S[e] : -__builtin_inff(); mx = __builtin_fmaxf(mx, S[e]); }
            mx = max_x32(mx);
            const float al = __builtin_amdgcn_exp2f(m - mx); m = mx;
#pragma unroll
            for (int e = 0; e < 16; ++e) { O[e] *= al; S[e] -= mx; }
            pv_tile(S, Va, Vb, O);
        }
        oa = (f4){O[0], O[1], O[2], O[3]}; ob = (f4){O[4], O[5], O[6], O[7]}; den = O[8];
    }
    const float rd = __builtin_amdgcn_rcpf(den);
    st16m((u4){pk2(lo2(oa) * rd), pk2(hi2(oa) * rd), pk2(lo2(ob) * rd), pk2(hi2(ob) * rd)}, l16, rsF, sOw);
    FLAG(w == 4 ? WS_F2 + (U64 * 8 + fb) * 128 : w == 5 ? WS_F3 + (g * 32 + r) * 128 : OOB);
    const unsigned ta0 = (4 * h + q4) * 128 + 64 * (q4 >> 1) + 32 * (g4 & 1) + 8 * pp, ta1 = ta0 ^ 64;
    f16 acc0 = {}, acc1 = {};
    {
        const unsigned sO = WS_O + (2 * U64 + j) * OS + hh * 8192;
        const unsigned oa0 = L_A + hh * 16384 + ta0, oa1 = L_A + hh * 16384 + ta1;
        u4 A0[8], A1[8], B[8];
#pragma unroll
        for (int i = 0; i < 8; ++i) { A0[i] = ldtr(oa0 + i * 2048, oa0 + i * 2048 + 1024); A1[i] = ldtr(oa1 + i * 2048, oa1 + i * 2048 + 1024); }
        if (w == 7 && UNLIKELY(any_lane(ck[0] != tag[0] || ck[1] != tag[1]))) POLL(vc1);
        BAR_VM(1);
#pragma unroll
        for (int i = 0; i < 8; ++i) ld16(B[i], l16, rsF, sO + i * 1024);
#define P3_PAIR(i, NW, M0, M1) do { asm volatile("s_waitcnt vmcnt(%2)" : "+v"(B[i]), "+v"(B[(i) + 1]) : "i"(NW) : "memory"); \
            if (UNLIKELY(any_lane(max(B[i][3], B[(i) + 1][3]) == ~0u))) { \
                _Pragma("unroll 1") for (int s_ = 0; s_ < SPIN_CAP; ++s_) { __builtin_amdgcn_s_sleep(10); asm volatile("s_waitcnt vmcnt(0)" ::: "memory"); unsigned m_ = 0; \
                    _Pragma("unroll") for (int q = (i); q < 8; ++q) ld16(B[q], l16, rsF, sO + q * 1024); \
                    _Pragma("unroll") for (int q = (i); q < 8; ++q) { WAITV1(7 - q, B[q]); m_ = max(m_, B[q][3]); } \
                    if (!any_lane(m_ == ~0u)) break; } } \
            acc0 = MFMA32(A0[i], B[i], acc0); acc1 = MFMA32(A1[i], B[i], acc1); M0; SBAR(); acc0 = MFMA32(A0[(i) + 1], B[(i) + 1], acc0); acc1 = MFMA32(A1[(i) + 1], B[(i) + 1], acc1); M1; SBAR(); } while (0)
        P3_PAIR(0, 6, NO, NO); P3_PAIR(2, 4, NO, NO); P3_PAIR(4, 2, GU(0, 0), GU(0, 1)); P3_PAIR(6, 2, GU(0, 2), NO);
    }
    const unsigned vc2 = WS_F2 + (g * 32 + t32) * 128;
    ld8(ck, w == 7 ? vc2 : OOB, rsF, 0);
    const unsigned rw0 = L_A + (hh * 2 + j) * 8192 + h * 512 + t32 * 16, rw1 = rw0 ^ 128, rr = L_A + j * 8192 + u8 * 1024 + (((8 * hh + l7) ^ ((u8 & 1) << 3)) * 16);
    auto reduce = [&](f4& lo, f4& hi) {
#pragma unroll
        for (int u = 0; u < 4; ++u) {
            sts16f(((u & 1) ? rw1 : rw0) + u * 1024, (f4){acc0[4 * u], acc0[4 * u + 1], acc0[4 * u + 2], acc0[4 * u + 3]});
            sts16f(((u & 1) ? rw1 : rw0) + (4 + u) * 1024, (f4){acc1[4 * u], acc1[4 * u + 1], acc1[4 * u + 2], acc1[4 * u + 3]});
        }
        BAR_L();
        lo = (lds16f(rr) + lds16f(rr + 16384)) + (lds16f(rr + 32768) + lds16f(rr + 49152));
        hi = (lds16f(rr + 512) + lds16f(rr + 16896)) + (lds16f(rr + 33280) + lds16f(rr + 49664));
    };
    u4 Hres;
    const unsigned ga = (8 * h + q4) * 192 + (g4 & 1) * 96 + 8 * pp;
    f16 cs[3] = {};
    {
        f4 lo, hi; reduce(lo, hi);
        Hres = (u4){pk2(lo2(lo) + (f2){blo(xres[0]), bhi(xres[0])}), pk2(hi2(lo) + (f2){blo(xres[1]), bhi(xres[1])}), pk2(lo2(hi) + (f2){blo(xres[2]), bhi(xres[2])}), pk2(hi2(hi) + (f2){blo(xres[3]), bhi(xres[3])})};
        st16m(Hres, vh, rsF, sHw);
        f2 s1 = {0.f, 0.f}, s2 = {0.f, 0.f};
#pragma unroll
        for (int e = 0; e < 4; ++e) { const f2 a = {blo(Hres[e]), bhi(Hres[e])}; s1 += a; s2 += a * a; }
        const float t1 = add_x32(add_x16(add_x8(s1[0] + s1[1]))), t2 = add_x32(add_x16(add_x8(s2[0] + s2[1])));
        { const unsigned q2 = min(uf(t2), 0xfffffffeu); st16((u4){uf(t1), q2, uf(t1), q2}, vs, rsF, sSw); }
        GU(3, 0); GU(3, 1); GU(3, 2);
        asm volatile("s_waitcnt vmcnt(5) lgkmcnt(0)\n\ts_barrier" : "+v"(ck) :: "memory");
#define COLSUM(k0, N) do { const unsigned o = ga + (k0) * 3072; u4 a[3 * (N)]; \
        _Pragma("unroll") for (int i = 0; i < 3 * (N); ++i) a[i] = ldtr(o + (i / 3) * 3072 + (i % 3) * 32, o + (i / 3) * 3072 + (i % 3) * 32 + 768); SBAR(); \
        _Pragma("unroll") for (int i = 0; i < 3 * (N); ++i) cs[i % 3] = MFMA32(ones, a[i], cs[i % 3]); PIN16(cs[0]); PIN16(cs[1]); PIN16(cs[2]); SBAR(); } while (0)
        COLSUM(3 * w, 3);
    }
    const u4 rsWd = mk_rsrc(wd + fb * 128u, 1376u * 1024u - fb * 128u);
    if (w == 7 && UNLIKELY(any_lane(ck[0] != tag[0] || ck[1] != tag[1]))) POLL(vc2);
    if (w == 5 && b == 255) {
#pragma unroll 1
        for (int s_ = 0; s_ < 4 * SPIN_CAP; ++s_) { u2 f0, f1, f2_, f3; const unsigned vc = WS_F1 + l * 128;
            ld8(f0, vc, rsF, 0); ld8(f1, vc, rsF, 8192); ld8(f2_, vc, rsF, 16384); ld8(f3, vc, rsF, 24576); WAITV4(0, f0, f1, f2_, f3);
            if (!any_lane(((f0[0] ^ tag[0]) | (f1[0] ^ tag[0]) | (f2_[0] ^ tag[0]) | (f3[0] ^ tag[0]) | (f0[1] ^ tag[1]) | (f1[1] ^ tag[1]) | (f2_[1] ^ tag[1]) | (f3[1] ^ tag[1])) != 0u)) break; }
        FLAG(0u);
    }
    BAR_VM(0);
    const unsigned a0 = 15 * hh, a1 = a0 + 1, jo = j * 8192;
    const unsigned wde = (48 * (a0 >> 1) + 16 * (a0 & 1)) * 1024 + jo, wdo = (48 * (a1 >> 1) + 16 * (a1 & 1)) * 1024 + jo, wdt = (336 * hh + 32) * 1024 + jo;
    const unsigned wla = L_A + hh * 14336 + j * 1024, wlb = hh * 12288 + j * 1024, wlc = 49152 + hh * 4096 + j * 1024;
#define WD(i) dma_bufi<((i) < 14 ? (i) % 7 : (i) < 20 ? (i) - 14 : (i) - 20) * 2048u, ((i) < 15 ? (i) >> 1 : (i) - 15) * 49152u>((i) < 14 ? wla : (i) < 20 ? wlb : wlc, vrow, rsWd, (i) >= 15 ? wdt : ((i) & 1) ? wdo : wde)
    u4 D0[7], D1[7];
    {
        const unsigned sH = r > 28 ? OOB : WS_H + (8 * g + w) * OS, sS = r > 28 ? OOB : WS_ST + (8 * g + w) * 4096u;
        constexpr int PD = 6;
        u4 Hb[32], A0[3], A1[3], A2[4];
        u2 st[4];
        f16 ag[3] = {};
        const unsigned cf = L_CF + w * 384, vstat = h * 2048 + t32 * 16;
#define GA(k) (ga + (k) * 3072)
#define HO(k) (sH + (k) * 1024)
#pragma unroll
        for (int i = 0; i < PD; ++i) ld16(Hb[kgo(i)], l16, rsF, HO(kgo(i)));
        COLSUM(24 + w, 1); sts16f(L_CSP + w * 512 + t32 * 16, (f4){cs[0][0], cs[1][0], cs[2][0], 0.f}); BAR_L();
        {
            const unsigned o = L_CSP + h * 2048 + t32 * 16; const f4 s = (lds16f(o) + lds16f(o + 512)) + (lds16f(o + 1024) + lds16f(o + 1536));
            const float c0 = add_x32(s[0]), c1 = add_x32(s[1]), c2 = add_x32(s[2]);
            sts4f(cf + l * 4, h ? c1 : c0); sts4f(cf + 256 + t32 * 4, c2); }
        A0[0] = ldtr(GA(0), GA(0) + 768); A1[0] = ldtr(GA(0) + 32, GA(0) + 800); A0[1] = ldtr(GA(kgo(1)), GA(kgo(1)) + 768); A1[1] = ldtr(GA(kgo(1)) + 32, GA(kgo(1)) + 800);
        unsigned mx = 0;
#define P4_STEP(i) do { constexpr int c = (i) % 3, n = ((i) + 2) % 3, k = kgo(i); const unsigned o = GA((i) < 30 ? kgo((i) + 2) : 0); \
            if ((i) + PD < 32) ld16(Hb[kgo((i) + PD < 32 ? (i) + PD : 0)], l16, rsF, HO(kgo((i) + PD))); \
            else if ((i) + PD < 36) ld8(st[((i) + PD - 32) & 3], vstat, rsF, sS + ((i) + PD - 32) * 512); \
            if ((i) == 0) YOUNG_FIRST(); if ((i) == 24) EVEN(); \
            WAITV1((i) + PD < 36 ? PD : 35 - (i), Hb[k]); if ((i) == 10) HOTB(3, wd, 43); \
            if ((i) < 8) FRESH16(Hb[k], l16, rsF, HO(k)); else { mx = max(mx, Hb[k][3]); asm volatile("" : "+v"(mx)); } SBAR(); \
            ag[0] = MFMA32(A0[c], Hb[k], ag[0]); if ((i) < 30) A0[n] = ldtr(o, o + 768); else A2[((i) - 30) & 1] = ldtr(GA(((i) - 30) & 1) + 64, GA(((i) - 30) & 1) + 832); SBAR(); \
            ag[1] = MFMA32(A1[c], Hb[k], ag[1]); if ((i) < 30) A1[n] = ldtr(o + 32, o + 800); else if ((i) == 31) A2[2] = ldtr(GA(2) + 64, GA(2) + 832); SBAR(); } while (0)
#define P4_4(i) P4_STEP(i); P4_STEP((i) + 1); P4_STEP((i) + 2); P4_STEP((i) + 3)
        P4_4(0); P4_4(4); P4_4(8); P4_4(12); P4_4(16); P4_4(20); P4_4(24); P4_4(28);
        WAITV4(0, st[0], st[1], st[2], st[3]);
        if (UNLIKELY(any_lane(max(max(mx, max(st[0][1], st[1][1])), max(st[2][1], st[3][1])) == ~0u))) {
#pragma unroll
            for (int i = 0; i < 32; ++i) FRESH16(Hb[i], l16, rsF, HO(i));
#pragma unroll 1
            for (int s_ = 0; s_ < SPIN_CAP; ++s_) {
#pragma unroll
                for (int i = 0; i < 4; ++i) ld8(st[i], vstat, rsF, sS + i * 512);
                WAITV4(0, st[0], st[1], st[2], st[3]);
                if (!any_lane(max(max(st[0][1], st[1][1]), max(st[2][1], st[3][1])) == ~0u)) break;
            }
            ag[0] = z16; ag[1] = z16;
#pragma unroll
            for (int i = 0; i < 32; ++i) { const unsigned o = GA(kgo(i)); ag[0] = MFMA32(ldtr(o, o + 768), Hb[kgo(i)], ag[0]); ag[1] = MFMA32(ldtr(o + 32, o + 800), Hb[kgo(i)], ag[1]); }
        }
        const float t1 = add_x32(fu(st[0].x) + fu(st[1].x) + fu(st[2].x) + fu(st[3].x)), t2 = add_x32(fu(st[0].y) + fu(st[1].y) + fu(st[2].y) + fu(st[3].y));
        const float nm = t1 * (-1.f / 512), ve = __builtin_fmaxf(t2 * (1.f / 512) - nm * nm, 0.f) + 1e-5f, rs = __builtin_amdgcn_rsqf(ve);
        const float ka_ = rs * -2.3022081986f, kb_ = rs * rs * rs * -0.1029432397f;
#define G_UNIT(u, so_, M0, M1) do { u4 y; _Pragma("unroll") for (int qd = 0; qd < 2; ++qd) { \
                const f4 cg = lds16f(cf + (u) * 128 + qd * 32 + h * 16), cu = lds16f(cf + (u) * 128 + (2 + qd) * 32 + h * 16); \
                _Pragma("unroll") for (int e = 0; e < 2; ++e) { \
                    const f2 gt = (f2){ag[u][4 * qd + 2 * e], ag[u][4 * qd + 2 * e + 1]} + (e ? hi2(cg) : lo2(cg)) * nm, up = (f2){ag[u][8 + 4 * qd + 2 * e], ag[u][9 + 4 * qd + 2 * e]} + (e ? hi2(cu) : lo2(cu)) * nm; \
                    const f2 z = gt * (gt * gt * kb_ + ka_), d = (f2){__builtin_amdgcn_exp2f(z[0]), __builtin_amdgcn_exp2f(z[1])} * ve + ve; \
                    unsigned y_ = pk2(gt * up * (f2){__builtin_amdgcn_rcpf(d[0]), __builtin_amdgcn_rcpf(d[1])}); asm volatile("" : "+v"(y_)); y[2 * qd + e] = y_; } \
                if (qd == 0) MEM(M0); } \
            MEM(st16m(y, l16, rsF, so_); M1); } while (0)
        MEM(WD(0), WD(1), WD(2));
        G_UNIT(0, sGa, (WD(3), WD(4), WD(5), WD(6)), NO); G_UNIT(1, sGa + 1024, NO, NO);
        const unsigned da0 = L_A + hh * 14336 + ta0, da1 = L_A + hh * 14336 + ta1;
        const unsigned vc3 = WS_F3 + ((U64 >> 2) * 32 + min(t32, 28u)) * 128, vc4 = vc3 + (WS_F4 - WS_F3);
#define P4B_STEP(i) do { constexpr int c = (i) & 3, n = ((i) + 3) & 3; const unsigned o = GA(((i) + 3) & 31) + 64; \
            if ((i) == 6) BAR_VM(2);         \
            if ((i) == 14) { BAR_VM(0); FLAG(w == 4 ? WS_F4 + (g * 32 + r) * 128 : OOB); }        SBAR(); \
            ag[2] = MFMA32(A2[c], Hb[i], ag[2]); if ((i) < 29) A2[n] = ldtr(o, o + 768); \
            if ((i) >= 6 && (i) < 13) { constexpr int q = ((i) + 8) % 7; D0[q] = ldtr(da0 + q * 2048, da0 + q * 2048 + 1024); D1[q] = ldtr(da1 + q * 2048, da1 + q * 2048 + 1024); } SBAR(); \
            if ((i) >= 14 && (i) < 27) WD((i) >= 14 && (i) < 27 ? (i) - 7 : 0); \
            if ((i) == 27) ld8(ck, w == 7 ? vc3 : w == 6 ? vc4 : OOB, rsF, 0); SBAR(); } while (0)
#define P4B_4(i) P4B_STEP(i); P4B_STEP((i) + 1); P4B_STEP((i) + 2); P4B_STEP((i) + 3)
        P4B_4(0); P4B_4(4); P4B_4(8); P4B_4(12); P4B_4(16); P4B_4(20); P4B_4(24); P4B_4(28);
        G_UNIT(2, sGb, NO, NO);
        WAITV1(1, ck);
        if (w >= 6 && UNLIKELY(any_lane(ck[0] != tag[0] || ck[1] != tag[1]))) { const unsigned vc = w == 7 ? vc3 : vc4; POLL(vc); }
    }
    BAR_L();
    {
        constexpr int PD = 5;
        u4 Gb[22], A0[2], A1[2];
        const unsigned gs = WS_G + (2 * U64 + j) * 90112u + 15 * hh * 1024, gz = hh == 3 ? OOB : gs, gt = WS_G + (2 * U64 + j) * 90112u + (60 + 7 * hh - 15) * 1024;
        const unsigned o0 = L_A + hh * 14336 + ta0, o1 = L_A + hh * 14336 + ta1, p0 = hh * 12288 + ta0, p1 = hh * 12288 + ta1, q0 = 49152 + hh * 4096 + ta0, q1 = 49152 + hh * 4096 + ta1;
#define GOFF(i) (((i) >= 15 ? gt : (i) >= 13 ? gz : gs) + (i) * 1024)
#pragma unroll
        for (int i = 0; i < PD; ++i) ld16(Gb[i], l16, rsF, GOFF(i));
        WD(20); WD(21);
        acc0 = z16; acc1 = z16;
#define P5_STEP(i) do { constexpr int c = (i) & 1, n = c ^ 1; if ((i) == 0) YOUNG_FIRST(); if ((i) == 14) EVEN(); \
            if ((i) + PD < 22) ld16(Gb[(i) + PD < 22 ? (i) + PD : 0], l16, rsF, GOFF((i) + PD)); \
            WAITV1(((i) + PD < 22 ? PD : 21 - (i)) + ((i) < PD ? 2 : 0), Gb[i]); if ((i) >= 15) FRESH16(Gb[i], l16, rsF, GOFF(i)); \
            if ((i) == 14) BAR_L(); SBAR();        \
            const unsigned a0_ = (i) < 13 ? o0 + ((i) - 6) * 2048 : (i) < 19 ? p0 + ((i) - 13) * 2048 : q0 + ((i) - 19) * 2048, a1_ = (i) < 13 ? o1 + ((i) - 6) * 2048 : (i) < 19 ? p1 + ((i) - 13) * 2048 : q1 + ((i) - 19) * 2048; \
            acc0 = MFMA32((i) < 7 ? D0[(i) < 7 ? (i) : 0] : A0[c], Gb[i], acc0); if ((i) >= 6 && (i) < 21) A0[n] = ldtr(a0_, a0_ + 1024); SBAR(); \
            acc1 = MFMA32((i) < 7 ? D1[(i) < 7 ? (i) : 0] : A1[c], Gb[i], acc1); if ((i) >= 6 && (i) < 21) A1[n] = ldtr(a1_, a1_ + 1024); SBAR(); } while (0)
        P5_STEP(0); P5_STEP(1); P5_STEP(2); P5_STEP(3); P5_STEP(4); P5_STEP(5); P5_STEP(6); P5_STEP(7); P5_STEP(8); P5_STEP(9); P5_STEP(10);
        P5_STEP(11); P5_STEP(12); P5_STEP(13); P5_STEP(14); P5_STEP(15); P5_STEP(16); P5_STEP(17); P5_STEP(18); P5_STEP(19); P5_STEP(20); P5_STEP(21);
    }
    {
        f4 lo, hi; reduce(lo, hi);
        const u4 Ores = {pk2(lo2(lo) + (f2){blo(Hres[0]), bhi(Hres[0])}), pk2(hi2(lo) + (f2){blo(Hres[1]), bhi(Hres[1])}), pk2(lo2(hi) + (f2){blo(Hres[2]), bhi(Hres[2])}), pk2(hi2(hi) + (f2){blo(Hres[3]), bhi(Hres[3])})};
        st16_plain(Ores, vres, mk_rsrc(out + U64 * 65536u, 65536u), 0);
    }
    asm volatile(".Lfb_code1:");
#endif
}

extern "C" void kernel_launch(void* const* d_in, const int* in_sizes, int n_in, void* d_out, int out_size, void* d_ws, size_t ws_size, hipStream_t stream) {
    (void)in_sizes; (void)n_in; (void)out_size; (void)ws_size;
    (void)hipFuncSetAttribute((const void*)fused_block, hipFuncAttributeMaxDynamicSharedMemorySize, LDS_BYTES);
    fused_block<<<256, 512, LDS_BYTES, stream>>>((const char*)d_in[0], (const char*)d_in[1], (const char*)d_in[2], (const char*)d_in[4], (const char*)d_in[5], (const char*)d_in[6],
                                                 (char*)d_out, (char*)d_ws);
}
```
